```python
import torch

def _softmax(x, axis=-1):
    return torch.softmax(x, dim=axis)
def _gelu(x):
    return torch.nn.functional.gelu(x, approximate="tanh")
def _silu(x):
    return torch.nn.functional.silu(x)
def _sigmoid(x):
    return torch.sigmoid(x)
def _layernorm(x, axis=-1, eps=1e-5):
    m = x.mean(dim=axis, keepdim=True)
    v = x.var(dim=axis, keepdim=True, unbiased=False)
    return (x - m) / torch.sqrt(v + eps)
def _scatter_add(tgt, idx, src, axis=0):
    return tgt.clone().index_add_(axis, idx, src)
def _segment_sum(src, seg, n):
    out = torch.zeros((n,) + src.shape[1:], dtype=src.dtype)
    return out.index_add_(0, seg, src)
def _pad(x, pw):
    flat = tuple(v for pair in reversed(pw) for v in pair)
    return torch.nn.functional.pad(x, flat)

kernel_name = 'interleave_layers__ssm_conv_h3__ssm_conv_s4__prompt'

def setup_inputs():

    _w = (lambda a: (lambda t: t.to(torch.bfloat16) if t.is_floating_point() else t)(torch.as_tensor(a)))
    def _A_setup():
        B, S, D = 1, 2048, 512
        ks = 4
        return {
            "q": _w((lambda *s: torch.randn(*s, dtype=torch.bfloat16) * 0.05)(B, S, D)),
            "k": _w((lambda *s: torch.randn(*s, dtype=torch.bfloat16) * 0.4)(B, S, D)),
            "v": _w((lambda *s: torch.randn(*s, dtype=torch.bfloat16) * 0.4)(B, S, D)),
            "shift_k": _w((lambda *s: torch.randn(*s, dtype=torch.bfloat16) * 0.4)(D, ks) * 0.5),
            "ssm_k": _w((lambda *s: torch.randn(*s, dtype=torch.bfloat16) * 0.4)(D, S) * (1.0 / S ** 0.5)),
            "ssm_D": _w((lambda *s: torch.randn(*s, dtype=torch.bfloat16) * 0.4)(D)),
        }
    _A_in = _A_setup()

    def _B_setup():
        B, L, D, N = 1, 2048, 512, 128
        A_re = -(lambda _x: torch.exp(torch.as_tensor(_x)))((lambda *s: torch.randn(*s, dtype=torch.bfloat16) * 6.4)(D, N))
        A_im = torch.pi * (lambda *s: torch.randn(*s, dtype=torch.bfloat16) * 0.05)(D, N)
        B_re = (lambda *s: torch.randn(*s, dtype=torch.bfloat16) * 0.1)(D, N)
        B_im = (lambda *s: torch.randn(*s, dtype=torch.bfloat16) * 0.1)(D, N)
        C_re = (lambda *s: torch.randn(*s, dtype=torch.bfloat16) * 0.1)(D, N)
        C_im = (lambda *s: torch.randn(*s, dtype=torch.bfloat16) * 0.1)(D, N)
        log_dt = (lambda *s: torch.randn(*s, dtype=torch.bfloat16) * 0.05)(D) * 0.5 - 4.0
        Dskip = (lambda *s: torch.randn(*s, dtype=torch.bfloat16) * 0.05)(D)
        return {
            "u": _w((lambda *s: torch.randn(*s, dtype=torch.bfloat16) * 0.05)(B, L, D)),
            "A_re": _w(A_re),
            "A_im": _w(A_im),
            "B_re": _w(B_re),
            "B_im": _w(B_im),
            "C_re": _w(C_re),
            "C_im": _w(C_im),
            "log_dt": _w(log_dt),
            "Dskip": _w(Dskip),
        }
    _B_in = _B_setup()

    return {
        "A_q": _A_in["q"],
        "A_k": _A_in["k"],
        "A_v": _A_in["v"],
        "A_shift_k": _A_in["shift_k"],
        "A_ssm_k": _A_in["ssm_k"],
        "A_ssm_D": _A_in["ssm_D"],
        "B_u": _B_in["u"],
        "B_A_re": _B_in["A_re"],
        "B_A_im": _B_in["A_im"],
        "B_B_re": _B_in["B_re"],
        "B_B_im": _B_in["B_im"],
        "B_C_re": _B_in["C_re"],
        "B_C_im": _B_in["C_im"],
        "B_log_dt": _B_in["log_dt"],
        "B_Dskip": _B_in["Dskip"],
    }

def reference(A_q, A_k, A_v, A_shift_k, A_ssm_k, A_ssm_D, B_u, B_A_re, B_A_im, B_B_re, B_B_im, B_C_re, B_C_im, B_log_dt, B_Dskip):
    def _A_ref(q, k, v, shift_k, ssm_k, ssm_D):
        B, S, D = q.shape
        ks = shift_k.shape[-1]
        k_pad = _pad(k, ((0, 0), (ks - 1, 0), (0, 0)))
        k_shift = torch.zeros_like(k)
        for t in range(ks):
            k_shift = k_shift + k_pad[:, t:t + S, :] * shift_k[:, ks - 1 - t][None, None, :]
        kv = k_shift * v
        L = 2 * S
        kv_pad = _pad(kv, ((0, 0), (0, S), (0, 0)))
        kf_pad = _pad(ssm_k, ((0, 0), (0, S)))
        kv_f = (lambda x, axes=(-2,-1): torch.fft.rfft2(x.float(), dim=axes))(kv_pad[:, None, :, :], axes=(1, 2))
        kf_f = (lambda x, axes=(-2,-1): torch.fft.rfft2(x.float(), dim=axes))(kf_pad[:, None, :], axes=(1, 2))
        kf_f = (lambda x, *d: x.permute(*d))(kf_f, 1, 2, 0)
        y_f = kv_f * kf_f[None, :, :, :]
        y = (lambda x, s, axes=(-2,-1): torch.fft.irfft2(x, s=s, dim=axes).to(torch.bfloat16))(y_f, (1, L), axes=(1, 2))[:, 0, :S, :]
        y = y + kv * ssm_D[None, None, :]
        return q * y

    def _B_ref(u, A_re, A_im, B_re, B_im, C_re, C_im, log_dt, Dskip):
        Bsz, L, D = u.shape
        dt = torch.exp(log_dt)[:, None]
        A = A_re + 1j * A_im
        Bc = B_re + 1j * B_im
        Cc = C_re + 1j * C_im
        logdA = dt * A
        dB = dt * Bc
        l = (lambda x, dt: x.to(dt))(torch.arange(L), A_re.dtype)
        P = torch.exp(logdA[:, :, None] * l[None, None, :])
        K = torch.einsum("dn,dnl,dn->dl", Cc, P, dB).real
        L2 = 2 * L
        Kp = _pad(K, ((0, 0), (0, L)))
        up = (lambda x, *d: x.permute(*d))(u, 0, 2, 1)
        up = _pad(up, ((0, 0), (0, 0), (0, L)))
        Kf = (lambda x, axes=(-2,-1): torch.fft.rfft2(x.float(), dim=axes))(Kp[:, None, :], axes=(-2, -1))
        Uf = (lambda x, axes=(-2,-1): torch.fft.rfft2(x.float(), dim=axes))(up[:, :, None, :], axes=(-2, -1))
        Yf = Uf * Kf[None, :, :, :]
        y = (lambda x, s, axes=(-2,-1): torch.fft.irfft2(x, s=s, dim=axes).to(torch.bfloat16))(Yf, (1, L2), axes=(-2, -1))[:, :, 0, :L]
        y = (lambda x, *d: x.permute(*d))(y, 0, 2, 1) + u * Dskip[None, None, :]
        return y

    x = A_q
    x = x + _A_ref(_layernorm(x, axis=-1), A_k, A_v, A_shift_k, A_ssm_k, A_ssm_D)
    x = x + _B_ref(_layernorm(x, axis=-1), B_A_re, B_A_im, B_B_re, B_B_im, B_C_re, B_C_im, B_log_dt, B_Dskip)
    x = x + _A_ref(_layernorm(x, axis=-1), A_k, A_v, A_shift_k, A_ssm_k, A_ssm_D)
    x = x + _B_ref(_layernorm(x, axis=-1), B_A_re, B_A_im, B_B_re, B_B_im, B_C_re, B_C_im, B_log_dt, B_Dskip)
    return x
```

```cpp
#include <hip/hip_runtime.h>
#include <hip/hip_bf16.h>
#include <stdint.h>

typedef unsigned int u32;
typedef unsigned short u16;
typedef __attribute__((ext_vector_type(4))) float f32x4;
typedef __attribute__((ext_vector_type(2))) float f32x2;
typedef __attribute__((ext_vector_type(4))) u32 u32x4;
typedef __attribute__((ext_vector_type(2))) u32 u32x2;
typedef __attribute__((ext_vector_type(8))) __bf16 bf16x8;
typedef __attribute__((ext_vector_type(2))) __bf16 bf16x2;

constexpr int SL = 2048;
constexpr int DM = 512;
constexpr int NS = 128;

__device__ __forceinline__ float bf_lo(u32 p) { return __builtin_bit_cast(float, p << 16); }
__device__ __forceinline__ float bf_hi(u32 p) { return __builtin_bit_cast(float, p & 0xffff0000u); }
__device__ __forceinline__ float bf_u16(u16 p) { return __builtin_bit_cast(float, (u32)p << 16); }
__device__ __forceinline__ u32 pk_bf(float lo, float hi) {
    bf16x2 v = {(__bf16)lo, (__bf16)hi};
    return __builtin_bit_cast(u32, v);
}
__device__ __forceinline__ float rbf(float x) { return bf_lo(pk_bf(x, x)); }
__device__ __forceinline__ f32x4 mfma16(u32x4 a, u32x4 b, f32x4 c) {
    return __builtin_amdgcn_mfma_f32_16x16x32_bf16(__builtin_bit_cast(bf16x8, a), __builtin_bit_cast(bf16x8, b), c, 0, 0, 0);
}
template <int CTRL, int RMASK = 0xf, int BMASK = 0xf>
__device__ __forceinline__ float dpp_add(float acc, float v) {
    int t = __builtin_amdgcn_update_dpp(0, __builtin_bit_cast(int, v), CTRL, RMASK, BMASK, true);
    return acc + __builtin_bit_cast(float, t);
}
__device__ __forceinline__ float wave_sum63(float x) {
    x = dpp_add<0xB1>(x, x);
    x = dpp_add<0x4E>(x, x);
    x = dpp_add<0x141>(x, x);
    x = dpp_add<0x140>(x, x);
    x = dpp_add<0x142, 0xa>(x, x);
    x = dpp_add<0x143, 0xc>(x, x);
    return x;
}

__device__ __forceinline__ void row_stats8(const float (&a)[8][2], float (&m)[8], float (&rs)[8], float* red  ) {
    const int lane = threadIdx.x & 63, wave = threadIdx.x >> 6;
    float mt[8];
#pragma unroll
    for (int r = 0; r < 8; ++r) {
        float s = wave_sum63(a[r][0] + a[r][1]);
        if (lane == 63) red[r * 4 + wave] = s;
    }
    __syncthreads();
#pragma unroll
    for (int r = 0; r < 8; ++r) {
        f32x4 p = *(const f32x4*)(red + r * 4);
        mt[r] = ((p[0] + p[1]) + (p[2] + p[3])) * (1.0f / DM);
    }
#pragma unroll
    for (int r = 0; r < 8; ++r) {
        float d0 = a[r][0] - mt[r], d1 = a[r][1] - mt[r];
        float s = wave_sum63(d0 * d0 + d1 * d1);
        if (lane == 63) red[32 + r * 4 + wave] = s;
    }
    __syncthreads();
#pragma unroll
    for (int r = 0; r < 8; ++r) {
        f32x4 p = *(const f32x4*)(red + 32 + r * 4);
        float var = ((p[0] + p[1]) + (p[2] + p[3])) * (1.0f / DM);
        float s = rbf(__builtin_sqrtf(rbf(rbf(var) + 1e-5f)));
        m[r] = rbf(mt[r]);
        rs[r] = 1.0f / s;
    }
}

__device__ __forceinline__ int chan_pair(int b) { return (b & 7) * 32 + (b >> 3); }

__device__ __forceinline__ void prep_rows(int w, const u32* __restrict__ k, const u32* __restrict__ v,
                                          const u32x4* __restrict__ shiftk, u32x4* __restrict__ kvT) {
    const int tid = threadIdx.x;
    const int t0 = 8 * w;
    u32 kk[11], vv[8];
#pragma unroll
    for (int i = 0; i < 11; ++i) {
        int t = t0 - 3 + i;
        kk[i] = (t >= 0) ? k[t * 256 + tid] : 0u;
    }
#pragma unroll
    for (int i = 0; i < 8; ++i) vv[i] = v[(t0 + i) * 256 + tid];
    u32x4 sk = shiftk[tid];
    float w0[4] = {bf_lo(sk[0]), bf_hi(sk[0]), bf_lo(sk[1]), bf_hi(sk[1])};
    float w1[4] = {bf_lo(sk[2]), bf_hi(sk[2]), bf_lo(sk[3]), bf_hi(sk[3])};
    float o0[8], o1[8];
#pragma unroll
    for (int i = 0; i < 8; ++i) {
        float s0 = bf_lo(kk[i]) * w0[3];
        s0 = fmaf(bf_lo(kk[i + 1]), w0[2], s0);
        s0 = fmaf(bf_lo(kk[i + 2]), w0[1], s0);
        s0 = fmaf(bf_lo(kk[i + 3]), w0[0], s0);
        float s1 = bf_hi(kk[i]) * w1[3];
        s1 = fmaf(bf_hi(kk[i + 1]), w1[2], s1);
        s1 = fmaf(bf_hi(kk[i + 2]), w1[1], s1);
        s1 = fmaf(bf_hi(kk[i + 3]), w1[0], s1);
        o0[i] = s0 * bf_lo(vv[i]);
        o1[i] = s1 * bf_hi(vv[i]);
    }
    u32x4 a = {pk_bf(o0[0], o0[1]), pk_bf(o0[2], o0[3]), pk_bf(o0[4], o0[5]), pk_bf(o0[6], o0[7])};
    u32x4 b = {pk_bf(o1[0], o1[1]), pk_bf(o1[2], o1[3]), pk_bf(o1[4], o1[5]), pk_bf(o1[6], o1[7])};
    kvT[(w * 512 + 2 * tid) + 0] = a;
    kvT[(w * 512 + 2 * tid) + 1] = b;
}

struct cplx { float r, i; };
__device__ __forceinline__ cplx csq(cplx a) { return {fmaf(a.r, a.r, -a.i * a.i), 2.0f * a.r * a.i}; }
__device__ __forceinline__ cplx cmul(cplx a, cplx b) { return {fmaf(a.r, b.r, -a.i * b.i), fmaf(a.r, b.i, a.i * b.r)}; }

__device__ __forceinline__ void kgen(int c0, const u16* __restrict__ Are, const u16* __restrict__ Aim,
                                     const u16* __restrict__ Bre, const u16* __restrict__ Bim,
                                     const u16* __restrict__ Cre, const u16* __restrict__ Cim,
                                     const u16* __restrict__ logdt, u32x4* __restrict__ Kb, char* smem) {
    f32x4* P = (f32x4*)smem;
    float* T = (float*)(smem + 4096);
    const int tid = threadIdx.x, lane = tid & 63, wave = tid >> 6;
    {
        const int ch = tid >> 7, n = tid & 127, d = c0 + ch;
        const float dt = rbf(expf(bf_u16(logdt[d])));
        const int o = d * NS + n;
        const float are = dt * bf_u16(Are[o]), aim = dt * bf_u16(Aim[o]);
        const float br = dt * bf_u16(Bre[o]), bi = dt * bf_u16(Bim[o]);
        const float cr = bf_u16(Cre[o]), ci = bf_u16(Cim[o]);
        f32x4 p;
        p[0] = are * 1.4426950408889634f;
        p[1] = aim * 0.15915494309189535f;
        p[2] = cr * br - ci * bi;
        p[3] = cr * bi + ci * br;
        P[ch * 128 + n] = p;
    }
    __syncthreads();
    const int ch = wave >> 1, hf = wave & 1;
    const int m = lane & 15, g = lane >> 4;
    const float pm = (float)m;
    f32x4 acc[4] = {{0, 0, 0, 0}, {0, 0, 0, 0}, {0, 0, 0, 0}, {0, 0, 0, 0}};
    float k2048 = 0.f;
#pragma unroll 1
    for (int kk = 0; kk < 4; ++kk) {
        const int ks = hf * 4 + kk;
        u32x4 A[4], B[4];
#pragma unroll
        for (int s = 0; s < 4; ++s) {
            const f32x4 p = P[ch * 128 + 16 * ks + 4 * g + s];
            const float mag = __builtin_amdgcn_exp2f(p[0] * pm);
            const float ph = __builtin_amdgcn_fractf(p[1] * pm);
            cplx b0 = {mag * __builtin_amdgcn_cosf(ph), mag * __builtin_amdgcn_sinf(ph)};
            cplx b1 = csq(b0), b2 = csq(b1), b3 = csq(b2);
            cplx b4 = csq(b3), b5 = csq(b4), b6 = csq(b5), b7 = csq(b6);
            cplx W = {p[2], p[3]};
            cplx x0 = cmul(W, b4), x1 = cmul(W, b5), x2 = cmul(W, b6), x3 = cmul(W, b7);
            k2048 += fmaf(x3.r, b7.r, -x3.i * b7.i);
            A[0][s] = pk_bf(x0.r, x0.i); A[1][s] = pk_bf(x1.r, x1.i);
            A[2][s] = pk_bf(x2.r, x2.i); A[3][s] = pk_bf(x3.r, x3.i);
            B[0][s] = pk_bf(b0.r, -b0.i); B[1][s] = pk_bf(b1.r, -b1.i);
            B[2][s] = pk_bf(b2.r, -b2.i); B[3][s] = pk_bf(b3.r, -b3.i);
        }
#pragma unroll
        for (int e = 0; e < 4; ++e) acc[e] = mfma16(A[e], B[e], acc[e]);
    }
    float* Tw = T + (hf * 2 + ch) * 648;
#pragma unroll
    for (int r = 0; r < 4; ++r) {
        Tw[16 * (4 * g + r) + m] = acc[0][r];
        if (g >= 2) {
#pragma unroll
            for (int e = 1; e < 4; ++e) Tw[128 * e + 16 * (4 * g + r) + m] = acc[e][r];
        }
    }
    k2048 += __shfl_xor(k2048, 16);
    k2048 += __shfl_xor(k2048, 32);
    if (lane == 8) Tw[640] = k2048;
    __syncthreads();
#pragma unroll
    for (int c = 0; c < 2; ++c) {
        const float* T0 = T + c * 648;
        const float* T1 = T + (2 + c) * 648;
        float kv[8];
#pragma unroll
        for (int j = 0; j < 8; ++j) {
            const int l = 8 * tid + j;
            const int bits = pk_bf((float)l, 0.f) & 0xffff;
            const int idx = (l < 128) ? l : bits - 17024;
            kv[j] = T0[idx] + T1[idx];
        }
        u32x4 o = {pk_bf(kv[0], kv[1]), pk_bf(kv[2], kv[3]), pk_bf(kv[4], kv[5]), pk_bf(kv[6], kv[7])};
        Kb[(c0 + c) * 256 + tid] = o;
    }
}

__global__ void __launch_bounds__(256) k_prep(const u32* __restrict__ k, const u32* __restrict__ v, const u32x4* __restrict__ shiftk,
                                              u32x4* __restrict__ kvT,
                                              const u16* __restrict__ Are, const u16* __restrict__ Aim,
                                              const u16* __restrict__ Bre, const u16* __restrict__ Bim,
                                              const u16* __restrict__ Cre, const u16* __restrict__ Cim,
                                              const u16* __restrict__ logdt, u32x4* __restrict__ Kb) {
    __shared__ __attribute__((aligned(16))) char smem[4096 + 4 * 648 * 4];
    const int b = blockIdx.x;
    if (b < 256) prep_rows(b, k, v, shiftk, kvT);
    else kgen(2 * (b - 256), Are, Aim, Bre, Bim, Cre, Cim, logdt, Kb, smem);
}

constexpr int U_STRIDE = 4608;
constexpr int K_ARR = 4160;
constexpr int OFF_U = 0;
constexpr int OFF_K = 2 * U_STRIDE;
constexpr int OFF_X = OFF_K + 4 * K_ARR;
constexpr int OFF_Y = OFF_X + 16384;
constexpr int CONV_LDS = OFF_Y + 8192;

__device__ __forceinline__ void stage_taps(char* smem, int c, u32x4 d, u32 prev) {
    const int tid = threadIdx.x;
    u32x4 e, o;
    e[0] = __builtin_amdgcn_alignbit(d[3], d[3], 16);
    e[1] = __builtin_amdgcn_alignbit(d[2], d[2], 16);
    e[2] = __builtin_amdgcn_alignbit(d[1], d[1], 16);
    e[3] = __builtin_amdgcn_alignbit(d[0], d[0], 16);
    o[0] = (d[3] & 0xffffu) | (d[2] & 0xffff0000u);
    o[1] = (d[2] & 0xffffu) | (d[1] & 0xffff0000u);
    o[2] = (d[1] & 0xffffu) | (d[0] & 0xffff0000u);
    o[3] = (d[0] & 0xffffu) | (prev & 0xffff0000u);
    char* base = smem + OFF_K + c * 2 * K_ARR;
    *(u32x4*)(base + (1020 - 4 * tid) * 4) = e;
    *(u32x4*)(base + K_ARR + (1020 - 4 * tid) * 4) = o;
}
__device__ __forceinline__ void stage_zeros(char* smem) {
    const int tid = threadIdx.x;
    const u32x4 z = {0, 0, 0, 0};
    if (tid < 16) {
        *(u32x4*)(smem + OFF_K + (tid >> 2) * K_ARR + 4096 + (tid & 3) * 16) = z;
    } else if (tid >= 64 && tid < 124) {
        const int i = tid - 64;
        *(u32x4*)(smem + OFF_U + (i / 30) * U_STRIDE + (i % 30) * 16) = z;
    }
}

__device__ __forceinline__ void conv_core(char* smem) {
    const int tid = threadIdx.x, lane = tid & 63, wave = tid >> 6;
    const int ch = wave >> 1, hf = wave & 1;
    const int m = lane & 15, g = lane >> 4;
    f32x4 acc[8];
#pragma unroll
    for (int t = 0; t < 8; ++t) acc[t] = (f32x4){0, 0, 0, 0};
    const char* abase = smem + OFF_K + ch * 2 * K_ARR + ((m & 1) ? 0 : K_ARR) + (1015 - (m >> 1) + 4 * g) * 4;
    const char* bbase = smem + OFF_U + ch * U_STRIDE + 448 + 32 * m + 16 * g;
#pragma unroll 1
    for (int rr = 0; rr < 4; ++rr) {
        const int rho = hf * 4 + rr;
        const char* ap = abase - 64 * rho - 512 * 7;
        const char* bp = bbase - 64 * rho;
        u32x4 A[8], B[8];
#pragma unroll
        for (int a = 0; a < 8; ++a) {
            const u32* p = (const u32*)(ap + 512 * (7 - a));
            A[a] = (u32x4){p[0], p[1], p[2], p[3]};
            B[a] = *(const u32x4*)(bp + 512 * a);
        }
#pragma unroll
        for (int a = 0; a < 8; ++a)
#pragma unroll
            for (int b = 0; b + a < 8; ++b) acc[a + b] = mfma16(A[a], B[b], acc[a + b]);
    }
    f32x4* X = (f32x4*)(smem + OFF_X);
    f32x4 keep[4];
#pragma unroll
    for (int i = 0; i < 4; ++i) {
        f32x4 snd;
#pragma unroll
        for (int r = 0; r < 4; ++r) {
            snd[r] = hf ? acc[i][r] : acc[4 + i][r];
            keep[i][r] = hf ? acc[4 + i][r] : acc[i][r];
        }
        X[(wave * 4 + i) * 64 + lane] = snd;
    }
    __syncthreads();
    char* Y = smem + OFF_Y + ch * 4096;
#pragma unroll
    for (int i = 0; i < 4; ++i) {
        f32x4 s = keep[i] + X[((wave ^ 1) * 4 + i) * 64 + lane];
        u32x2 pk = {pk_bf(s[0], s[1]), pk_bf(s[2], s[3])};
        *(u32x2*)(Y + (256 * (hf * 4 + i) + 16 * m + 4 * g) * 2) = pk;
    }
    __syncthreads();
}

__device__ __forceinline__ void unpack8(u32x4 p, float (&f)[8]) {
#pragma unroll
    for (int i = 0; i < 4; ++i) { f[2 * i] = bf_lo(p[i]); f[2 * i + 1] = bf_hi(p[i]); }
}
__device__ __forceinline__ u32x4 pack8(const float (&f)[8]) {
    return (u32x4){pk_bf(f[0], f[1]), pk_bf(f[2], f[3]), pk_bf(f[4], f[5]), pk_bf(f[6], f[7])};
}

__global__ void __launch_bounds__(256) k_convA(const u32x4* __restrict__ kvT, const u32x4* __restrict__ ssmk,
                                               const u32* __restrict__ ssmD, u32x4* __restrict__ yT) {
    __shared__ __attribute__((aligned(16))) char smem[CONV_LDS];
    const int tid = threadIdx.x;
    const int cp = chan_pair(blockIdx.x), c0 = 2 * cp;
    u32x4 kd[2], ku[2]; u32 kp[2];
#pragma unroll
    for (int c = 0; c < 2; ++c) {
        kd[c] = ssmk[(c0 + c) * 256 + tid];
        kp[c] = tid ? ((const u32*)ssmk)[((c0 + c) * 256 + tid) * 4 - 1] : 0u;
        ku[c] = kvT[tid * 512 + c0 + c];
    }
    const u32 dd = ssmD[cp];
    stage_zeros(smem);
#pragma unroll
    for (int c = 0; c < 2; ++c) {
        stage_taps(smem, c, kd[c], kp[c]);
        *(u32x4*)(smem + OFF_U + c * U_STRIDE + 480 + 16 * tid) = ku[c];
    }
    __syncthreads();
    conv_core(smem);
#pragma unroll
    for (int c = 0; c < 2; ++c) {
        float y[8], u[8], o[8];
        unpack8(*(const u32x4*)(smem + OFF_Y + c * 4096 + 16 * tid), y);
        unpack8(ku[c], u);
        const float dsk = c ? bf_hi(dd) : bf_lo(dd);
#pragma unroll
        for (int j = 0; j < 8; ++j) o[j] = y[j] + rbf(u[j] * dsk);
        yT[tid * 512 + c0 + c] = pack8(o);
    }
}

template <bool LAST>
__global__ void __launch_bounds__(256) k_convB(const u32x4* __restrict__ xT, const f32x4* __restrict__ stats,
                                               const u32x4* __restrict__ Kb, const u32* __restrict__ Dskip,
                                               u32x4* __restrict__ oT, u32* __restrict__ out) {
    __shared__ __attribute__((aligned(16))) char smem[CONV_LDS];
    const int tid = threadIdx.x;
    const int cp = chan_pair(blockIdx.x), c0 = 2 * cp;
    u32x4 kd[2], kx[2], ku[2]; u32 kp[2];
#pragma unroll
    for (int c = 0; c < 2; ++c) {
        kd[c] = Kb[(c0 + c) * 256 + tid];
        kp[c] = tid ? ((const u32*)Kb)[((c0 + c) * 256 + tid) * 4 - 1] : 0u;
        kx[c] = xT[tid * 512 + c0 + c];
    }
    f32x4 st[4];
#pragma unroll
    for (int i = 0; i < 4; ++i) st[i] = stats[tid * 4 + i];
    const u32 dd = Dskip[cp];
    stage_zeros(smem);
#pragma unroll
    for (int c = 0; c < 2; ++c) {
        stage_taps(smem, c, kd[c], kp[c]);
        float x[8], u[8];
        unpack8(kx[c], x);
#pragma unroll
        for (int j = 0; j < 8; ++j) u[j] = rbf(x[j] - st[j >> 1][2 * (j & 1)]) * st[j >> 1][2 * (j & 1) + 1];
        ku[c] = pack8(u);
        *(u32x4*)(smem + OFF_U + c * U_STRIDE + 480 + 16 * tid) = ku[c];
    }
    __syncthreads();
    conv_core(smem);
    float o[2][8];
#pragma unroll
    for (int c = 0; c < 2; ++c) {
        float y[8], u[8], x[8];
        unpack8(*(const u32x4*)(smem + OFF_Y + c * 4096 + 16 * tid), y);
        unpack8(ku[c], u);
        unpack8(kx[c], x);
        const float dsk = c ? bf_hi(dd) : bf_lo(dd);
#pragma unroll
        for (int j = 0; j < 8; ++j) o[c][j] = x[j] + rbf(y[j] + rbf(u[j] * dsk));
        if (!LAST) oT[tid * 512 + c0 + c] = pack8(o[c]);
    }
    if (LAST) {
#pragma unroll
        for (int j = 0; j < 8; ++j) out[(8 * tid + j) * 256 + cp] = pk_bf(o[0][j], o[1][j]);
    }
}

template <bool FIRST>
__global__ void __launch_bounds__(256) k_row(const u32* __restrict__ q, const u32x4* __restrict__ xT, const u32x4* __restrict__ yT,
                                             u32x4* __restrict__ oT, f32x2* __restrict__ stats) {
    __shared__ __attribute__((aligned(16))) float red[2][64];
    const int tid = threadIdx.x, w = blockIdx.x;
    float x[8][2], y[2][8];
    if (FIRST) {
#pragma unroll
        for (int r = 0; r < 8; ++r) { u32 p = q[(8 * w + r) * 256 + tid]; x[r][0] = bf_lo(p); x[r][1] = bf_hi(p); }
    } else {
#pragma unroll
        for (int c = 0; c < 2; ++c) {
            float t[8];
            unpack8(xT[w * 512 + 2 * tid + c], t);
#pragma unroll
            for (int r = 0; r < 8; ++r) x[r][c] = t[r];
        }
    }
#pragma unroll
    for (int c = 0; c < 2; ++c) unpack8(yT[w * 512 + 2 * tid + c], y[c]);
    float m[8], rs[8];
    row_stats8(x, m, rs, red[0]);
    u32x4 o[2];
#pragma unroll
    for (int c = 0; c < 2; ++c) {
        float t[8];
#pragma unroll
        for (int r = 0; r < 8; ++r) {
            float u = rbf(rbf(x[r][c] - m[r]) * rs[r]);
            t[r] = x[r][c] + rbf(u * y[c][r]);
        }
        o[c] = pack8(t);
        oT[w * 512 + 2 * tid + c] = o[c];
        unpack8(o[c], t);
#pragma unroll
        for (int r = 0; r < 8; ++r) x[r][c] = t[r];
    }
    row_stats8(x, m, rs, red[1]);
    if (tid < 8) {
        float mm = 0.f, rr = 0.f;
#pragma unroll
        for (int r = 0; r < 8; ++r) if (tid == r) { mm = m[r]; rr = rs[r]; }
        stats[8 * w + tid] = (f32x2){mm, rr};
    }
}

extern "C" void kernel_launch(void* const* d_in, const int* in_sizes, int n_in,
                              void* d_out, int out_size, void* d_ws, size_t ws_size,
                              hipStream_t stream) {
    const u32* q = (const u32*)d_in[0];
    const u32* k = (const u32*)d_in[1];
    const u32* v = (const u32*)d_in[2];
    const u32x4* shiftk = (const u32x4*)d_in[3];
    const u32x4* ssmk = (const u32x4*)d_in[4];
    const u32* ssmD = (const u32*)d_in[5];
    const u16* Are = (const u16*)d_in[7];
    const u16* Aim = (const u16*)d_in[8];
    const u16* Bre = (const u16*)d_in[9];
    const u16* Bim = (const u16*)d_in[10];
    const u16* Cre = (const u16*)d_in[11];
    const u16* Cim = (const u16*)d_in[12];
    const u16* logdt = (const u16*)d_in[13];
    const u32* Dskip = (const u32*)d_in[14];
    char* ws = (char*)d_ws;
    const size_t MB = 1u << 20;
    u32x4* kvT = (u32x4*)(ws + 0 * MB);
    u32x4* yT = (u32x4*)(ws + 2 * MB);
    u32x4* x1T = (u32x4*)(ws + 4 * MB);
    u32x4* x2T = (u32x4*)(ws + 6 * MB);
    u32x4* x3T = (u32x4*)(ws + 8 * MB);
    u32x4* Kb = (u32x4*)(ws + 10 * MB);
    f32x2* st1 = (f32x2*)(ws + 12 * MB);
    f32x2* st3 = (f32x2*)(ws + 12 * MB + 65536);
    k_prep<<<512, 256, 0, stream>>>(k, v, shiftk, kvT, Are, Aim, Bre, Bim, Cre, Cim, logdt, Kb);
    k_convA<<<256, 256, 0, stream>>>(kvT, ssmk, ssmD, yT);
    k_row<true><<<256, 256, 0, stream>>>(q, nullptr, yT, x1T, st1);
    k_convB<false><<<256, 256, 0, stream>>>(x1T, (const f32x4*)st1, Kb, Dskip, x2T, nullptr);
    k_row<false><<<256, 256, 0, stream>>>(nullptr, x2T, yT, x3T, st3);
    k_convB<true><<<256, 256, 0, stream>>>(x3T, (const f32x4*)st3, Kb, Dskip, nullptr, (u32*)d_out);
}
```

```cpp
#include <hip/hip_runtime.h>
#include <hip/hip_bf16.h>
#include <stdint.h>

typedef unsigned int u32;
typedef unsigned short u16;
typedef unsigned long long u64;
typedef __attribute__((ext_vector_type(4))) float f32x4;
typedef __attribute__((ext_vector_type(2))) float f32x2;
typedef __attribute__((ext_vector_type(4))) u32 u32x4;
typedef __attribute__((ext_vector_type(2))) u32 u32x2;
typedef __attribute__((ext_vector_type(4))) short s16x4;
typedef __attribute__((ext_vector_type(8))) __bf16 bf16x8;
typedef __attribute__((ext_vector_type(2))) __bf16 bf16x2;
#define LAS __attribute__((address_space(3)))
#define GAS __attribute__((address_space(1)))
#define RLX_AGENT __ATOMIC_RELAXED, __HIP_MEMORY_SCOPE_AGENT
#define SB() __builtin_amdgcn_sched_barrier(0)
#define BAR() asm volatile("s_waitcnt lgkmcnt(0)\n\ts_barrier" ::: "memory")
#define MEMFENCE() asm volatile("" ::: "memory")

constexpr int DM = 512;
constexpr int NS = 128;
constexpr u32 SENT = 0xffffffffu;
constexpr u32 SPIN_CAP = 20000;

constexpr u32 WS_EPOCH = 0;
constexpr u32 WS_FLAGS = 4096;
constexpr u32 WS_H = 1u << 20;
constexpr u32 WS_HS = 2u << 20;
constexpr u32 WS_USED = WS_H + 6 * WS_HS;

constexpr int T_COPY = 4160, T_CH = 8 * T_COPY;
constexpr int U_STRIDE = 4672;
constexpr int R_ROW = 1088;
constexpr int OFF_T = 0;
constexpr int OFF_U = OFF_T + 2 * T_CH;
constexpr int OFF_X = OFF_U + 2 * U_STRIDE;
constexpr int OFF_Y = OFF_X + 16384;
constexpr int OFF_R = OFF_Y + 8192;
constexpr int OFF_P = OFF_R;
constexpr int OFF_KP = OFF_R + 4096;
constexpr int OFF_KC = OFF_KP + 8 * 648 * 4;
constexpr int OFF_RED = OFF_R + 28 * R_ROW;
constexpr int LDS_BYTES = OFF_RED + 1024;
static_assert(OFF_KC + 2 * 648 * 2 <= OFF_RED, "table alias");

#define STAMP(i)

__device__ __forceinline__ float bf_lo(u32 p) { return __builtin_bit_cast(float, p << 16); }
__device__ __forceinline__ float bf_hi(u32 p) { return __builtin_bit_cast(float, p & 0xffff0000u); }
__device__ __forceinline__ float bf_u16(u16 p) { return __builtin_bit_cast(float, (u32)p << 16); }
__device__ __forceinline__ u32 pk_bf(float lo, float hi) {
    bf16x2 v = {(__bf16)lo, (__bf16)hi};
    return __builtin_bit_cast(u32, v);
}
__device__ __forceinline__ float as_f(u32 x) { return __builtin_bit_cast(float, x); }
__device__ __forceinline__ u32 as_u(float x) { return __builtin_bit_cast(u32, x); }
__device__ __forceinline__ float rbf(float x) { return bf_lo(pk_bf(x, x)); }
__device__ __forceinline__ void unpack8(u32x4 p, float (&f)[8]) {
#pragma unroll
    for (int i = 0; i < 4; ++i) { f[2 * i] = bf_lo(p[i]); f[2 * i + 1] = bf_hi(p[i]); }
}
__device__ __forceinline__ u32x4 pack8(const float (&f)[8]) {
    return (u32x4){pk_bf(f[0], f[1]), pk_bf(f[2], f[3]), pk_bf(f[4], f[5]), pk_bf(f[6], f[7])};
}
template <int CTRL> __device__ __forceinline__ float dppf(float v) {
    return __builtin_bit_cast(float, __builtin_amdgcn_update_dpp(0, __builtin_bit_cast(int, v), CTRL, 0xf, 0xf, true));
}
__device__ __forceinline__ u32x2 tr_read(const char* p) {
    return __builtin_bit_cast(u32x2, __builtin_amdgcn_ds_read_tr16_b64_v4i16((LAS s16x4*)p));
}

typedef __amdgpu_buffer_rsrc_t rsrc_t;
__device__ __forceinline__ u32x4 ld_dev(rsrc_t rs, u32 off) { return __builtin_amdgcn_raw_buffer_load_b128(rs, off, 0, 16); }
__device__ __forceinline__ void st_dev(rsrc_t rs, u32 off, u32x4 v) { __builtin_amdgcn_raw_buffer_store_b128(v, rs, off, 0, 16); }
__device__ __forceinline__ void publish(rsrc_t rs, u32 off, u32x4 v) {
#pragma unroll
    for (int i = 0; i < 4; ++i) v[i] = v[i] < SENT - 1u ? v[i] : SENT - 1u;
    st_dev(rs, off, v);
}
__device__ __forceinline__ void wait_until(u64 t) {
    while (__builtin_amdgcn_s_memrealtime() < t) __builtin_amdgcn_s_sleep(1);
}
__device__ __forceinline__ u32x4 poll_issue(rsrc_t rs, u32 off) {
    MEMFENCE();
    u32x4 x = ld_dev(rs, off);
    MEMFENCE();
    return x;
}
__device__ __forceinline__ void poll_finish(u32x4& x, rsrc_t rs, u32 off) {
    u32 spins = 0;
    while (true) {
        u32 mx = x[0] > x[1] ? x[0] : x[1];
        mx = mx > x[2] ? mx : x[2];
        mx = mx > x[3] ? mx : x[3];
        const bool bad = mx == SENT;
        if (__builtin_amdgcn_ballot_w64(bad) == 0ull) break;
        if (++spins > SPIN_CAP) break;
        __builtin_amdgcn_s_sleep(1);
        MEMFENCE();
        if (bad) x = ld_dev(rs, off);
        MEMFENCE();
    }
}

__device__ __forceinline__ void row_stats(const float (&x)[8], float* red, int lane, int wave, float& mean, float& rstd) {
    float r[8];
#pragma unroll
    for (int j = 0; j < 8; ++j) {
        const auto s = __builtin_amdgcn_permlane32_swap(as_u(x[j]), as_u(x[j] * x[j]), false, false);
        r[j] = as_f(s[0]) + as_f(s[1]);
    }
    float s4[4];
#pragma unroll
    for (int j = 0; j < 4; ++j) {
        const auto s = __builtin_amdgcn_permlane16_swap(as_u(r[j]), as_u(r[j + 4]), false, false);
        s4[j] = as_f(s[0]) + as_f(s[1]);
    }
    float t[2];
#pragma unroll
    for (int j = 0; j < 2; ++j) {
        const float a = s4[j] + dppf<0x128>(s4[j]), b = s4[j + 2] + dppf<0x128>(s4[j + 2]);
        t[j] = (lane & 8) ? b : a;
    }
    const float a = t[0] + dppf<0x141>(t[0]), b = t[1] + dppf<0x141>(t[1]);
    float w = (lane & 4) ? b : a;
    w += dppf<0xB1>(w);
    w += dppf<0x4E>(w);
    if ((lane & 3) == 0) red[(lane >> 2) * 8 + wave] = w;
    BAR();
    const f32x4 p0 = *(const f32x4*)(red + (lane & 15) * 8), p1 = *(const f32x4*)(red + (lane & 15) * 8 + 4);
    const float tot = ((p0[0] + p0[1]) + (p0[2] + p0[3])) + ((p1[0] + p1[1]) + (p1[2] + p1[3]));
    const float m = tot * (1.0f / DM);
    const float ex2 = dppf<0x128>(m);
    const float var = fmaxf(fmaf(-m, m, ex2), 0.f);
    const float sd = rbf(__builtin_amdgcn_sqrtf(rbf(rbf(var) + 1e-5f)));
    mean = rbf(m);
    rstd = __builtin_amdgcn_rcpf(sd);
}
template <int J> __device__ __forceinline__ float ln_row(float x, float mean, float rstd) {
    return (x - dppf<0x150 + J>(mean)) * dppf<0x150 + J>(rstd);
}
__device__ __forceinline__ void layer_norm(const float (&x)[8], float (&u)[8], float* red, int lane, int wave) {
    float mean, rstd;
    row_stats(x, red, lane, wave, mean, rstd);
    u[0] = ln_row<0>(x[0], mean, rstd); u[1] = ln_row<1>(x[1], mean, rstd);
    u[2] = ln_row<2>(x[2], mean, rstd); u[3] = ln_row<3>(x[3], mean, rstd);
    u[4] = ln_row<4>(x[4], mean, rstd); u[5] = ln_row<5>(x[5], mean, rstd);
    u[6] = ln_row<6>(x[6], mean, rstd); u[7] = ln_row<7>(x[7], mean, rstd);
}

__device__ __forceinline__ void stage_taps(char* smem, int tid, u32x4 prev, u32x4 own) {
    const u32 D[8] = {prev[0], prev[1], prev[2], prev[3], own[0], own[1], own[2], own[3]};
    u32 E[8], O[8];
#pragma unroll
    for (int y = 1; y < 8; ++y) {
        E[y] = __builtin_amdgcn_alignbit(D[y], D[y], 16);
        O[y] = (D[y] & 0xffffu) | (D[y - 1] & 0xffff0000u);
    }
    char* base = smem + OFF_T + (tid >> 8) * T_CH + (255 - (tid & 255)) * 16;
#pragma unroll
    for (int s = 0; s < 8; ++s) {
        const int y = 7 - (s >> 1);
        const u32x4 v = (s & 1) ? (u32x4){O[y], O[y - 1], O[y - 2], O[y - 3]} : (u32x4){E[y], E[y - 1], E[y - 2], E[y - 3]};
        *(u32x4*)(base + s * T_COPY) = v;
    }
}

struct Frags { u32x4 A[8], B[8]; };
template <bool INIT> __device__ __forceinline__ void mma(f32x4& acc, const u32x4& a, const u32x4& b) {
    if constexpr (INIT) asm volatile("v_mfma_f32_16x16x32_bf16 %0, %1, %2, 0" : "=&v"(acc) : "v"(a), "v"(b));
    else asm volatile("v_mfma_f32_16x16x32_bf16 %0, %1, %2, %0" : "+v"(acc) : "v"(a), "v"(b));
}
template <int I> __device__ __forceinline__ void load_one(Frags& f, const char* ap, const char* bp) {
    if constexpr (I == 0) f.A[0] = *(const u32x4*)(ap + 512 * 7);
    else if constexpr (I <= 8) f.B[I - 1] = *(const u32x4*)(bp + 512 * (I - 1));
    else f.A[I - 8] = *(const u32x4*)(ap + 512 * (7 - (I - 8)));
}
template <int I> __device__ __forceinline__ void load_all(Frags& f, const char* ap, const char* bp) {
    if constexpr (I < 16) { load_one<I>(f, ap, bp); SB(); load_all<I + 1>(f, ap, bp); }
}
template <int T0, int T1, int T2, int T3> struct TileSet {
    static constexpr int tile(int i) { return i == 0 ? T0 : i == 1 ? T1 : i == 2 ? T2 : T3; }
    static constexpr int walk(int I, bool want_tile) {
        int n = 0;
        for (int a = 0; a < 8; ++a)
            for (int j = 0; j < 4; ++j)
                if (tile(j) >= a) { if (n == I) return want_tile ? tile(j) : a; ++n; }
        return -1;
    }
};
constexpr int tri_a(int I) { int a = 0; while (I >= 8 - a) { I -= 8 - a; ++a; } return a; }
constexpr int tri_b(int I) { int a = 0; while (I >= 8 - a) { I -= 8 - a; ++a; } return I; }
template <int I, bool FIRST> __device__ __forceinline__ void class_step(const Frags& cur, Frags& nxt, f32x4 (&acc)[8], const char* ap, const char* bp) {
    if constexpr (I < 36) {
        constexpr int a = tri_a(I), b = tri_b(I);
        mma<FIRST && a == 0>(acc[a + b], cur.A[a], cur.B[b]);
        if constexpr ((I & 1) == 1 && (I >> 1) < 16) load_one<(I >> 1)>(nxt, ap, bp);
        SB();
        class_step<I + 1, FIRST>(cur, nxt, acc, ap, bp);
    }
}
template <class TS, int I, int TO, bool PRE> __device__ __forceinline__ void set_step(const Frags& cur, Frags& nxt, f32x4 (&acc)[8], const char* ap, const char* bp) {
    if constexpr (I < TO) {
        constexpr int t = TS::walk(I, true), a = TS::walk(I, false);
        mma<false>(acc[t], cur.A[a], cur.B[t - a]);
        if constexpr (PRE && I < 16) load_one<I>(nxt, ap, bp);
        SB();
        set_step<TS, I + 1, TO, PRE>(cur, nxt, acc, ap, bp);
    }
}
template <class SEND, class KEEP>
__device__ __forceinline__ void conv_half(char* smem, const char* ap, const char* bp, int wave, int lane, int ch) {
    f32x4 acc[8];
    Frags f0, f1;
    load_all<0>(f0, ap, bp);
    class_step<0, true>(f0, f1, acc, ap - 64, bp - 64);
    class_step<0, false>(f1, f0, acc, ap - 128, bp - 128);
    set_step<SEND, 0, 18, true>(f0, f1, acc, ap - 192, bp - 192);
    set_step<SEND, 0, 18, false>(f1, f0, acc, ap, bp);
    set_step<KEEP, 0, 4, false>(f0, f1, acc, ap, bp);
    asm volatile("s_nop 3" : "+v"(acc[SEND::tile(0)]), "+v"(acc[SEND::tile(1)]), "+v"(acc[SEND::tile(2)]), "+v"(acc[SEND::tile(3)]));
    f32x4* X = (f32x4*)(smem + OFF_X);
#pragma unroll
    for (int i = 0; i < 4; ++i) X[(wave * 4 + i) * 64 + lane] = acc[SEND::tile(i)];
    SB();
    set_step<KEEP, 4, 18, false>(f0, f1, acc, ap, bp);
    BAR();
    SB();
    f32x4 o[4];
#pragma unroll
    for (int i = 0; i < 4; ++i) o[i] = X[((wave ^ 1) * 4 + i) * 64 + lane];
    SB();
    set_step<KEEP, 0, 18, false>(f1, f0, acc, ap, bp);
    asm volatile("s_nop 7" : "+v"(acc[KEEP::tile(0)]), "+v"(acc[KEEP::tile(1)]), "+v"(acc[KEEP::tile(2)]), "+v"(acc[KEEP::tile(3)]));
    char* Y = smem + OFF_Y + ch * 4096 + (16 * (lane & 15) + 4 * (lane >> 4)) * 2;
#pragma unroll
    for (int i = 0; i < 4; ++i) {
        const f32x4 s = acc[KEEP::tile(i)] + o[i];
        const u32x2 pk = {pk_bf(s[0], s[1]), pk_bf(s[2], s[3])};
        *(u32x2*)(Y + 512 * KEEP::tile(i)) = pk;
    }
}
__device__ __forceinline__ void conv(char* smem, int wave, int lane, rsrc_t rs, int id, int fill0) {
    if (wave < 4) {
        const int ch = wave >> 1, hf = wave & 1;
        const int m = lane & 15, g = lane >> 4;
        const char* ap = smem + OFF_T + ch * T_CH + ((7 - m) & 7) * T_COPY + (m < 8 ? 4048 : 4032) + 16 * g - 512 * 7 - 256 * hf;
        const char* bp = smem + OFF_U + ch * U_STRIDE + 448 + 32 * m + 16 * g - 256 * hf;
        if (hf == 0) conv_half<TileSet<1, 3, 4, 6>, TileSet<0, 2, 5, 7>>(smem, ap, bp, wave, lane, ch);
        else conv_half<TileSet<0, 2, 5, 7>, TileSet<1, 3, 4, 6>>(smem, ap, bp, wave, lane, ch);
    } else {
        if (fill0 >= 0) {
            const u32x4 sent = {SENT, SENT, SENT, SENT};
            const u32 off = WS_H + fill0 * WS_HS + id * 8192 + ((wave - 4) * 64 + lane) * 32;
            st_dev(rs, off, sent); st_dev(rs, off + 16, sent);
            st_dev(rs, off + WS_HS, sent); st_dev(rs, off + WS_HS + 16, sent);
            asm volatile("s_waitcnt vmcnt(0)" ::: "memory");
        }
        BAR();
    }
    BAR();
}

struct cplx { float r, i; };
__device__ __forceinline__ cplx csq(cplx a) { return {fmaf(a.r, a.r, -a.i * a.i), (a.r + a.r) * a.i}; }
__device__ __forceinline__ cplx cmul(cplx a, cplx b) { return {fmaf(a.r, b.r, -a.i * b.i), fmaf(a.r, b.i, a.i * b.r)}; }
__device__ __forceinline__ f32x4 mfma16(u32x4 a, u32x4 b, f32x4 c) {
    return __builtin_amdgcn_mfma_f32_16x16x32_bf16(__builtin_bit_cast(bf16x8, a), __builtin_bit_cast(bf16x8, b), c, 0, 0, 0);
}
__device__ __forceinline__ void kgen_step(const f32x4* P, int lane, f32x4 (&acc)[4], float& k2048) {
    const int g = lane >> 4;
    const float pm = (float)(lane & 15);
    u32x4 A[4], B[4];
#pragma unroll
    for (int s = 0; s < 4; ++s) {
        const f32x4 p = P[4 * g + s];
        const float mag = __builtin_amdgcn_exp2f(p[0] * pm);
        const float ph = __builtin_amdgcn_fractf(p[1] * pm);
        const cplx b0 = {mag * __builtin_amdgcn_cosf(ph), mag * __builtin_amdgcn_sinf(ph)};
        const cplx b1 = csq(b0), b2 = csq(b1), b3 = csq(b2);
        const cplx b4 = csq(b3), b5 = csq(b4), b6 = csq(b5), b7 = csq(b6);
        const cplx W = {p[2], p[3]};
        const cplx x0 = cmul(W, b4), x1 = cmul(W, b5), x2 = cmul(W, b6), x3 = cmul(W, b7);
        k2048 += fmaf(x3.r, b7.r, -x3.i * b7.i);
        A[0][s] = pk_bf(x0.r, x0.i); A[1][s] = pk_bf(x1.r, x1.i);
        A[2][s] = pk_bf(x2.r, x2.i); A[3][s] = pk_bf(x3.r, x3.i);
        B[0][s] = pk_bf(b0.r, -b0.i); B[1][s] = pk_bf(b1.r, -b1.i);
        B[2][s] = pk_bf(b2.r, -b2.i); B[3][s] = pk_bf(b3.r, -b3.i);
    }
#pragma unroll
    for (int e = 0; e < 4; ++e) acc[e] = mfma16(A[e], B[e], acc[e]);
}

__global__ void __launch_bounds__(512, 2)
h3s4_kernel(const char* __restrict__ q, const char* __restrict__ k, const char* __restrict__ v,
            const u32x2* __restrict__ shiftk, const u32x4* __restrict__ ssmk, const u32* __restrict__ ssmD,
            const u16* __restrict__ Are, const u16* __restrict__ Aim, const u16* __restrict__ Bre, const u16* __restrict__ Bim,
            const u16* __restrict__ Cre, const u16* __restrict__ Cim, const u16* __restrict__ logdt, const u32* __restrict__ Dskip,
            u16* __restrict__ out, char* ws
            ) {
    __shared__ __attribute__((aligned(16))) char smem[LDS_BYTES];
    const int tid = threadIdx.x, lane = tid & 63, wave = __builtin_amdgcn_readfirstlane(tid >> 6);
    const int id = (blockIdx.x & 7) * 32 + (blockIdx.x >> 3);
    const rsrc_t rs = __builtin_amdgcn_make_buffer_rsrc(ws, 0, WS_USED, 0x00020000);
    float* red = (float*)(smem + OFF_RED);
    STAMP(0);

    const u32x4 sent = {SENT, SENT, SENT, SENT};
    const u32 own_off = id * 8192 + tid * 16;
    st_dev(rs, WS_H + 0 * WS_HS + own_off, sent);
    st_dev(rs, WS_H + 1 * WS_HS + own_off, sent);
#pragma unroll
    for (int s = 0; s < 4; ++s) {
        const int slot = wave + 8 * s;
        if (slot < 28) {
            const int row = 8 * id + (slot < 12 ? slot - 4 : slot < 20 ? slot - 12 : slot - 20);
            const char* src = slot < 12 ? k : slot < 20 ? v : q;
            char* dst = smem + OFF_R + slot * R_ROW;
            if (row >= 0) __builtin_amdgcn_global_load_lds((const GAS u32*)(src + row * 1024 + lane * 16), (LAS u32*)dst, 16, 0, 0);
            else *(u32x4*)(dst + lane * 16) = (u32x4){0, 0, 0, 0};
        }
    }
    const u32 epoch = __hip_atomic_load((GAS u32*)(ws + WS_EPOCH), RLX_AGENT) + 1u;
    const u32x2 sk = shiftk[tid];
    const int tch = tid >> 8, ti = tid & 255;
    u32x4 tap_own = ssmk[(2 * id + tch) * 256 + ti];
    u32x4 tap_prev = ssmk[(2 * id + tch) * 256 + (ti ? ti - 1 : 0)];
    const u32 dA = ssmD[id], dB = Dskip[id];
    f32x4 prm = {0, 0, 0, 0};
    {
        const int d = 2 * id + ((tid >> 7) & 1), o = d * NS + (tid & 127);
        const float dt = rbf(expf(bf_u16(logdt[d])));
        const float are = dt * bf_u16(Are[o]), aim = dt * bf_u16(Aim[o]);
        const float br = dt * bf_u16(Bre[o]), bi = dt * bf_u16(Bim[o]);
        const float cr = bf_u16(Cre[o]), ci = bf_u16(Cim[o]);
        prm[0] = are * 1.4426950408889634f;
        prm[1] = aim * 0.15915494309189535f;
        prm[2] = cr * br - ci * bi;
        prm[3] = cr * bi + ci * br;
    }
    if (tid < 60) *(u32x4*)(smem + OFF_U + (tid / 30) * U_STRIDE + (tid % 30) * 16) = (u32x4){0, 0, 0, 0};
    else if (tid >= 64 && tid < 80) *(u32x4*)(smem + OFF_T + (tid - 64) * T_COPY + 4096) = (u32x4){0, 0, 0, 0};
    asm volatile("s_waitcnt vmcnt(0)" ::: "memory");
    BAR();
    if (tid < 32) __hip_atomic_store((GAS u32*)(ws + WS_FLAGS) + tid * 256 + id, epoch, RLX_AGENT);
    STAMP(1);

    float x[8];
    {
        const char* rp = smem + OFF_R + ((lane & 15) >> 2) * R_ROW + (64 * wave + 16 * (lane >> 4) + 4 * (lane & 3)) * 2;
        const u32x2 k0 = tr_read(rp), k1 = tr_read(rp + 4 * R_ROW), k2 = tr_read(rp + 8 * R_ROW);
        const u32x2 v0 = tr_read(rp + 12 * R_ROW), v1 = tr_read(rp + 16 * R_ROW);
        const u32x2 q0 = tr_read(rp + 20 * R_ROW), q1 = tr_read(rp + 24 * R_ROW);
        const float kf[12] = {bf_lo(k0[0]), bf_hi(k0[0]), bf_lo(k0[1]), bf_hi(k0[1]), bf_lo(k1[0]), bf_hi(k1[0]),
                              bf_lo(k1[1]), bf_hi(k1[1]), bf_lo(k2[0]), bf_hi(k2[0]), bf_lo(k2[1]), bf_hi(k2[1])};
        float vf[8];
        unpack8((u32x4){v0[0], v0[1], v1[0], v1[1]}, vf);
        const float w0 = bf_lo(sk[0]), w1 = bf_hi(sk[0]), w2 = bf_lo(sk[1]), w3 = bf_hi(sk[1]);
        float kv[8];
#pragma unroll
        for (int j = 0; j < 8; ++j) kv[j] = fmaf(kf[j + 4], w0, fmaf(kf[j + 3], w1, fmaf(kf[j + 2], w2, kf[j + 1] * w3))) * vf[j];
        publish(rs, WS_H + 0 * WS_HS + own_off, pack8(kv));
        unpack8((u32x4){q0[0], q0[1], q1[0], q1[1]}, x);
    }
    const u64 t_s0 = __builtin_amdgcn_s_memrealtime();
    STAMP(2);

    if (ti == 0) {
        tap_prev = (u32x4){0, 0, 0, 0};
        tap_own[0] = (tap_own[0] & 0xffff0000u) | (pk_bf(bf_lo(tap_own[0]) + (tch ? bf_hi(dA) : bf_lo(dA)), 0.f) & 0xffffu);
    }
    stage_taps(smem, tid, tap_prev, tap_own);
    float u0[8];
    layer_norm(x, u0, red, lane, wave);
    if (tid < 256) ((f32x4*)(smem + OFF_P))[tid] = prm;
    BAR();
    STAMP(3);

    f32x4 kacc[4] = {{0, 0, 0, 0}, {0, 0, 0, 0}, {0, 0, 0, 0}, {0, 0, 0, 0}};
    float k2048 = 0.f;
    const f32x4* Pw = (const f32x4*)(smem + OFF_P) + (wave >> 2) * 128 + (wave & 3) * 32;
    const u32 flag_off = WS_FLAGS + (blockIdx.x & 31) * 1024 + lane * 16;
    u32x4 fl = {epoch, epoch, epoch, epoch};
    if (wave == 0) fl = poll_issue(rs, flag_off);
    kgen_step(Pw, lane, kacc, k2048);
    if (wave == 0) {
        u32 spins = 0;
        while (__builtin_amdgcn_ballot_w64(fl[0] != epoch || fl[1] != epoch || fl[2] != epoch || fl[3] != epoch) != 0ull) {
            if (++spins > SPIN_CAP) break;
            __builtin_amdgcn_s_sleep(2);
            fl = poll_issue(rs, flag_off);
        }
        if (blockIdx.x == 0 && lane == 0) __hip_atomic_store((GAS u32*)(ws + WS_EPOCH), epoch, RLX_AGENT);
    }
    BAR();
    STAMP(4);

    const u32 col_in = (tid >> 1) * 8192 + (2 * id + (tid & 1)) * 16;
    const u32 row_in = (tid >> 1) * 8192 + id * 32 + (tid & 1) * 16;
    char* const u_dst = smem + OFF_U + (tid & 1) * U_STRIDE + 480 + (tid >> 1) * 16;
    const char* const y_src = smem + OFF_Y + (tid & 1) * 4096 + (tid >> 1) * 16;

    wait_until(t_s0 + 50);
    u32x4 pc = poll_issue(rs, WS_H + 0 * WS_HS + col_in);
    kgen_step(Pw + 16, lane, kacc, k2048);
    {
        float* Tw = (float*)(smem + OFF_KP) + ((wave & 3) * 2 + (wave >> 2)) * 648;
        const int m = lane & 15, g = lane >> 4;
#pragma unroll
        for (int r = 0; r < 4; ++r) {
            Tw[16 * (4 * g + r) + m] = kacc[0][r];
            if (g >= 2) {
#pragma unroll
                for (int e = 1; e < 4; ++e) Tw[128 * e + 16 * (4 * g + r) + m] = kacc[e][r];
            }
        }
        k2048 += __shfl_xor(k2048, 16);
        k2048 += __shfl_xor(k2048, 32);
        if (lane == 8) Tw[640] = k2048;
    }
    poll_finish(pc, rs, WS_H + 0 * WS_HS + col_in);
    *(u32x4*)u_dst = pc;
    BAR();
    STAMP(5);

    conv(smem, wave, lane, rs, id, 2);
    STAMP(6);
    publish(rs, WS_H + 1 * WS_HS + own_off, *(const u32x4*)y_src);
    u64 t_s = __builtin_amdgcn_s_memrealtime();

    {
        const float* T = (const float*)(smem + OFF_KP);
        u16* KC = (u16*)(smem + OFF_KC);
        for (int i = tid; i < 2 * 641; i += 512) {
            const int c = i >= 641, e = i - 641 * c;
            float s = (T[c * 648 + e] + T[(2 + c) * 648 + e]) + (T[(4 + c) * 648 + e] + T[(6 + c) * 648 + e]);
            if (e == 0) s += c ? bf_hi(dB) : bf_lo(dB);
            KC[c * 648 + e] = (u16)pk_bf(s, 0.f);
        }
        BAR();
        u32 d[8];
#pragma unroll
        for (int j = 0; j < 8; ++j) {
            const int l0 = 8 * ti - 8 + 2 * j;
            const u32 bits = pk_bf((float)l0, (float)(l0 + 1));
            const int i0 = l0 < 128 ? l0 : (int)(bits & 0xffffu) - 17024;
            const int i1 = l0 + 1 < 128 ? l0 + 1 : (int)(bits >> 16) - 17024;
            const u32 a = l0 < 0 ? 0u : KC[tch * 648 + i0], b = l0 < 0 ? 0u : KC[tch * 648 + i1];
            d[j] = a | (b << 16);
        }
        stage_taps(smem, tid, (u32x4){d[0], d[1], d[2], d[3]}, (u32x4){d[4], d[5], d[6], d[7]});
    }
    STAMP(7);

    wait_until(t_s + 50);
    u32x4 pr = poll_issue(rs, WS_H + 1 * WS_HS + row_in);
    poll_finish(pr, rs, WS_H + 1 * WS_HS + row_in);
    STAMP(8);
    float yA[8], u[8];
    unpack8(pr, yA);
#pragma unroll
    for (int j = 0; j < 8; ++j) u[j] = fmaf(u0[j], yA[j], x[j]);
    unpack8(pack8(u), x);
    layer_norm(x, u, red + 128, lane, wave);
    publish(rs, WS_H + 2 * WS_HS + own_off, pack8(u));
    t_s = __builtin_amdgcn_s_memrealtime();
    STAMP(9);

    wait_until(t_s + 50);
    pc = poll_issue(rs, WS_H + 2 * WS_HS + col_in);
    poll_finish(pc, rs, WS_H + 2 * WS_HS + col_in);
    *(u32x4*)u_dst = pc;
    BAR();
    STAMP(10);
    conv(smem, wave, lane, rs, id, 4);
    STAMP(11);
    publish(rs, WS_H + 3 * WS_HS + own_off, *(const u32x4*)y_src);
    t_s = __builtin_amdgcn_s_memrealtime();

    wait_until(t_s + 50);
    pr = poll_issue(rs, WS_H + 3 * WS_HS + row_in);
    poll_finish(pr, rs, WS_H + 3 * WS_HS + row_in);
    STAMP(12);
    {
        float y[8];
        unpack8(pr, y);
#pragma unroll
        for (int j = 0; j < 8; ++j) y[j] += x[j];
        unpack8(pack8(y), x);
        layer_norm(x, u, red, lane, wave);
#pragma unroll
        for (int j = 0; j < 8; ++j) u[j] = fmaf(u[j], yA[j], x[j]);
        unpack8(pack8(u), x);
        layer_norm(x, u, red + 128, lane, wave);
    }
    publish(rs, WS_H + 4 * WS_HS + own_off, pack8(u));
    t_s = __builtin_amdgcn_s_memrealtime();
    STAMP(13);

    wait_until(t_s + 50);
    pc = poll_issue(rs, WS_H + 4 * WS_HS + col_in);
    poll_finish(pc, rs, WS_H + 4 * WS_HS + col_in);
    *(u32x4*)u_dst = pc;
    BAR();
    STAMP(14);
    conv(smem, wave, lane, rs, id, -1);
    STAMP(15);
    publish(rs, WS_H + 5 * WS_HS + own_off, *(const u32x4*)y_src);
    t_s = __builtin_amdgcn_s_memrealtime();

    wait_until(t_s + 50);
    pr = poll_issue(rs, WS_H + 5 * WS_HS + row_in);
    poll_finish(pr, rs, WS_H + 5 * WS_HS + row_in);
    STAMP(16);
    {
        float y[8];
        unpack8(pr, y);
#pragma unroll
        for (int j = 0; j < 8; ++j) y[j] += x[j];
        const u32x4 o = pack8(y);
        u16* dst = out + (8 * id) * DM + tid;
#pragma unroll
        for (int j = 0; j < 4; ++j) {
            dst[(2 * j) * DM] = (u16)o[j];
            dst[(2 * j + 1) * DM] = (u16)(o[j] >> 16);
        }
    }
    STAMP(17);
}

extern "C" void kernel_launch(void* const* d_in, const int* in_sizes, int n_in,
                              void* d_out, int out_size, void* d_ws, size_t ws_size,
                              hipStream_t stream) {
    h3s4_kernel<<<256, 512, 0, stream>>>(
        (const char*)d_in[0], (const char*)d_in[1], (const char*)d_in[2], (const u32x2*)d_in[3], (const u32x4*)d_in[4],
        (const u32*)d_in[5], (const u16*)d_in[7], (const u16*)d_in[8], (const u16*)d_in[9], (const u16*)d_in[10],
        (const u16*)d_in[11], (const u16*)d_in[12], (const u16*)d_in[13], (const u32*)d_in[14], (u16*)d_out, (char*)d_ws
        );
}
```

```cpp
#include <hip/hip_runtime.h>
#include <hip/hip_bf16.h>
#include <stdint.h>

typedef unsigned int u32;
typedef unsigned short u16;
typedef unsigned long long u64;
typedef __attribute__((ext_vector_type(4))) float f32x4;
typedef __attribute__((ext_vector_type(2))) float f32x2;
typedef __attribute__((ext_vector_type(4))) u32 u32x4;
typedef __attribute__((ext_vector_type(2))) u32 u32x2;
typedef __attribute__((ext_vector_type(4))) short s16x4;
typedef __attribute__((ext_vector_type(8))) __bf16 bf16x8;
typedef __attribute__((ext_vector_type(2))) __bf16 bf16x2;
#define LAS __attribute__((address_space(3)))
#define GAS __attribute__((address_space(1)))
#define RLX_AGENT __ATOMIC_RELAXED, __HIP_MEMORY_SCOPE_AGENT
#define SB() __builtin_amdgcn_sched_barrier(0)
#define BAR() asm volatile("s_waitcnt lgkmcnt(0)\n\ts_barrier" ::: "memory")
#define MEMFENCE() asm volatile("" ::: "memory")
#define WAIT_VM(n) do { MEMFENCE(); __builtin_amdgcn_s_waitcnt(0x0F70 | ((n) & 15) | (((n) >> 4) << 14)); MEMFENCE(); } while (0)

constexpr int DM = 512;
constexpr int NS = 128;
constexpr u32 SENT = 0xffffffffu;
constexpr u32 SPIN_CAP = 20000;
#define POLL_DELAY 50

constexpr u32 WS_EPOCH = 0;
constexpr u32 WS_FLAGS = 4096;
constexpr u32 WS_H = 1u << 20;
constexpr u32 WS_HS = 2u << 20;
constexpr u32 WS_USED = WS_H + 6 * WS_HS;

constexpr int T_COPY = 4160, T_CH = 8 * T_COPY;
constexpr int U_STRIDE = 4672;
constexpr int R_ROW = 1088;
constexpr int OFF_T = 0;
constexpr int OFF_U = OFF_T + 2 * T_CH;
constexpr int OFF_X = OFF_U + 2 * U_STRIDE;
constexpr int OFF_Y = OFF_X + 16384;
constexpr int OFF_R = OFF_Y + 8192;
constexpr int OFF_P = OFF_R;
constexpr int OFF_KP = OFF_R + 4096;
constexpr int KP_WAVE = 2576;
constexpr int OFF_KC = OFF_KP + 8 * KP_WAVE;
constexpr int OFF_RED = OFF_R + 28 * R_ROW;
constexpr int OFF_ST = OFF_RED + 1024;
constexpr int LDS_BYTES = OFF_ST + 256;
static_assert(OFF_KC + 2 * 648 * 2 <= OFF_RED, "table alias");

#define STAMP(i)

__device__ __forceinline__ float bf_lo(u32 p) { return __builtin_bit_cast(float, p << 16); }
__device__ __forceinline__ float bf_hi(u32 p) { return __builtin_bit_cast(float, p & 0xffff0000u); }
__device__ __forceinline__ float bf_u16(u16 p) { return __builtin_bit_cast(float, (u32)p << 16); }
__device__ __forceinline__ u32 pk_bf(float lo, float hi) {
    bf16x2 v = {(__bf16)lo, (__bf16)hi};
    return __builtin_bit_cast(u32, v);
}
__device__ __forceinline__ float as_f(u32 x) { return __builtin_bit_cast(float, x); }
__device__ __forceinline__ u32 as_u(float x) { return __builtin_bit_cast(u32, x); }
__device__ __forceinline__ float rbf(float x) { return bf_lo(pk_bf(x, x)); }
__device__ __forceinline__ void unpack8(u32x4 p, float (&f)[8]) {
#pragma unroll
    for (int i = 0; i < 4; ++i) { f[2 * i] = bf_lo(p[i]); f[2 * i + 1] = bf_hi(p[i]); }
}
__device__ __forceinline__ u32x4 pack8(const float (&f)[8]) {
    return (u32x4){pk_bf(f[0], f[1]), pk_bf(f[2], f[3]), pk_bf(f[4], f[5]), pk_bf(f[6], f[7])};
}
template <int CTRL> __device__ __forceinline__ float dppf(float v) {
    return __builtin_bit_cast(float, __builtin_amdgcn_update_dpp(0, __builtin_bit_cast(int, v), CTRL, 0xf, 0xf, true));
}

__device__ __forceinline__ void lds_dma16(const char* src, u32 lds) {
    u32 keep;
    asm volatile("s_mov_b32 %0, m0\n\ts_mov_b32 m0, %2\n\ts_nop 0\n\tglobal_load_lds_dwordx4 %1, off\n\ts_mov_b32 m0, %0"
                 : "=&s"(keep) : "v"(src), "s"(lds) : "memory");
}

typedef __amdgpu_buffer_rsrc_t rsrc_t;
__device__ __forceinline__ u32x4 ld_dev(rsrc_t rs, u32 off) { return __builtin_amdgcn_raw_buffer_load_b128(rs, off, 0, 16); }
__device__ __forceinline__ void st_dev(rsrc_t rs, u32 off, u32x4 v) { __builtin_amdgcn_raw_buffer_store_b128(v, rs, off, 0, 16); }
__device__ __forceinline__ void publish(rsrc_t rs, u32 off, u32x4 v) {
#pragma unroll
    for (int i = 0; i < 4; ++i) v[i] = v[i] < SENT - 1u ? v[i] : SENT - 1u;
    st_dev(rs, off, v);
}
__device__ __forceinline__ void wait_until(u64 t) {
    while (__builtin_amdgcn_s_memrealtime() < t) __builtin_amdgcn_s_sleep(1);
}
__device__ __forceinline__ u32x4 poll_issue(rsrc_t rs, u32 off) {
    MEMFENCE();
    u32x4 x = ld_dev(rs, off);
    MEMFENCE();
    return x;
}
__device__ __forceinline__ void poll_finish(u32x4& x, rsrc_t rs, u32 off) {
    u32 spins = 0;
    while (true) {
        u32 mx = x[0] > x[1] ? x[0] : x[1];
        mx = mx > x[2] ? mx : x[2];
        mx = mx > x[3] ? mx : x[3];
        const bool bad = mx == SENT;
        if (__builtin_amdgcn_ballot_w64(bad) == 0ull) break;
        if (++spins > SPIN_CAP) break;
        __builtin_amdgcn_s_sleep(1);
        MEMFENCE();
        if (bad) x = ld_dev(rs, off);
        MEMFENCE();
    }
}

__device__ __forceinline__ void flags_finish(u32x4& fl, rsrc_t rs, u32 off, u32 epoch) {
    u32 spins = 0;
    while (__builtin_amdgcn_ballot_w64(fl[0] != epoch || fl[1] != epoch || fl[2] != epoch || fl[3] != epoch) != 0ull) {
        if (++spins > SPIN_CAP) break;
        __builtin_amdgcn_s_sleep(2);
        fl = poll_issue(rs, off);
    }
}

__device__ __forceinline__ void row_stats(const float (&x)[8], float* red, int lane, int wave, float& mean, float& rstd) {
    u32 a[8], b[8];
#pragma unroll
    for (int j = 0; j < 8; ++j) { a[j] = as_u(x[j]); b[j] = as_u(x[j] * x[j]); asm volatile("" : "+v"(a[j]), "+v"(b[j])); }
    SB();
    float r[8];
#pragma unroll
    for (int j = 0; j < 8; ++j) {
        const auto s = __builtin_amdgcn_permlane32_swap(a[j], b[j], false, false);
        a[j] = s[0]; b[j] = s[1];
    }
    SB();
#pragma unroll
    for (int j = 0; j < 8; ++j) r[j] = as_f(a[j]) + as_f(b[j]);
    SB();
    float s4[4];
#pragma unroll
    for (int j = 0; j < 4; ++j) {
        const auto s = __builtin_amdgcn_permlane16_swap(as_u(r[j]), as_u(r[j + 4]), false, false);
        a[j] = s[0]; b[j] = s[1];
    }
    SB();
#pragma unroll
    for (int j = 0; j < 4; ++j) s4[j] = as_f(a[j]) + as_f(b[j]);
    SB();
#pragma unroll
    for (int j = 0; j < 4; ++j) s4[j] += dppf<0x128>(s4[j]);
    const float t0 = (lane & 8) ? s4[2] : s4[0], t1 = (lane & 8) ? s4[3] : s4[1];
    const float c0 = t0 + dppf<0x141>(t0), c1 = t1 + dppf<0x141>(t1);
    float w = (lane & 4) ? c1 : c0;
    w += dppf<0xB1>(w);
    w += dppf<0x4E>(w);
    if ((lane & 3) == 0) red[(lane >> 2) * 8 + wave] = w;
    BAR();
    const f32x4 p0 = *(const f32x4*)(red + (lane & 15) * 8), p1 = *(const f32x4*)(red + (lane & 15) * 8 + 4);
    const f32x4 p = p0 + p1;
    const float m = ((p[0] + p[1]) + (p[2] + p[3])) * (1.0f / DM);
    const float ex2 = dppf<0x128>(m);
    const float var = fmaxf(fmaf(-m, m, ex2), 0.f);
    const float sd = rbf(__builtin_amdgcn_sqrtf(rbf(rbf(var) + 1e-5f)));
    mean = rbf(m);
    rstd = __builtin_amdgcn_rcpf(sd);
}
__device__ __forceinline__ void layer_norm(const float (&x)[8], float (&u)[8], float* red, int lane, int wave) {
    float mean, rstd;
    row_stats(x, red, lane, wave, mean, rstd);
    asm volatile("s_nop 1\n\t"
                 "v_subrev_f32_dpp %0, %8, %10 row_newbcast:0 row_mask:0xf bank_mask:0xf\n\t"
                 "v_subrev_f32_dpp %1, %8, %11 row_newbcast:1 row_mask:0xf bank_mask:0xf\n\t"
                 "v_subrev_f32_dpp %2, %8, %12 row_newbcast:2 row_mask:0xf bank_mask:0xf\n\t"
                 "v_subrev_f32_dpp %3, %8, %13 row_newbcast:3 row_mask:0xf bank_mask:0xf\n\t"
                 "v_subrev_f32_dpp %4, %8, %14 row_newbcast:4 row_mask:0xf bank_mask:0xf\n\t"
                 "v_subrev_f32_dpp %5, %8, %15 row_newbcast:5 row_mask:0xf bank_mask:0xf\n\t"
                 "v_subrev_f32_dpp %6, %8, %16 row_newbcast:6 row_mask:0xf bank_mask:0xf\n\t"
                 "v_subrev_f32_dpp %7, %8, %17 row_newbcast:7 row_mask:0xf bank_mask:0xf\n\t"
                 "v_mul_f32_dpp %0, %9, %0 row_newbcast:0 row_mask:0xf bank_mask:0xf\n\t"
                 "v_mul_f32_dpp %1, %9, %1 row_newbcast:1 row_mask:0xf bank_mask:0xf\n\t"
                 "v_mul_f32_dpp %2, %9, %2 row_newbcast:2 row_mask:0xf bank_mask:0xf\n\t"
                 "v_mul_f32_dpp %3, %9, %3 row_newbcast:3 row_mask:0xf bank_mask:0xf\n\t"
                 "v_mul_f32_dpp %4, %9, %4 row_newbcast:4 row_mask:0xf bank_mask:0xf\n\t"
                 "v_mul_f32_dpp %5, %9, %5 row_newbcast:5 row_mask:0xf bank_mask:0xf\n\t"
                 "v_mul_f32_dpp %6, %9, %6 row_newbcast:6 row_mask:0xf bank_mask:0xf\n\t"
                 "v_mul_f32_dpp %7, %9, %7 row_newbcast:7 row_mask:0xf bank_mask:0xf"
                 : "=&v"(u[0]), "=&v"(u[1]), "=&v"(u[2]), "=&v"(u[3]), "=&v"(u[4]), "=&v"(u[5]), "=&v"(u[6]), "=&v"(u[7])
                 : "v"(mean), "v"(rstd), "v"(x[0]), "v"(x[1]), "v"(x[2]), "v"(x[3]), "v"(x[4]), "v"(x[5]), "v"(x[6]), "v"(x[7]));
}

__device__ __forceinline__ void stage_taps(char* smem, int tid, u32x4 prev, u32x4 own) {
    const u32 D[8] = {prev[0], prev[1], prev[2], prev[3], own[0], own[1], own[2], own[3]};
    u32 E[8], O[8];
#pragma unroll
    for (int y = 1; y < 8; ++y) {
        E[y] = __builtin_amdgcn_alignbit(D[y], D[y], 16);
        O[y] = (D[y] & 0xffffu) | (D[y - 1] & 0xffff0000u);
    }
    char* base = smem + OFF_T + (tid >> 8) * T_CH + (255 - (tid & 255)) * 16;
#pragma unroll
    for (int s = 0; s < 8; ++s) {
        const int y = 7 - (s >> 1);
        const u32x4 v = (s & 1) ? (u32x4){O[y], O[y - 1], O[y - 2], O[y - 3]} : (u32x4){E[y], E[y - 1], E[y - 2], E[y - 3]};
        *(u32x4*)(base + s * T_COPY) = v;
    }
}

struct cplx2 { f32x2 r, i; };
__device__ __forceinline__ cplx2 csq(cplx2 a) { return {a.r * a.r - a.i * a.i, (a.r + a.r) * a.i}; }
__device__ __forceinline__ cplx2 cmul(cplx2 a, cplx2 b) { return {a.r * b.r - a.i * b.i, a.r * b.i + a.i * b.r}; }
__device__ __forceinline__ f32x4 mfma16(u32x4 a, u32x4 b, f32x4 c) {
    return __builtin_amdgcn_mfma_f32_16x16x32_bf16(__builtin_bit_cast(bf16x8, a), __builtin_bit_cast(bf16x8, b), c, 0, 0, 0);
}
__device__ __forceinline__ void kgen_step(const f32x4* P, int lane, f32x4 (&acc)[4], float& k2048) {
#pragma clang fp contract(fast)
    const int g = lane >> 4;
    const float pm = (float)(lane & 15);
    cplx2 b[2], W[2];
#pragma unroll
    for (int h = 0; h < 2; ++h) {
        const f32x4 p0 = P[4 * g + 2 * h], p1 = P[4 * g + 2 * h + 1];
        const f32x2 ex = (f32x2){p0[0], p1[0]} * pm, an = (f32x2){p0[1], p1[1]} * pm;
        const f32x2 mag = {__builtin_amdgcn_exp2f(ex[0]), __builtin_amdgcn_exp2f(ex[1])};
        const float f0 = __builtin_amdgcn_fractf(an[0]), f1 = __builtin_amdgcn_fractf(an[1]);
        b[h].r = mag * (f32x2){__builtin_amdgcn_cosf(f0), __builtin_amdgcn_cosf(f1)};
        b[h].i = mag * (f32x2){__builtin_amdgcn_sinf(f0), __builtin_amdgcn_sinf(f1)};
        W[h].r = (f32x2){p0[2], p1[2]};
        W[h].i = (f32x2){p0[3], p1[3]};
    }
    u32x4 A[4], B[4];
#pragma unroll
    for (int e = 0; e < 4; ++e) {
#pragma unroll
        for (int h = 0; h < 2; ++h) {
            B[e][2 * h] = pk_bf(b[h].r[0], -b[h].i[0]);
            B[e][2 * h + 1] = pk_bf(b[h].r[1], -b[h].i[1]);
            b[h] = csq(b[h]);
        }
    }
#pragma unroll
    for (int e = 0; e < 4; ++e) {
#pragma unroll
        for (int h = 0; h < 2; ++h) {
            const cplx2 x = cmul(W[h], b[h]);
            A[e][2 * h] = pk_bf(x.r[0], x.i[0]);
            A[e][2 * h + 1] = pk_bf(x.r[1], x.i[1]);
            if (e == 3) {
                const f32x2 z = x.r * b[h].r - x.i * b[h].i;
                k2048 += z[0] + z[1];
            } else b[h] = csq(b[h]);
        }
        acc[e] = mfma16(A[e], B[e], acc[e]);
    }
}

struct KState { f32x4 acc[4]; float k2048; };
__device__ __forceinline__ const f32x4* kgen_params(char* smem, int wave) {
    return (const f32x4*)(smem + OFF_P) + (wave >> 2) * 128 + (wave & 3) * 32;
}
__device__ __forceinline__ void kgen_save(char* smem, int wave, int lane, const KState& ks) {
    char* p = smem + OFF_KP + wave * KP_WAVE;
    *(f32x4*)(p + lane * 16) = ks.acc[0];
    if (lane >= 32) {
#pragma unroll
        for (int e = 1; e < 4; ++e) *(f32x4*)(p + 512 * e + lane * 16) = ks.acc[e];
    }
    const auto s = __builtin_amdgcn_permlane32_swap(as_u(ks.k2048), as_u(ks.k2048), false, false);
    const float h = as_f(s[0]) + as_f(s[1]);
    const auto t = __builtin_amdgcn_permlane16_swap(as_u(h), as_u(h), false, false);
    const float z = as_f(t[0]) + as_f(t[1]);
    if (lane == 8) *(float*)(p + 2560) = z;
}
__device__ __forceinline__ void kgen_load(const char* smem, int wave, int lane, KState& ks) {
    const char* p = smem + OFF_KP + wave * KP_WAVE;
    ks.acc[0] = *(const f32x4*)(p + lane * 16);
#pragma unroll
    for (int e = 1; e < 4; ++e) ks.acc[e] = *(const f32x4*)(p + 512 * e + (lane | 32) * 16);
    ks.k2048 = lane == 8 ? *(const float*)(p + 2560) : 0.f;
}
__device__ __forceinline__ void kgen_reduce(char* smem, int wave, int lane, u32 dskip) {
    const int c = wave >> 2, e = wave & 3, m = lane & 15, g = lane >> 4;
    const char* p = smem + OFF_KP + c * 4 * KP_WAVE + (e ? 512 * e + (lane | 32) * 16 : lane * 16);
    f32x4 s = (*(const f32x4*)p + *(const f32x4*)(p + KP_WAVE)) + (*(const f32x4*)(p + 2 * KP_WAVE) + *(const f32x4*)(p + 3 * KP_WAVE));
    u16* KC = (u16*)(smem + OFF_KC) + c * 648;
    if (e == 0 && lane == 0) s[0] += c ? bf_hi(dskip) : bf_lo(dskip);
    const u32 lo = pk_bf(s[0], s[1]), hi = pk_bf(s[2], s[3]);
    if (e == 0 || g >= 2) {
        u16* o = KC + 128 * e + 64 * g + m;
        o[0] = (u16)lo; o[16] = (u16)(lo >> 16); o[32] = (u16)hi; o[48] = (u16)(hi >> 16);
    }
    if (e == 3 && lane == 0) {
        const char* z = smem + OFF_KP + c * 4 * KP_WAVE + 2560;
        const float k = (*(const float*)z + *(const float*)(z + KP_WAVE)) + (*(const float*)(z + 2 * KP_WAVE) + *(const float*)(z + 3 * KP_WAVE));
        KC[640] = (u16)pk_bf(k, 0.f);
    }
}

struct Frags { u32x4 A[8], B[8]; };
template <bool INIT> __device__ __forceinline__ void mma(f32x4& acc, const u32x4& a, const u32x4& b) {
    if constexpr (INIT) asm volatile("v_mfma_f32_16x16x32_bf16 %0, %1, %2, 0" : "=&v"(acc) : "v"(a), "v"(b));
    else asm volatile("v_mfma_f32_16x16x32_bf16 %0, %1, %2, %0" : "+v"(acc) : "v"(a), "v"(b));
}
template <int I> __device__ __forceinline__ void load_one(Frags& f, const char* ap, const char* bp) {
    if constexpr (I == 0) f.A[0] = *(const u32x4*)(ap + 512 * 7);
    else if constexpr (I <= 8) f.B[I - 1] = *(const u32x4*)(bp + 512 * (I - 1));
    else f.A[I - 8] = *(const u32x4*)(ap + 512 * (7 - (I - 8)));
}
template <int I> __device__ __forceinline__ void load_all(Frags& f, const char* ap, const char* bp) {
    if constexpr (I < 16) { load_one<I>(f, ap, bp); SB(); load_all<I + 1>(f, ap, bp); }
}
template <int T0, int T1, int T2, int T3> struct TileSet {
    static constexpr int tile(int i) { return i == 0 ? T0 : i == 1 ? T1 : i == 2 ? T2 : T3; }
    static constexpr int walk(int I, bool want_tile) {
        int n = 0;
        for (int a = 0; a < 8; ++a)
            for (int j = 0; j < 4; ++j)
                if (tile(j) >= a) { if (n == I) return want_tile ? tile(j) : a; ++n; }
        return -1;
    }
};
constexpr int tri_a(int I) { int a = 0; while (I >= 8 - a) { I -= 8 - a; ++a; } return a; }
constexpr int tri_b(int I) { int a = 0; while (I >= 8 - a) { I -= 8 - a; ++a; } return I; }
template <int I, bool FIRST> __device__ __forceinline__ void class_step(const Frags& cur, Frags& nxt, f32x4 (&acc)[8], const char* ap, const char* bp) {
    if constexpr (I < 36) {
        constexpr int a = tri_a(I), b = tri_b(I);
        mma<FIRST && a == 0>(acc[a + b], cur.A[a], cur.B[b]);
        if constexpr ((I & 1) == 1 && (I >> 1) < 16) load_one<(I >> 1)>(nxt, ap, bp);
        SB();
        class_step<I + 1, FIRST>(cur, nxt, acc, ap, bp);
    }
}
template <class TS, int I, int TO, bool PRE> __device__ __forceinline__ void set_step(const Frags& cur, Frags& nxt, f32x4 (&acc)[8], const char* ap, const char* bp) {
    if constexpr (I < TO) {
        constexpr int t = TS::walk(I, true), a = TS::walk(I, false);
        mma<false>(acc[t], cur.A[a], cur.B[t - a]);
        if constexpr (PRE && I < 16) load_one<I>(nxt, ap, bp);
        SB();
        set_step<TS, I + 1, TO, PRE>(cur, nxt, acc, ap, bp);
    }
}
template <class TS, int J> __device__ __forceinline__ void tile_step(const Frags& cur, f32x4 (&acc)[8]) {
    constexpr int t = TS::tile(J);
#pragma unroll
    for (int a = 0; a <= t; ++a) { mma<false>(acc[t], cur.A[a], cur.B[t - a]); SB(); }
}
template <class TS, int J> __device__ __forceinline__ void tile_out(char* Y, f32x4 (&acc)[8], const f32x4 (&o)[4]) {
    constexpr int t = TS::tile(J);
    const f32x4 s = acc[t] + o[J];
    const u32x2 pk = {pk_bf(s[0], s[1]), pk_bf(s[2], s[3])};
    *(u32x2*)(Y + 512 * t) = pk;
    SB();
}
template <class SEND, class KEEP>
__device__ __forceinline__ void conv_half(char* smem, const u32x4 (&A0)[8], const char* ap, const char* bp, int wave, int lane, int ch) {
    f32x4 acc[8];
    Frags f0, f1;
#pragma unroll
    for (int a = 0; a < 8; ++a) f0.A[a] = A0[a];
#pragma unroll
    for (int b = 0; b < 8; ++b) { f0.B[b] = *(const u32x4*)(bp + 512 * b); SB(); }
    class_step<0, true>(f0, f1, acc, ap - 64, bp - 64);
    class_step<0, false>(f1, f0, acc, ap - 128, bp - 128);
    set_step<SEND, 0, 18, true>(f0, f1, acc, ap - 192, bp - 192);
    set_step<SEND, 0, 18, false>(f1, f0, acc, ap, bp);
    set_step<KEEP, 0, 4, false>(f0, f1, acc, ap, bp);
    asm volatile("s_nop 3" : "+v"(acc[SEND::tile(0)]), "+v"(acc[SEND::tile(1)]), "+v"(acc[SEND::tile(2)]), "+v"(acc[SEND::tile(3)]));
    f32x4* X = (f32x4*)(smem + OFF_X);
#pragma unroll
    for (int i = 0; i < 4; ++i) X[(wave * 4 + i) * 64 + lane] = acc[SEND::tile(i)];
    SB();
    set_step<KEEP, 4, 18, false>(f0, f1, acc, ap, bp);
    BAR();
    SB();
    f32x4 o[4];
#pragma unroll
    for (int i = 0; i < 4; ++i) o[i] = X[((wave ^ 1) * 4 + i) * 64 + lane];
    SB();
    char* Y = smem + OFF_Y + ch * 4096 + (16 * (lane & 15) + 4 * (lane >> 4)) * 2;
    tile_step<KEEP, 0>(f1, acc);
    tile_step<KEEP, 1>(f1, acc);
    tile_step<KEEP, 2>(f1, acc);
    asm volatile("s_nop 3" : "+v"(acc[KEEP::tile(0)]), "+v"(acc[KEEP::tile(1)]));
    tile_out<KEEP, 0>(Y, acc, o);
    tile_out<KEEP, 1>(Y, acc, o);
    tile_step<KEEP, 3>(f1, acc);
    asm volatile("" : "+v"(acc[KEEP::tile(2)]));
    tile_out<KEEP, 2>(Y, acc, o);
    asm volatile("s_nop 7" : "+v"(acc[KEEP::tile(3)]));
    tile_out<KEEP, 3>(Y, acc, o);
}
struct ConvAddr { const char* ap; const char* bp; };
__device__ __forceinline__ ConvAddr conv_addr(char* smem, int wave, int lane) {
    const int ch = (wave >> 1) & 1, hf = wave & 1;
    const int m = lane & 15, g = lane >> 4;
    ConvAddr c;
    c.ap = smem + OFF_T + ch * T_CH + ((7 - m) & 7) * T_COPY + (m < 8 ? 4048 : 4032) + 16 * g - 512 * 7 - 256 * hf;
    c.bp = smem + OFF_U + ch * U_STRIDE + 448 + 32 * m + 16 * g - 256 * hf;
    return c;
}
__device__ __forceinline__ void conv_preload(u32x4 (&A0)[8], const ConvAddr& c) {
#pragma unroll
    for (int a = 0; a < 8; ++a) A0[a] = *(const u32x4*)(c.ap + 512 * (7 - a));
}
template <bool FLAGS>
__device__ __forceinline__ void conv(char* smem, const u32x4 (&A0)[8], const ConvAddr& c, int wave, int lane, rsrc_t rs, int id, int fill0,
                                     u32 epoch) {
    if (wave < 4) {
        if ((wave & 1) == 0) conv_half<TileSet<1, 3, 4, 6>, TileSet<0, 2, 5, 7>>(smem, A0, c.ap, c.bp, wave, lane, wave >> 1);
        else conv_half<TileSet<0, 2, 5, 7>, TileSet<1, 3, 4, 6>>(smem, A0, c.ap, c.bp, wave, lane, wave >> 1);
    } else {
        if (fill0 >= 0) {
            const u32x4 sent = {SENT, SENT, SENT, SENT};
            const u32 off = WS_H + fill0 * WS_HS + id * 8192 + ((wave - 4) * 64 + lane) * 16;
            st_dev(rs, off, sent); st_dev(rs, off + 4096, sent);
            st_dev(rs, off + WS_HS, sent); st_dev(rs, off + WS_HS + 4096, sent);
        }
        BAR();
        if (FLAGS && wave == 7) {
            const u32 flag_off = WS_FLAGS + 32768 + (blockIdx.x & 31) * 1024 + lane * 16;
            u32x4 fl = poll_issue(rs, flag_off);
            flags_finish(fl, rs, flag_off, epoch);
        }
        if (fill0 >= 0) asm volatile("s_waitcnt vmcnt(0)" ::: "memory");
    }
    BAR();
}

__global__ void __launch_bounds__(512, 2)
h3s4_kernel(const char* __restrict__ q, const char* __restrict__ k, const char* __restrict__ v,
            const u32x2* __restrict__ shiftk, const u32x4* __restrict__ ssmk, const u32* __restrict__ ssmD,
            const u16* __restrict__ Are, const u16* __restrict__ Aim, const u16* __restrict__ Bre, const u16* __restrict__ Bim,
            const u16* __restrict__ Cre, const u16* __restrict__ Cim, const u16* __restrict__ logdt, const u32* __restrict__ Dskip,
            u16* __restrict__ out, char* ws
            ) {
    __shared__ __attribute__((aligned(16))) char smem[LDS_BYTES];
    const int tid = threadIdx.x, lane = tid & 63, wave = __builtin_amdgcn_readfirstlane(tid >> 6);
    const int id = (blockIdx.x & 7) * 32 + (blockIdx.x >> 3);
    const rsrc_t rs = __builtin_amdgcn_make_buffer_rsrc(ws, 0, WS_USED, 0x00020000);
    float* red = (float*)(smem + OFF_RED);
    STAMP(0);

    const u32x4 sent = {SENT, SENT, SENT, SENT};
    const u32 own_off = id * 8192 + tid * 16;
    u32 epoch;
    asm volatile("s_load_dword %0, %1, 0x0" : "=s"(epoch) : "s"(ws + WS_EPOCH));
    const u32 dA = ssmD[id], dB = Dskip[id];
    MEMFENCE(); SB();
    st_dev(rs, WS_H + 0 * WS_HS + own_off, sent);
    MEMFENCE(); SB();
#pragma unroll
    for (int s = 0; s < 4; ++s) {
        const int slot = wave + 8 * s;
        if (slot < 28) {
            int row = 8 * id + (slot < 12 ? slot - 4 : slot < 20 ? slot - 12 : slot - 20);
            row = row < 0 ? 0 : row;
            const char* src = slot < 12 ? k : slot < 20 ? v : q;
            lds_dma16(src + row * 1024 + lane * 16, (u32)(size_t)(LAS char*)(smem + OFF_R) + slot * R_ROW);
        }
    }
    const u32x2 sk = shiftk[tid];
    const int tch = tid >> 8, ti = tid & 255;
    u32x4 tap_own = ssmk[(2 * id + tch) * 256 + ti];
    u32x4 tap_prev = ssmk[(2 * id + tch) * 256 + (ti ? ti - 1 : 0)];
    const int pd = 2 * id + ((tid >> 7) & 1), po = pd * NS + (tid & 127);
    const u16 r_dt = logdt[pd], r_are = Are[po], r_aim = Aim[po], r_bre = Bre[po], r_bim = Bim[po], r_cre = Cre[po], r_cim = Cim[po];
    MEMFENCE(); SB();
    st_dev(rs, WS_H + 1 * WS_HS + own_off, sent);
    MEMFENCE(); SB();
    asm volatile("s_waitcnt vmcnt(13) lgkmcnt(0)\n\ts_barrier" : "+s"(epoch) :: "memory");
    epoch += 1u;
    if (tid < 32) __hip_atomic_store((GAS u32*)(ws + WS_FLAGS) + tid * 256 + id, epoch, RLX_AGENT);
    STAMP(19);
    if (tid < 60) *(u32x4*)(smem + OFF_U + (tid / 30) * U_STRIDE + (tid % 30) * 16) = (u32x4){0, 0, 0, 0};
    else if (tid >= 64 && tid < 80) *(u32x4*)(smem + OFF_T + (tid - 64) * T_COPY + 4096) = (u32x4){0, 0, 0, 0};
    if (wave == 0) WAIT_VM(2); else WAIT_VM(1);
    asm volatile("" :: "v"(sk), "v"(tap_own), "v"(tap_prev), "v"(r_dt), "v"(r_are), "v"(r_aim), "v"(r_bre), "v"(r_bim), "v"(r_cre), "v"(r_cim));
    BAR();
    STAMP(1);

    float x[8];
    u32x4 xq;
    {
        const u32 rp = (u32)(size_t)(LAS char*)(smem + OFF_R + ((lane & 15) >> 2) * R_ROW + (64 * wave + 16 * (lane >> 4) + 4 * (lane & 3)) * 2);
        u32x2 k0, k1, k2, v0, v1, q0, q1;
        static_assert(4 * R_ROW == 4352, "offsets below");
        asm volatile("ds_read_b64_tr_b16 %0, %7\n\tds_read_b64_tr_b16 %1, %7 offset:4352\n\tds_read_b64_tr_b16 %2, %7 offset:8704\n\t"
                     "ds_read_b64_tr_b16 %3, %7 offset:13056\n\tds_read_b64_tr_b16 %4, %7 offset:17408\n\t"
                     "ds_read_b64_tr_b16 %5, %7 offset:21760\n\tds_read_b64_tr_b16 %6, %7 offset:26112\n\ts_waitcnt lgkmcnt(0)"
                     : "=&v"(k0), "=&v"(k1), "=&v"(k2), "=&v"(v0), "=&v"(v1), "=&v"(q0), "=&v"(q1) : "v"(rp) : "memory");
        const u32 first = id ? 0xffffffffu : 0u;
        const u32 kp = k0[0] & first, kq = k0[1] & first;
        const float kf[12] = {0.f, bf_hi(kp), bf_lo(kq), bf_hi(kq), bf_lo(k1[0]), bf_hi(k1[0]),
                              bf_lo(k1[1]), bf_hi(k1[1]), bf_lo(k2[0]), bf_hi(k2[0]), bf_lo(k2[1]), bf_hi(k2[1])};
        float vf[8];
        unpack8((u32x4){v0[0], v0[1], v1[0], v1[1]}, vf);
        const float w0 = bf_lo(sk[0]), w1 = bf_hi(sk[0]), w2 = bf_lo(sk[1]), w3 = bf_hi(sk[1]);
        float kv[8];
#pragma unroll
        for (int j = 0; j < 8; ++j) kv[j] = fmaf(kf[j + 4], w0, fmaf(kf[j + 3], w1, fmaf(kf[j + 2], w2, kf[j + 1] * w3))) * vf[j];
        publish(rs, WS_H + 0 * WS_HS + own_off, pack8(kv));
        xq = (u32x4){q0[0], q0[1], q1[0], q1[1]};
    }
    u64 t_s = __builtin_amdgcn_s_memrealtime();
    STAMP(2);
    u32 touch = 0;
    if (wave == 6) {
        if (lane < 4) touch = __builtin_amdgcn_raw_buffer_load_b32(rs, WS_H + (2 + lane) * WS_HS + id * 8192, 0, 16);
        else if (lane == 4) touch = ((const u32*)out)[id * 2048];
    }

    const u32 flag_off = WS_FLAGS + (blockIdx.x & 31) * 1024 + lane * 16;
    u32x4 fl = {epoch, epoch, epoch, epoch};
    if (wave == 7) fl = poll_issue(rs, flag_off);
    if (ti == 0) {
        tap_prev = (u32x4){0, 0, 0, 0};
        tap_own[0] = (tap_own[0] & 0xffff0000u) | (pk_bf(bf_lo(tap_own[0]) + (tch ? bf_hi(dA) : bf_lo(dA)), 0.f) & 0xffffu);
    }
    stage_taps(smem, tid, tap_prev, tap_own);
    f32x4 prm;
    {
        const float dt = rbf(expf(bf_u16(r_dt)));
        const float are = dt * bf_u16(r_are), aim = dt * bf_u16(r_aim);
        const float br = dt * bf_u16(r_bre), bi = dt * bf_u16(r_bim);
        const float cr = bf_u16(r_cre), ci = bf_u16(r_cim);
        prm = (f32x4){are * 1.4426950408889634f, aim * 0.15915494309189535f, cr * br - ci * bi, cr * bi + ci * br};
        asm volatile("" : "+v"(prm));
    }
    if (wave == 7) {
        flags_finish(fl, rs, flag_off, epoch);
        if (blockIdx.x == 0 && lane == 0) __hip_atomic_store((GAS u32*)(ws + WS_EPOCH), epoch, RLX_AGENT);
    }
    BAR();
    if (tid < 256) ((f32x4*)(smem + OFF_P))[tid] = prm;
    STAMP(3);

    const u32 col_in = (tid >> 1) * 8192 + (2 * id + (tid & 1)) * 16;
    const u32 row_in = (tid >> 1) * 8192 + id * 32 + (tid & 1) * 16;
    char* const u_dst = smem + OFF_U + (tid & 1) * U_STRIDE + 480 + (tid >> 1) * 16;
    const char* const y_src = smem + OFF_Y + (tid & 1) * 4096 + (tid >> 1) * 16;
    const ConvAddr ca = conv_addr(smem, wave, lane);
    u32x4 A0[8];

    wait_until(t_s + POLL_DELAY);
    u32x4 pc = poll_issue(rs, WS_H + 0 * WS_HS + col_in);
    BAR();
    {
        KState ks;
#pragma unroll
        for (int e = 0; e < 4; ++e) ks.acc[e] = (f32x4){0, 0, 0, 0};
        ks.k2048 = 0.f;
        kgen_step(kgen_params(smem, wave), lane, ks.acc, ks.k2048);
        kgen_save(smem, wave, lane, ks);
    }
    STAMP(4);
    if (wave < 4) conv_preload(A0, ca);
    poll_finish(pc, rs, WS_H + 0 * WS_HS + col_in);
    *(u32x4*)u_dst = pc;
    asm volatile("" :: "v"(touch));
    BAR();
    if (wave == 7 && lane < 32) __hip_atomic_store((GAS u32*)(ws + WS_FLAGS + 32768) + lane * 256 + id, epoch, RLX_AGENT);
    STAMP(5);

    conv<true>(smem, A0, ca, wave, lane, rs, id, 2, epoch);
    STAMP(6);
    publish(rs, WS_H + 1 * WS_HS + own_off, *(const u32x4*)y_src);
    t_s = __builtin_amdgcn_s_memrealtime();

    float u[8];
    unpack8(xq, x);
    layer_norm(x, u, red, lane, wave);
    const u32x4 u0q = pack8(u);
    wait_until(t_s + POLL_DELAY);
    u32x4 pr = poll_issue(rs, WS_H + 1 * WS_HS + row_in);
    {
        KState ks;
        kgen_load(smem, wave, lane, ks);
        kgen_step(kgen_params(smem, wave) + 16, lane, ks.acc, ks.k2048);
        kgen_save(smem, wave, lane, ks);
    }
    STAMP(7);

    poll_finish(pr, rs, WS_H + 1 * WS_HS + row_in);
    STAMP(8);
    float yA[8];
    unpack8(pr, yA);
    unpack8(u0q, u);
#pragma unroll
    for (int j = 0; j < 8; ++j) u[j] = fmaf(u[j], yA[j], x[j]);
    unpack8(pack8(u), x);
    layer_norm(x, u, red + 128, lane, wave);
    publish(rs, WS_H + 2 * WS_HS + own_off, pack8(u));
    t_s = __builtin_amdgcn_s_memrealtime();
    STAMP(9);

    {
        kgen_reduce(smem, wave, lane, dB);
        BAR();
        const u16* KC = (const u16*)(smem + OFF_KC);
        u32 d[8];
#pragma unroll
        for (int j = 0; j < 8; ++j) {
            const int l0 = 8 * ti - 8 + 2 * j;
            const u32 bits = pk_bf((float)l0, (float)(l0 + 1));
            const int i0 = l0 < 128 ? (l0 < 0 ? 0 : l0) : (int)(bits & 0xffffu) - 17024;
            const int i1 = l0 < 128 ? (l0 < 0 ? 0 : l0 + 1) : (int)(bits >> 16) - 17024;
            const u32 a = KC[tch * 648 + i0], b = KC[tch * 648 + i1];
            d[j] = l0 < 0 ? 0u : (a | (b << 16));
        }
        wait_until(t_s + POLL_DELAY);
        pc = poll_issue(rs, WS_H + 2 * WS_HS + col_in);
        stage_taps(smem, tid, (u32x4){d[0], d[1], d[2], d[3]}, (u32x4){d[4], d[5], d[6], d[7]});
        BAR();
    }

    if (wave < 4) conv_preload(A0, ca);
    poll_finish(pc, rs, WS_H + 2 * WS_HS + col_in);
    *(u32x4*)u_dst = pc;
    BAR();
    STAMP(10);
    conv<false>(smem, A0, ca, wave, lane, rs, id, 4, epoch);
    STAMP(11);
    publish(rs, WS_H + 3 * WS_HS + own_off, *(const u32x4*)y_src);
    t_s = __builtin_amdgcn_s_memrealtime();

    wait_until(t_s + POLL_DELAY);
    pr = poll_issue(rs, WS_H + 3 * WS_HS + row_in);
    poll_finish(pr, rs, WS_H + 3 * WS_HS + row_in);
    STAMP(12);
    {
        float y[8];
        unpack8(pr, y);
#pragma unroll
        for (int j = 0; j < 8; ++j) y[j] += x[j];
        unpack8(pack8(y), x);
        layer_norm(x, u, red, lane, wave);
#pragma unroll
        for (int j = 0; j < 8; ++j) u[j] = fmaf(u[j], yA[j], x[j]);
        unpack8(pack8(u), x);
        layer_norm(x, u, red + 128, lane, wave);
    }
    publish(rs, WS_H + 4 * WS_HS + own_off, pack8(u));
    t_s = __builtin_amdgcn_s_memrealtime();
    STAMP(13);

    wait_until(t_s + POLL_DELAY);
    pc = poll_issue(rs, WS_H + 4 * WS_HS + col_in);
    if (wave < 4) conv_preload(A0, ca);
    poll_finish(pc, rs, WS_H + 4 * WS_HS + col_in);
    *(u32x4*)u_dst = pc;
    BAR();
    STAMP(14);
    conv<false>(smem, A0, ca, wave, lane, rs, id, -1, epoch);
    STAMP(15);
    publish(rs, WS_H + 5 * WS_HS + own_off, *(const u32x4*)y_src);
    t_s = __builtin_amdgcn_s_memrealtime();

    wait_until(t_s + POLL_DELAY);
    pr = poll_issue(rs, WS_H + 5 * WS_HS + row_in);
    poll_finish(pr, rs, WS_H + 5 * WS_HS + row_in);
    STAMP(16);
    {
        float y[8];
        unpack8(pr, y);
#pragma unroll
        for (int j = 0; j < 8; ++j) y[j] += x[j];
        const u32x4 o = pack8(y);
        u16* dst = out + (8 * id) * DM + tid;
#pragma unroll
        for (int j = 0; j < 4; ++j) {
            dst[(2 * j) * DM] = (u16)o[j];
            dst[(2 * j + 1) * DM] = (u16)(o[j] >> 16);
        }
    }
    STAMP(17);
}

extern "C" void kernel_launch(void* const* d_in, const int* in_sizes, int n_in,
                              void* d_out, int out_size, void* d_ws, size_t ws_size,
                              hipStream_t stream) {
    h3s4_kernel<<<256, 512, 0, stream>>>(
        (const char*)d_in[0], (const char*)d_in[1], (const char*)d_in[2], (const u32x2*)d_in[3], (const u32x4*)d_in[4],
        (const u32*)d_in[5], (const u16*)d_in[7], (const u16*)d_in[8], (const u16*)d_in[9], (const u16*)d_in[10],
        (const u16*)d_in[11], (const u16*)d_in[12], (const u16*)d_in[13], (const u32*)d_in[14], (u16*)d_out, (char*)d_ws
        );
}
```

```cpp
#define DIAG 0
#include <hip/hip_runtime.h>
#include <hip/hip_bf16.h>
#include <stdint.h>

typedef unsigned int u32;
typedef unsigned short u16;
typedef unsigned long long u64;
typedef __attribute__((ext_vector_type(4))) float f32x4;
typedef __attribute__((ext_vector_type(2))) float f32x2;
typedef __attribute__((ext_vector_type(4))) u32 u32x4;
typedef __attribute__((ext_vector_type(2))) u32 u32x2;
typedef __attribute__((ext_vector_type(4))) short s16x4;
typedef __attribute__((ext_vector_type(8))) __bf16 bf16x8;
typedef __attribute__((ext_vector_type(2))) __bf16 bf16x2;
#define LAS __attribute__((address_space(3)))
#define GAS __attribute__((address_space(1)))
#define RLX_AGENT __ATOMIC_RELAXED, __HIP_MEMORY_SCOPE_AGENT
#define SB() __builtin_amdgcn_sched_barrier(0)
#define BAR() asm volatile("s_waitcnt lgkmcnt(0)\n\ts_barrier" ::: "memory")
#define MEMFENCE() asm volatile("" ::: "memory")
#define WAIT_VM(n) do { MEMFENCE(); __builtin_amdgcn_s_waitcnt(0x0F70 | ((n) & 15) | (((n) >> 4) << 14)); MEMFENCE(); } while (0)

#define DIAG_AMP 4
constexpr int DM = 512;
constexpr int NS = 128;
constexpr u32 SENT = 0xffffffffu;
constexpr u32 SPIN_CAP = 20000;
#define POLL_DELAY 50

constexpr u32 WS_EPOCH = 0;
constexpr u32 WS_FLAGS = 4096;
constexpr u32 WS_H = 1u << 20;
constexpr u32 WS_HS = 2u << 20;
constexpr u32 WS_USED = WS_H + 6 * WS_HS;

constexpr int T_COPY = 4160, T_CH = 8 * T_COPY;
constexpr int U_STRIDE = 4672;
constexpr int R_ROW = 1088;
constexpr int OFF_T = 0;
constexpr int OFF_U = OFF_T + 2 * T_CH;
constexpr int OFF_X = OFF_U + 2 * U_STRIDE;
constexpr int OFF_Y = OFF_X + 16384;
constexpr int OFF_R = OFF_Y + 8192;
constexpr int OFF_P = OFF_R;
constexpr int OFF_KP = OFF_R + 4096;
constexpr int KP_WAVE = 2576;
constexpr int OFF_KC = OFF_KP + 8 * KP_WAVE;
constexpr int OFF_RED = OFF_R + 28 * R_ROW;
constexpr int OFF_ST = OFF_RED + 1024;
constexpr int LDS_BYTES = OFF_ST + 256;
static_assert(OFF_KC + 2 * 648 * 2 <= OFF_RED, "table alias");

#define STAMP(i) do { if (threadIdx.x == 0) ((u64*)(smem + OFF_ST))[i] = __builtin_amdgcn_s_memrealtime(); } while (0)

__device__ __forceinline__ float bf_lo(u32 p) { return __builtin_bit_cast(float, p << 16); }
__device__ __forceinline__ float bf_hi(u32 p) { return __builtin_bit_cast(float, p & 0xffff0000u); }
__device__ __forceinline__ float bf_u16(u16 p) { return __builtin_bit_cast(float, (u32)p << 16); }
__device__ __forceinline__ u32 pk_bf(float lo, float hi) {
    bf16x2 v = {(__bf16)lo, (__bf16)hi};
    return __builtin_bit_cast(u32, v);
}
__device__ __forceinline__ float as_f(u32 x) { return __builtin_bit_cast(float, x); }
__device__ __forceinline__ u32 as_u(float x) { return __builtin_bit_cast(u32, x); }
__device__ __forceinline__ float rbf(float x) { return bf_lo(pk_bf(x, x)); }
__device__ __forceinline__ void unpack8(u32x4 p, float (&f)[8]) {
#pragma unroll
    for (int i = 0; i < 4; ++i) { f[2 * i] = bf_lo(p[i]); f[2 * i + 1] = bf_hi(p[i]); }
}
__device__ __forceinline__ u32x4 pack8(const float (&f)[8]) {
    return (u32x4){pk_bf(f[0], f[1]), pk_bf(f[2], f[3]), pk_bf(f[4], f[5]), pk_bf(f[6], f[7])};
}
template <int CTRL> __device__ __forceinline__ float dppf(float v) {
    return __builtin_bit_cast(float, __builtin_amdgcn_update_dpp(0, __builtin_bit_cast(int, v), CTRL, 0xf, 0xf, true));
}

__device__ __forceinline__ void lds_dma16(const char* src, u32 lds) {
    u32 keep;
    asm volatile("s_mov_b32 %0, m0\n\ts_mov_b32 m0, %2\n\ts_nop 0\n\tglobal_load_lds_dwordx4 %1, off\n\ts_mov_b32 m0, %0"
                 : "=&s"(keep) : "v"(src), "s"(lds) : "memory");
}

typedef __amdgpu_buffer_rsrc_t rsrc_t;
__device__ __forceinline__ u32x4 ld_dev(rsrc_t rs, u32 off) { return __builtin_amdgcn_raw_buffer_load_b128(rs, off, 0, 16); }
__device__ __forceinline__ void st_dev(rsrc_t rs, u32 off, u32x4 v) { __builtin_amdgcn_raw_buffer_store_b128(v, rs, off, 0, 16); }
__device__ __forceinline__ void publish(rsrc_t rs, u32 off, u32x4 v) {
#pragma unroll
    for (int i = 0; i < 4; ++i) v[i] = v[i] < SENT - 1u ? v[i] : SENT - 1u;
    st_dev(rs, off, v);
}
__device__ __forceinline__ void wait_until(u64 t) {
    while (__builtin_amdgcn_s_memrealtime() < t) __builtin_amdgcn_s_sleep(1);
}
__device__ __forceinline__ u32x4 poll_issue(rsrc_t rs, u32 off) {
    MEMFENCE();
    u32x4 x = ld_dev(rs, off);
    MEMFENCE();
    return x;
}
__device__ __forceinline__ void poll_finish(u32x4& x, rsrc_t rs, u32 off) {
    u32 spins = 0;
    while (true) {
        u32 mx = x[0] > x[1] ? x[0] : x[1];
        mx = mx > x[2] ? mx : x[2];
        mx = mx > x[3] ? mx : x[3];
        const bool bad = mx == SENT;
        if (__builtin_amdgcn_ballot_w64(bad) == 0ull) break;
        if (++spins > SPIN_CAP) break;
        __builtin_amdgcn_s_sleep(1);
        MEMFENCE();
        if (bad) x = ld_dev(rs, off);
        MEMFENCE();
    }
}

__device__ __forceinline__ void flags_finish(u32x4& fl, rsrc_t rs, u32 off, u32 epoch) {
    u32 spins = 0;
    while (__builtin_amdgcn_ballot_w64(fl[0] != epoch || fl[1] != epoch || fl[2] != epoch || fl[3] != epoch) != 0ull) {
        if (++spins > SPIN_CAP) break;
        __builtin_amdgcn_s_sleep(2);
        fl = poll_issue(rs, off);
    }
}

__device__ __forceinline__ void row_stats(const float (&x)[8], float* red, int lane, int wave, float& mean, float& rstd) {
    u32 a[8], b[8];
#pragma unroll
    for (int j = 0; j < 8; ++j) { a[j] = as_u(x[j]); b[j] = as_u(x[j] * x[j]); asm volatile("" : "+v"(a[j]), "+v"(b[j])); }
    SB();
    float r[8];
#pragma unroll
    for (int j = 0; j < 8; ++j) {
        const auto s = __builtin_amdgcn_permlane32_swap(a[j], b[j], false, false);
        a[j] = s[0]; b[j] = s[1];
    }
    SB();
#pragma unroll
    for (int j = 0; j < 8; ++j) r[j] = as_f(a[j]) + as_f(b[j]);
    SB();
    float s4[4];
#pragma unroll
    for (int j = 0; j < 4; ++j) {
        const auto s = __builtin_amdgcn_permlane16_swap(as_u(r[j]), as_u(r[j + 4]), false, false);
        a[j] = s[0]; b[j] = s[1];
    }
    SB();
#pragma unroll
    for (int j = 0; j < 4; ++j) s4[j] = as_f(a[j]) + as_f(b[j]);
    SB();
#pragma unroll
    for (int j = 0; j < 4; ++j) s4[j] += dppf<0x128>(s4[j]);
    const float t0 = (lane & 8) ? s4[2] : s4[0], t1 = (lane & 8) ? s4[3] : s4[1];
    const float c0 = t0 + dppf<0x141>(t0), c1 = t1 + dppf<0x141>(t1);
    float w = (lane & 4) ? c1 : c0;
    w += dppf<0xB1>(w);
    w += dppf<0x4E>(w);
    if ((lane & 3) == 0) red[(lane >> 2) * 8 + wave] = w;
    BAR();
    const f32x4 p0 = *(const f32x4*)(red + (lane & 15) * 8), p1 = *(const f32x4*)(red + (lane & 15) * 8 + 4);
    const f32x4 p = p0 + p1;
    const float m = ((p[0] + p[1]) + (p[2] + p[3])) * (1.0f / DM);
    const float ex2 = dppf<0x128>(m);
    const float var = fmaxf(fmaf(-m, m, ex2), 0.f);
    const float sd = rbf(__builtin_amdgcn_sqrtf(rbf(rbf(var) + 1e-5f)));
    mean = rbf(m);
    rstd = __builtin_amdgcn_rcpf(sd);
}
__device__ __forceinline__ void layer_norm(const float (&x)[8], float (&u)[8], float* red, int lane, int wave) {
    float mean, rstd;
    row_stats(x, red, lane, wave, mean, rstd);
    asm volatile("s_nop 1\n\t"
                 "v_subrev_f32_dpp %0, %8, %10 row_newbcast:0 row_mask:0xf bank_mask:0xf\n\t"
                 "v_subrev_f32_dpp %1, %8, %11 row_newbcast:1 row_mask:0xf bank_mask:0xf\n\t"
                 "v_subrev_f32_dpp %2, %8, %12 row_newbcast:2 row_mask:0xf bank_mask:0xf\n\t"
                 "v_subrev_f32_dpp %3, %8, %13 row_newbcast:3 row_mask:0xf bank_mask:0xf\n\t"
                 "v_subrev_f32_dpp %4, %8, %14 row_newbcast:4 row_mask:0xf bank_mask:0xf\n\t"
                 "v_subrev_f32_dpp %5, %8, %15 row_newbcast:5 row_mask:0xf bank_mask:0xf\n\t"
                 "v_subrev_f32_dpp %6, %8, %16 row_newbcast:6 row_mask:0xf bank_mask:0xf\n\t"
                 "v_subrev_f32_dpp %7, %8, %17 row_newbcast:7 row_mask:0xf bank_mask:0xf\n\t"
                 "v_mul_f32_dpp %0, %9, %0 row_newbcast:0 row_mask:0xf bank_mask:0xf\n\t"
                 "v_mul_f32_dpp %1, %9, %1 row_newbcast:1 row_mask:0xf bank_mask:0xf\n\t"
                 "v_mul_f32_dpp %2, %9, %2 row_newbcast:2 row_mask:0xf bank_mask:0xf\n\t"
                 "v_mul_f32_dpp %3, %9, %3 row_newbcast:3 row_mask:0xf bank_mask:0xf\n\t"
                 "v_mul_f32_dpp %4, %9, %4 row_newbcast:4 row_mask:0xf bank_mask:0xf\n\t"
                 "v_mul_f32_dpp %5, %9, %5 row_newbcast:5 row_mask:0xf bank_mask:0xf\n\t"
                 "v_mul_f32_dpp %6, %9, %6 row_newbcast:6 row_mask:0xf bank_mask:0xf\n\t"
                 "v_mul_f32_dpp %7, %9, %7 row_newbcast:7 row_mask:0xf bank_mask:0xf"
                 : "=&v"(u[0]), "=&v"(u[1]), "=&v"(u[2]), "=&v"(u[3]), "=&v"(u[4]), "=&v"(u[5]), "=&v"(u[6]), "=&v"(u[7])
                 : "v"(mean), "v"(rstd), "v"(x[0]), "v"(x[1]), "v"(x[2]), "v"(x[3]), "v"(x[4]), "v"(x[5]), "v"(x[6]), "v"(x[7]));
}

__device__ __forceinline__ void stage_taps(char* smem, int tid, u32x4 prev, u32x4 own) {
    const u32 D[8] = {prev[0], prev[1], prev[2], prev[3], own[0], own[1], own[2], own[3]};
    u32 E[8], O[8];
#pragma unroll
    for (int y = 1; y < 8; ++y) {
        E[y] = __builtin_amdgcn_alignbit(D[y], D[y], 16);
        O[y] = (D[y] & 0xffffu) | (D[y - 1] & 0xffff0000u);
    }
    char* base = smem + OFF_T + (tid >> 8) * T_CH + (255 - (tid & 255)) * 16;
#pragma unroll
    for (int s = 0; s < 8; ++s) {
        const int y = 7 - (s >> 1);
        const u32x4 v = (s & 1) ? (u32x4){O[y], O[y - 1], O[y - 2], O[y - 3]} : (u32x4){E[y], E[y - 1], E[y - 2], E[y - 3]};
        *(u32x4*)(base + s * T_COPY) = v;
    }
}

struct cplx2 { f32x2 r, i; };
__device__ __forceinline__ cplx2 csq(cplx2 a) { return {a.r * a.r - a.i * a.i, (a.r + a.r) * a.i}; }
__device__ __forceinline__ cplx2 cmul(cplx2 a, cplx2 b) { return {a.r * b.r - a.i * b.i, a.r * b.i + a.i * b.r}; }
__device__ __forceinline__ f32x4 mfma16(u32x4 a, u32x4 b, f32x4 c) {
    return __builtin_amdgcn_mfma_f32_16x16x32_bf16(__builtin_bit_cast(bf16x8, a), __builtin_bit_cast(bf16x8, b), c, 0, 0, 0);
}
__device__ __forceinline__ void kgen_step(const f32x4* P, int lane, f32x4 (&acc)[4], float& k2048) {
#pragma clang fp contract(fast)
    const int g = lane >> 4;
    const float pm = (float)(lane & 15);
    cplx2 b[2], W[2];
#pragma unroll
    for (int h = 0; h < 2; ++h) {
        const f32x4 p0 = P[4 * g + 2 * h], p1 = P[4 * g + 2 * h + 1];
        const f32x2 ex = (f32x2){p0[0], p1[0]} * pm, an = (f32x2){p0[1], p1[1]} * pm;
        const f32x2 mag = {__builtin_amdgcn_exp2f(ex[0]), __builtin_amdgcn_exp2f(ex[1])};
        const float f0 = __builtin_amdgcn_fractf(an[0]), f1 = __builtin_amdgcn_fractf(an[1]);
        b[h].r = mag * (f32x2){__builtin_amdgcn_cosf(f0), __builtin_amdgcn_cosf(f1)};
        b[h].i = mag * (f32x2){__builtin_amdgcn_sinf(f0), __builtin_amdgcn_sinf(f1)};
        W[h].r = (f32x2){p0[2], p1[2]};
        W[h].i = (f32x2){p0[3], p1[3]};
    }
    u32x4 A[4], B[4];
#pragma unroll
    for (int e = 0; e < 4; ++e) {
#pragma unroll
        for (int h = 0; h < 2; ++h) {
            B[e][2 * h] = pk_bf(b[h].r[0], -b[h].i[0]);
            B[e][2 * h + 1] = pk_bf(b[h].r[1], -b[h].i[1]);
            b[h] = csq(b[h]);
        }
    }
#pragma unroll
    for (int e = 0; e < 4; ++e) {
#pragma unroll
        for (int h = 0; h < 2; ++h) {
            const cplx2 x = cmul(W[h], b[h]);
            A[e][2 * h] = pk_bf(x.r[0], x.i[0]);
            A[e][2 * h + 1] = pk_bf(x.r[1], x.i[1]);
            if (e == 3) {
                const f32x2 z = x.r * b[h].r - x.i * b[h].i;
                k2048 += z[0] + z[1];
            } else b[h] = csq(b[h]);
        }
        acc[e] = mfma16(A[e], B[e], acc[e]);
    }
}

struct KState { f32x4 acc[4]; float k2048; };
__device__ __forceinline__ const f32x4* kgen_params(char* smem, int wave) {
    return (const f32x4*)(smem + OFF_P) + (wave >> 2) * 128 + (wave & 3) * 32;
}
__device__ __forceinline__ void kgen_save(char* smem, int wave, int lane, const KState& ks) {
    char* p = smem + OFF_KP + wave * KP_WAVE;
    *(f32x4*)(p + lane * 16) = ks.acc[0];
    if (lane >= 32) {
#pragma unroll
        for (int e = 1; e < 4; ++e) *(f32x4*)(p + 512 * e + lane * 16) = ks.acc[e];
    }
    const auto s = __builtin_amdgcn_permlane32_swap(as_u(ks.k2048), as_u(ks.k2048), false, false);
    const float h = as_f(s[0]) + as_f(s[1]);
    const auto t = __builtin_amdgcn_permlane16_swap(as_u(h), as_u(h), false, false);
    const float z = as_f(t[0]) + as_f(t[1]);
    if (lane == 8) *(float*)(p + 2560) = z;
}
__device__ __forceinline__ void kgen_load(const char* smem, int wave, int lane, KState& ks) {
    const char* p = smem + OFF_KP + wave * KP_WAVE;
    ks.acc[0] = *(const f32x4*)(p + lane * 16);
#pragma unroll
    for (int e = 1; e < 4; ++e) ks.acc[e] = *(const f32x4*)(p + 512 * e + (lane | 32) * 16);
    ks.k2048 = lane == 8 ? *(const float*)(p + 2560) : 0.f;
}
__device__ __forceinline__ void kgen_reduce(char* smem, int wave, int lane, u32 dskip) {
    const int c = wave >> 2, e = wave & 3, m = lane & 15, g = lane >> 4;
    const char* p = smem + OFF_KP + c * 4 * KP_WAVE + (e ? 512 * e + (lane | 32) * 16 : lane * 16);
    f32x4 s = (*(const f32x4*)p + *(const f32x4*)(p + KP_WAVE)) + (*(const f32x4*)(p + 2 * KP_WAVE) + *(const f32x4*)(p + 3 * KP_WAVE));
    u16* KC = (u16*)(smem + OFF_KC) + c * 648;
    if (e == 0 && lane == 0) s[0] += c ? bf_hi(dskip) : bf_lo(dskip);
    const u32 lo = pk_bf(s[0], s[1]), hi = pk_bf(s[2], s[3]);
    if (e == 0 || g >= 2) {
        u16* o = KC + 128 * e + 64 * g + m;
        o[0] = (u16)lo; o[16] = (u16)(lo >> 16); o[32] = (u16)hi; o[48] = (u16)(hi >> 16);
    }
    if (e == 3 && lane == 0) {
        const char* z = smem + OFF_KP + c * 4 * KP_WAVE + 2560;
        const float k = (*(const float*)z + *(const float*)(z + KP_WAVE)) + (*(const float*)(z + 2 * KP_WAVE) + *(const float*)(z + 3 * KP_WAVE));
        KC[640] = (u16)pk_bf(k, 0.f);
    }
}

struct Frags { u32x4 A[8], B[8]; };
template <bool INIT> __device__ __forceinline__ void mma(f32x4& acc, const u32x4& a, const u32x4& b) {
    if constexpr (INIT) asm volatile("v_mfma_f32_16x16x32_bf16 %0, %1, %2, 0" : "=&v"(acc) : "v"(a), "v"(b));
    else asm volatile("v_mfma_f32_16x16x32_bf16 %0, %1, %2, %0" : "+v"(acc) : "v"(a), "v"(b));
}
template <int I> __device__ __forceinline__ void load_one(Frags& f, const char* ap, const char* bp) {
    if constexpr (I == 0) f.A[0] = *(const u32x4*)(ap + 512 * 7);
    else if constexpr (I <= 8) f.B[I - 1] = *(const u32x4*)(bp + 512 * (I - 1));
    else f.A[I - 8] = *(const u32x4*)(ap + 512 * (7 - (I - 8)));
}
template <int I> __device__ __forceinline__ void load_all(Frags& f, const char* ap, const char* bp) {
    if constexpr (I < 16) { load_one<I>(f, ap, bp); SB(); load_all<I + 1>(f, ap, bp); }
}
template <int T0, int T1, int T2, int T3> struct TileSet {
    static constexpr int tile(int i) { return i == 0 ? T0 : i == 1 ? T1 : i == 2 ? T2 : T3; }
    static constexpr int walk(int I, bool want_tile) {
        int n = 0;
        for (int a = 0; a < 8; ++a)
            for (int j = 0; j < 4; ++j)
                if (tile(j) >= a) { if (n == I) return want_tile ? tile(j) : a; ++n; }
        return -1;
    }
};
constexpr int tri_a(int I) { int a = 0; while (I >= 8 - a) { I -= 8 - a; ++a; } return a; }
constexpr int tri_b(int I) { int a = 0; while (I >= 8 - a) { I -= 8 - a; ++a; } return I; }
template <int I, bool FIRST> __device__ __forceinline__ void class_step(const Frags& cur, Frags& nxt, f32x4 (&acc)[8], const char* ap, const char* bp) {
    if constexpr (I < 36) {
        constexpr int a = tri_a(I), b = tri_b(I);
        mma<FIRST && a == 0>(acc[a + b], cur.A[a], cur.B[b]);
        if constexpr ((I & 1) == 1 && (I >> 1) < 16) load_one<(I >> 1)>(nxt, ap, bp);
        SB();
        class_step<I + 1, FIRST>(cur, nxt, acc, ap, bp);
    }
}
template <class TS, int I, int TO, bool PRE> __device__ __forceinline__ void set_step(const Frags& cur, Frags& nxt, f32x4 (&acc)[8], const char* ap, const char* bp) {
    if constexpr (I < TO) {
        constexpr int t = TS::walk(I, true), a = TS::walk(I, false);
        mma<false>(acc[t], cur.A[a], cur.B[t - a]);
        if constexpr (PRE && I < 16) load_one<I>(nxt, ap, bp);
        SB();
        set_step<TS, I + 1, TO, PRE>(cur, nxt, acc, ap, bp);
    }
}
template <class TS, int J> __device__ __forceinline__ void tile_step(const Frags& cur, f32x4 (&acc)[8]) {
    constexpr int t = TS::tile(J);
#pragma unroll
    for (int a = 0; a <= t; ++a) { mma<false>(acc[t], cur.A[a], cur.B[t - a]); SB(); }
}
template <class TS, int J> __device__ __forceinline__ void tile_out(char* Y, f32x4 (&acc)[8], const f32x4 (&o)[4]) {
    constexpr int t = TS::tile(J);
    const f32x4 s = acc[t] + o[J];
    const u32x2 pk = {pk_bf(s[0], s[1]), pk_bf(s[2], s[3])};
    *(u32x2*)(Y + 512 * t) = pk;
    SB();
}
template <class SEND, class KEEP>
__device__ __forceinline__ void conv_half(char* smem, const u32x4 (&A0)[8], const char* ap, const char* bp, int wave, int lane, int ch) {
    f32x4 acc[8];
    Frags f0, f1;
#pragma unroll
    for (int a = 0; a < 8; ++a) f0.A[a] = A0[a];
#pragma unroll
    for (int b = 0; b < 8; ++b) { f0.B[b] = *(const u32x4*)(bp + 512 * b); SB(); }
    class_step<0, true>(f0, f1, acc, ap - 64, bp - 64);
    class_step<0, false>(f1, f0, acc, ap - 128, bp - 128);
    set_step<SEND, 0, 18, true>(f0, f1, acc, ap - 192, bp - 192);
    set_step<SEND, 0, 18, false>(f1, f0, acc, ap, bp);
    set_step<KEEP, 0, 4, false>(f0, f1, acc, ap, bp);
    asm volatile("s_nop 3" : "+v"(acc[SEND::tile(0)]), "+v"(acc[SEND::tile(1)]), "+v"(acc[SEND::tile(2)]), "+v"(acc[SEND::tile(3)]));
    f32x4* X = (f32x4*)(smem + OFF_X);
#pragma unroll
    for (int i = 0; i < 4; ++i) X[(wave * 4 + i) * 64 + lane] = acc[SEND::tile(i)];
    SB();
    set_step<KEEP, 4, 18, false>(f0, f1, acc, ap, bp);
    BAR();
    SB();
    f32x4 o[4];
#pragma unroll
    for (int i = 0; i < 4; ++i) o[i] = X[((wave ^ 1) * 4 + i) * 64 + lane];
    SB();
    char* Y = smem + OFF_Y + ch * 4096 + (16 * (lane & 15) + 4 * (lane >> 4)) * 2;
    tile_step<KEEP, 0>(f1, acc);
    tile_step<KEEP, 1>(f1, acc);
    tile_step<KEEP, 2>(f1, acc);
    asm volatile("s_nop 3" : "+v"(acc[KEEP::tile(0)]), "+v"(acc[KEEP::tile(1)]));
    tile_out<KEEP, 0>(Y, acc, o);
    tile_out<KEEP, 1>(Y, acc, o);
    tile_step<KEEP, 3>(f1, acc);
    asm volatile("" : "+v"(acc[KEEP::tile(2)]));
    tile_out<KEEP, 2>(Y, acc, o);
    asm volatile("s_nop 7" : "+v"(acc[KEEP::tile(3)]));
    tile_out<KEEP, 3>(Y, acc, o);
}
struct ConvAddr { const char* ap; const char* bp; };
__device__ __forceinline__ ConvAddr conv_addr(char* smem, int wave, int lane) {
    const int ch = (wave >> 1) & 1, hf = wave & 1;
    const int m = lane & 15, g = lane >> 4;
    ConvAddr c;
    c.ap = smem + OFF_T + ch * T_CH + ((7 - m) & 7) * T_COPY + (m < 8 ? 4048 : 4032) + 16 * g - 512 * 7 - 256 * hf;
    c.bp = smem + OFF_U + ch * U_STRIDE + 448 + 32 * m + 16 * g - 256 * hf;
    return c;
}
__device__ __forceinline__ void conv_preload(u32x4 (&A0)[8], const ConvAddr& c) {
#pragma unroll
    for (int a = 0; a < 8; ++a) A0[a] = *(const u32x4*)(c.ap + 512 * (7 - a));
}
template <bool FLAGS>
__device__ __forceinline__ void conv(char* smem, const u32x4 (&A0)[8], const ConvAddr& c, int wave, int lane, rsrc_t rs, int id, int fill0,
                                     u32 epoch) {
    if (wave < 4) {
        if ((wave & 1) == 0) conv_half<TileSet<1, 3, 4, 6>, TileSet<0, 2, 5, 7>>(smem, A0, c.ap, c.bp, wave, lane, wave >> 1);
        else conv_half<TileSet<0, 2, 5, 7>, TileSet<1, 3, 4, 6>>(smem, A0, c.ap, c.bp, wave, lane, wave >> 1);
    } else {
        if (fill0 >= 0) {
            const u32x4 sent = {SENT, SENT, SENT, SENT};
            const u32 off = WS_H + fill0 * WS_HS + id * 8192 + ((wave - 4) * 64 + lane) * 16;
            st_dev(rs, off, sent); st_dev(rs, off + 4096, sent);
            st_dev(rs, off + WS_HS, sent); st_dev(rs, off + WS_HS + 4096, sent);
        }
        BAR();
        if (FLAGS && wave == 7) {
            const u32 flag_off = WS_FLAGS + 32768 + (blockIdx.x & 31) * 1024 + lane * 16;
            u32x4 fl = poll_issue(rs, flag_off);
            flags_finish(fl, rs, flag_off, epoch);
        }
        if (fill0 >= 0) asm volatile("s_waitcnt vmcnt(0)" ::: "memory");
    }
    BAR();
}

__global__ void __launch_bounds__(512, 2)
h3s4_kernel(const char* __restrict__ q, const char* __restrict__ k, const char* __restrict__ v,
            const u32x2* __restrict__ shiftk, const u32x4* __restrict__ ssmk, const u32* __restrict__ ssmD,
            const u16* __restrict__ Are, const u16* __restrict__ Aim, const u16* __restrict__ Bre, const u16* __restrict__ Bim,
            const u16* __restrict__ Cre, const u16* __restrict__ Cim, const u16* __restrict__ logdt, const u32* __restrict__ Dskip,
            u16* __restrict__ out, char* ws
            ) {
    __shared__ __attribute__((aligned(16))) char smem[LDS_BYTES];
    const int tid = threadIdx.x, lane = tid & 63, wave = __builtin_amdgcn_readfirstlane(tid >> 6);
    const int id = (blockIdx.x & 7) * 32 + (blockIdx.x >> 3);
    const rsrc_t rs = __builtin_amdgcn_make_buffer_rsrc(ws, 0, WS_USED, 0x00020000);
    float* red = (float*)(smem + OFF_RED);
    STAMP(0);
    const u64 clk0 = __builtin_amdgcn_s_memtime();

    const u32x4 sent = {SENT, SENT, SENT, SENT};
    const u32 own_off = id * 8192 + tid * 16;
    u32 epoch;
    asm volatile("s_load_dword %0, %1, 0x0" : "=s"(epoch) : "s"(ws + WS_EPOCH));
    const u32 dA = ssmD[id], dB = Dskip[id];
    MEMFENCE(); SB();
    st_dev(rs, WS_H + 0 * WS_HS + own_off, sent);
    MEMFENCE(); SB();
#pragma unroll
    for (int s = 0; s < 4; ++s) {
        const int slot = wave + 8 * s;
        if (slot < 28) {
            int row = 8 * id + (slot < 12 ? slot - 4 : slot < 20 ? slot - 12 : slot - 20);
            row = row < 0 ? 0 : row;
            const char* src = slot < 12 ? k : slot < 20 ? v : q;
            lds_dma16(src + row * 1024 + lane * 16, (u32)(size_t)(LAS char*)(smem + OFF_R) + slot * R_ROW);
        }
    }
    const u32x2 sk = shiftk[tid];
    const int tch = tid >> 8, ti = tid & 255;
    u32x4 tap_own = ssmk[(2 * id + tch) * 256 + ti];
    u32x4 tap_prev = ssmk[(2 * id + tch) * 256 + (ti ? ti - 1 : 0)];
    const int pd = 2 * id + ((tid >> 7) & 1), po = pd * NS + (tid & 127);
    const u16 r_dt = logdt[pd], r_are = Are[po], r_aim = Aim[po], r_bre = Bre[po], r_bim = Bim[po], r_cre = Cre[po], r_cim = Cim[po];
    MEMFENCE(); SB();
    st_dev(rs, WS_H + 1 * WS_HS + own_off, sent);
    MEMFENCE(); SB();
    asm volatile("s_waitcnt vmcnt(13) lgkmcnt(0)\n\ts_barrier" : "+s"(epoch) :: "memory");
    epoch += 1u;
    if (tid < 32) __hip_atomic_store((GAS u32*)(ws + WS_FLAGS) + tid * 256 + id, epoch, RLX_AGENT);
    STAMP(19);
    if (tid < 60) *(u32x4*)(smem + OFF_U + (tid / 30) * U_STRIDE + (tid % 30) * 16) = (u32x4){0, 0, 0, 0};
    else if (tid >= 64 && tid < 80) *(u32x4*)(smem + OFF_T + (tid - 64) * T_COPY + 4096) = (u32x4){0, 0, 0, 0};
    if (wave == 0) WAIT_VM(2); else WAIT_VM(1);
    asm volatile("" :: "v"(sk), "v"(tap_own), "v"(tap_prev), "v"(r_dt), "v"(r_are), "v"(r_aim), "v"(r_bre), "v"(r_bim), "v"(r_cre), "v"(r_cim));
    BAR();
    STAMP(1);

    float x[8];
    u32x4 xq;
    {
        const u32 rp = (u32)(size_t)(LAS char*)(smem + OFF_R + ((lane & 15) >> 2) * R_ROW + (64 * wave + 16 * (lane >> 4) + 4 * (lane & 3)) * 2);
        u32x2 k0, k1, k2, v0, v1, q0, q1;
        static_assert(4 * R_ROW == 4352, "offsets below");
        asm volatile("ds_read_b64_tr_b16 %0, %7\n\tds_read_b64_tr_b16 %1, %7 offset:4352\n\tds_read_b64_tr_b16 %2, %7 offset:8704\n\t"
                     "ds_read_b64_tr_b16 %3, %7 offset:13056\n\tds_read_b64_tr_b16 %4, %7 offset:17408\n\t"
                     "ds_read_b64_tr_b16 %5, %7 offset:21760\n\tds_read_b64_tr_b16 %6, %7 offset:26112\n\ts_waitcnt lgkmcnt(0)"
                     : "=&v"(k0), "=&v"(k1), "=&v"(k2), "=&v"(v0), "=&v"(v1), "=&v"(q0), "=&v"(q1) : "v"(rp) : "memory");
        const u32 first = id ? 0xffffffffu : 0u;
        const u32 kp = k0[0] & first, kq = k0[1] & first;
        const float kf[12] = {0.f, bf_hi(kp), bf_lo(kq), bf_hi(kq), bf_lo(k1[0]), bf_hi(k1[0]),
                              bf_lo(k1[1]), bf_hi(k1[1]), bf_lo(k2[0]), bf_hi(k2[0]), bf_lo(k2[1]), bf_hi(k2[1])};
        float vf[8];
        unpack8((u32x4){v0[0], v0[1], v1[0], v1[1]}, vf);
        const float w0 = bf_lo(sk[0]), w1 = bf_hi(sk[0]), w2 = bf_lo(sk[1]), w3 = bf_hi(sk[1]);
        float kv[8];
#pragma unroll
        for (int j = 0; j < 8; ++j) kv[j] = fmaf(kf[j + 4], w0, fmaf(kf[j + 3], w1, fmaf(kf[j + 2], w2, kf[j + 1] * w3))) * vf[j];
        publish(rs, WS_H + 0 * WS_HS + own_off, pack8(kv));
        xq = (u32x4){q0[0], q0[1], q1[0], q1[1]};
    }
    u64 t_s = __builtin_amdgcn_s_memrealtime();
    STAMP(2);
    u32 touch = 0;
    if (wave == 6) {
        if (lane < 4) touch = __builtin_amdgcn_raw_buffer_load_b32(rs, WS_H + (2 + lane) * WS_HS + id * 8192, 0, 16);
        else if (lane == 4) touch = ((const u32*)out)[id * 2048];
    }

    const u32 flag_off = WS_FLAGS + (blockIdx.x & 31) * 1024 + lane * 16;
    u32x4 fl = {epoch, epoch, epoch, epoch};
    if (wave == 7) fl = poll_issue(rs, flag_off);
    if (ti == 0) {
        tap_prev = (u32x4){0, 0, 0, 0};
        tap_own[0] = (tap_own[0] & 0xffff0000u) | (pk_bf(bf_lo(tap_own[0]) + (tch ? bf_hi(dA) : bf_lo(dA)), 0.f) & 0xffffu);
    }
    stage_taps(smem, tid, tap_prev, tap_own);
    f32x4 prm;
    {
        const float dt = rbf(expf(bf_u16(r_dt)));
        const float are = dt * bf_u16(r_are), aim = dt * bf_u16(r_aim);
        const float br = dt * bf_u16(r_bre), bi = dt * bf_u16(r_bim);
        const float cr = bf_u16(r_cre), ci = bf_u16(r_cim);
        prm = (f32x4){are * 1.4426950408889634f, aim * 0.15915494309189535f, cr * br - ci * bi, cr * bi + ci * br};
        asm volatile("" : "+v"(prm));
    }
    if (wave == 7) {
        flags_finish(fl, rs, flag_off, epoch);
        if (blockIdx.x == 0 && lane == 0) __hip_atomic_store((GAS u32*)(ws + WS_EPOCH), epoch, RLX_AGENT);
    }
    BAR();
    if (tid < 256) ((f32x4*)(smem + OFF_P))[tid] = prm;
    STAMP(3);

    const u32 col_in = (tid >> 1) * 8192 + (2 * id + (tid & 1)) * 16;
    const u32 row_in = (tid >> 1) * 8192 + id * 32 + (tid & 1) * 16;
    char* const u_dst = smem + OFF_U + (tid & 1) * U_STRIDE + 480 + (tid >> 1) * 16;
    const char* const y_src = smem + OFF_Y + (tid & 1) * 4096 + (tid >> 1) * 16;
    const ConvAddr ca = conv_addr(smem, wave, lane);
    u32x4 A0[8];

    wait_until(t_s + POLL_DELAY);
    u32x4 pc = poll_issue(rs, WS_H + 0 * WS_HS + col_in);
    BAR();
    {
        KState ks;
#pragma unroll
        for (int e = 0; e < 4; ++e) ks.acc[e] = (f32x4){0, 0, 0, 0};
        ks.k2048 = 0.f;
        kgen_step(kgen_params(smem, wave), lane, ks.acc, ks.k2048);
        kgen_save(smem, wave, lane, ks);
    }
    STAMP(4);
    if (wave < 4) conv_preload(A0, ca);
    poll_finish(pc, rs, WS_H + 0 * WS_HS + col_in);
    *(u32x4*)u_dst = pc;
    asm volatile("" :: "v"(touch));
    BAR();
    if (wave == 7 && lane < 32) __hip_atomic_store((GAS u32*)(ws + WS_FLAGS + 32768) + lane * 256 + id, epoch, RLX_AGENT);
    STAMP(5);

    conv<true>(smem, A0, ca, wave, lane, rs, id, 2, epoch);
    STAMP(6);
    publish(rs, WS_H + 1 * WS_HS + own_off, *(const u32x4*)y_src);
    t_s = __builtin_amdgcn_s_memrealtime();

    float u[8];
    unpack8(xq, x);
    layer_norm(x, u, red, lane, wave);
    const u32x4 u0q = pack8(u);
    wait_until(t_s + POLL_DELAY);
    u32x4 pr = poll_issue(rs, WS_H + 1 * WS_HS + row_in);
    {
        KState ks;
        kgen_load(smem, wave, lane, ks);
        kgen_step(kgen_params(smem, wave) + 16, lane, ks.acc, ks.k2048);
        kgen_save(smem, wave, lane, ks);
    }
    STAMP(7);

    poll_finish(pr, rs, WS_H + 1 * WS_HS + row_in);
    STAMP(8);
    float yA[8];
    unpack8(pr, yA);
    unpack8(u0q, u);
#pragma unroll
    for (int j = 0; j < 8; ++j) u[j] = fmaf(u[j], yA[j], x[j]);
    unpack8(pack8(u), x);
    layer_norm(x, u, red + 128, lane, wave);
    publish(rs, WS_H + 2 * WS_HS + own_off, pack8(u));
    t_s = __builtin_amdgcn_s_memrealtime();
    STAMP(9);

    {
        kgen_reduce(smem, wave, lane, dB);
        BAR();
        const u16* KC = (const u16*)(smem + OFF_KC);
        u32 d[8];
#pragma unroll
        for (int j = 0; j < 8; ++j) {
            const int l0 = 8 * ti - 8 + 2 * j;
            const u32 bits = pk_bf((float)l0, (float)(l0 + 1));
            const int i0 = l0 < 128 ? (l0 < 0 ? 0 : l0) : (int)(bits & 0xffffu) - 17024;
            const int i1 = l0 < 128 ? (l0 < 0 ? 0 : l0 + 1) : (int)(bits >> 16) - 17024;
            const u32 a = KC[tch * 648 + i0], b = KC[tch * 648 + i1];
            d[j] = l0 < 0 ? 0u : (a | (b << 16));
        }
        wait_until(t_s + POLL_DELAY);
        pc = poll_issue(rs, WS_H + 2 * WS_HS + col_in);
        stage_taps(smem, tid, (u32x4){d[0], d[1], d[2], d[3]}, (u32x4){d[4], d[5], d[6], d[7]});
        BAR();
    }

    if (wave < 4) conv_preload(A0, ca);
    poll_finish(pc, rs, WS_H + 2 * WS_HS + col_in);
    *(u32x4*)u_dst = pc;
    BAR();
    STAMP(10);
    conv<false>(smem, A0, ca, wave, lane, rs, id, 4, epoch);
    STAMP(11);
    publish(rs, WS_H + 3 * WS_HS + own_off, *(const u32x4*)y_src);
    t_s = __builtin_amdgcn_s_memrealtime();

    wait_until(t_s + POLL_DELAY);
    pr = poll_issue(rs, WS_H + 3 * WS_HS + row_in);
    poll_finish(pr, rs, WS_H + 3 * WS_HS + row_in);
    STAMP(12);
    {
        float y[8];
        unpack8(pr, y);
#pragma unroll
        for (int j = 0; j < 8; ++j) y[j] += x[j];
        unpack8(pack8(y), x);
        layer_norm(x, u, red, lane, wave);
#pragma unroll
        for (int j = 0; j < 8; ++j) u[j] = fmaf(u[j], yA[j], x[j]);
        unpack8(pack8(u), x);
        layer_norm(x, u, red + 128, lane, wave);
    }
    publish(rs, WS_H + 4 * WS_HS + own_off, pack8(u));
    t_s = __builtin_amdgcn_s_memrealtime();
    STAMP(13);

    wait_until(t_s + POLL_DELAY);
    pc = poll_issue(rs, WS_H + 4 * WS_HS + col_in);
    if (wave < 4) conv_preload(A0, ca);
    poll_finish(pc, rs, WS_H + 4 * WS_HS + col_in);
    *(u32x4*)u_dst = pc;
    BAR();
    STAMP(14);
    conv<false>(smem, A0, ca, wave, lane, rs, id, -1, epoch);
    STAMP(15);
    publish(rs, WS_H + 5 * WS_HS + own_off, *(const u32x4*)y_src);
    t_s = __builtin_amdgcn_s_memrealtime();

    wait_until(t_s + POLL_DELAY);
    pr = poll_issue(rs, WS_H + 5 * WS_HS + row_in);
    poll_finish(pr, rs, WS_H + 5 * WS_HS + row_in);
    STAMP(16);
    {
        float y[8];
        unpack8(pr, y);
#pragma unroll
        for (int j = 0; j < 8; ++j) y[j] += x[j];
        const u32x4 o = pack8(y);
        u16* dst = out + (8 * id) * DM + tid;
#pragma unroll
        for (int j = 0; j < 4; ++j) {
            dst[(2 * j) * DM] = (u16)o[j];
            dst[(2 * j + 1) * DM] = (u16)(o[j] >> 16);
        }
    }
    STAMP(17);
    asm volatile("s_waitcnt vmcnt(0)" ::: "memory");
    STAMP(18);
    BAR();
    if (tid == 0) {
        const u64* st = (const u64*)(smem + OFF_ST);
        u64 dur = 0;
        if (DIAG == 1) dur = st[1] - st[0];
        if (DIAG == 2) dur = st[5] - st[1];
        if (DIAG == 3) dur = st[11] - st[5];
        if (DIAG == 4) dur = st[18] - st[11];
        if (DIAG == 5) dur = (__builtin_amdgcn_s_memtime() - clk0) / 100;
        if (DIAG == 6) dur = st[19] - st[0];
        const u64 until = __builtin_amdgcn_s_memrealtime() + DIAG_AMP * dur;
        while (__builtin_amdgcn_s_memrealtime() < until) __builtin_amdgcn_s_sleep(2);
    }
}

extern "C" void kernel_launch(void* const* d_in, const int* in_sizes, int n_in,
                              void* d_out, int out_size, void* d_ws, size_t ws_size,
                              hipStream_t stream) {
    h3s4_kernel<<<256, 512, 0, stream>>>(
        (const char*)d_in[0], (const char*)d_in[1], (const char*)d_in[2], (const u32x2*)d_in[3], (const u32x4*)d_in[4],
        (const u32*)d_in[5], (const u16*)d_in[7], (const u16*)d_in[8], (const u16*)d_in[9], (const u16*)d_in[10],
        (const u16*)d_in[11], (const u16*)d_in[12], (const u16*)d_in[13], (const u32*)d_in[14], (u16*)d_out, (char*)d_ws
        );
}
```

```cpp
#include <hip/hip_runtime.h>
#include <hip/hip_bf16.h>
#include <stdint.h>

typedef unsigned int u32;
typedef unsigned short u16;
typedef unsigned long long u64;
typedef __attribute__((ext_vector_type(4))) float f32x4;
typedef __attribute__((ext_vector_type(2))) float f32x2;
typedef __attribute__((ext_vector_type(4))) u32 u32x4;
typedef __attribute__((ext_vector_type(2))) u32 u32x2;
typedef __attribute__((ext_vector_type(4))) short s16x4;
typedef __attribute__((ext_vector_type(8))) __bf16 bf16x8;
typedef __attribute__((ext_vector_type(2))) __bf16 bf16x2;
#define LAS __attribute__((address_space(3)))
#define GAS __attribute__((address_space(1)))
#define RLX_AGENT __ATOMIC_RELAXED, __HIP_MEMORY_SCOPE_AGENT
#define SB() __builtin_amdgcn_sched_barrier(0)
#define BAR() asm volatile("s_waitcnt lgkmcnt(0)\n\ts_barrier" ::: "memory")
#define MEMFENCE() asm volatile("" ::: "memory")
#define WAIT_VM(n) do { MEMFENCE(); __builtin_amdgcn_s_waitcnt(0x0F70 | ((n) & 15) | (((n) >> 4) << 14)); MEMFENCE(); } while (0)

constexpr int DM = 512;
constexpr int NS = 128;
constexpr u32 SENT = 0xffffffffu;
constexpr u32 SPIN_CAP = 20000;
#define POLL_DELAY 50

constexpr u32 WS_EPOCH = 0;
constexpr u32 WS_FLAGS = 4096;
constexpr u32 WS_H = 1u << 20;
constexpr u32 WS_HS = 2u << 20;
constexpr u32 WS_USED = WS_H + 6 * WS_HS;

constexpr int T_COPY = 4160, T_CH = 8 * T_COPY;
constexpr int U_STRIDE = 4672;
constexpr int R_ROW = 1088;
constexpr int OFF_T = 0;
constexpr int OFF_U = OFF_T + 2 * T_CH;
constexpr int OFF_X = OFF_U + 2 * U_STRIDE;
constexpr int OFF_Y = OFF_X + 16384;
constexpr int OFF_R = OFF_Y + 8192;
constexpr int OFF_P = OFF_R;
constexpr int OFF_KP = OFF_R + 4096;
constexpr int KP_WAVE = 2576;
constexpr int OFF_KC = OFF_KP + 8 * KP_WAVE;
constexpr int OFF_RED = OFF_R + 28 * R_ROW;
constexpr int OFF_ST = OFF_RED + 1024;
constexpr int LDS_BYTES = OFF_ST + 256;
static_assert(OFF_KC + 2 * 648 * 2 <= OFF_RED, "table alias");

#define STAMP(i)

__device__ __forceinline__ float bf_lo(u32 p) { return __builtin_bit_cast(float, p << 16); }
__device__ __forceinline__ float bf_hi(u32 p) { return __builtin_bit_cast(float, p & 0xffff0000u); }
__device__ __forceinline__ float bf_u16(u16 p) { return __builtin_bit_cast(float, (u32)p << 16); }
__device__ __forceinline__ u32 pk_bf(float lo, float hi) {
    bf16x2 v = {(__bf16)lo, (__bf16)hi};
    return __builtin_bit_cast(u32, v);
}
__device__ __forceinline__ float as_f(u32 x) { return __builtin_bit_cast(float, x); }
__device__ __forceinline__ u32 as_u(float x) { return __builtin_bit_cast(u32, x); }
__device__ __forceinline__ float rbf(float x) { return bf_lo(pk_bf(x, x)); }
__device__ __forceinline__ void unpack8(u32x4 p, float (&f)[8]) {
#pragma unroll
    for (int i = 0; i < 4; ++i) { f[2 * i] = bf_lo(p[i]); f[2 * i + 1] = bf_hi(p[i]); }
}
__device__ __forceinline__ u32x4 pack8(const float (&f)[8]) {
    return (u32x4){pk_bf(f[0], f[1]), pk_bf(f[2], f[3]), pk_bf(f[4], f[5]), pk_bf(f[6], f[7])};
}
template <int CTRL> __device__ __forceinline__ float dppf(float v) {
    return __builtin_bit_cast(float, __builtin_amdgcn_update_dpp(0, __builtin_bit_cast(int, v), CTRL, 0xf, 0xf, true));
}

__device__ __forceinline__ void lds_dma16(const char* src, u32 lds) {
    u32 keep;
    asm volatile("s_mov_b32 %0, m0\n\ts_mov_b32 m0, %2\n\ts_nop 0\n\tglobal_load_lds_dwordx4 %1, off\n\ts_mov_b32 m0, %0"
                 : "=&s"(keep) : "v"(src), "s"(lds) : "memory");
}

typedef __amdgpu_buffer_rsrc_t rsrc_t;
__device__ __forceinline__ u32x4 ld_dev(rsrc_t rs, u32 off) { return __builtin_amdgcn_raw_buffer_load_b128(rs, off, 0, 16); }
__device__ __forceinline__ void st_dev(rsrc_t rs, u32 off, u32x4 v) { __builtin_amdgcn_raw_buffer_store_b128(v, rs, off, 0, 16); }
__device__ __forceinline__ void publish(rsrc_t rs, u32 off, u32x4 v) {
#pragma unroll
    for (int i = 0; i < 4; ++i) v[i] = v[i] < SENT - 1u ? v[i] : SENT - 1u;
    st_dev(rs, off, v);
}
__device__ __forceinline__ void wait_until(u64 t) {
    while (__builtin_amdgcn_s_memrealtime() < t) __builtin_amdgcn_s_sleep(1);
}
__device__ __forceinline__ u32x4 poll_issue(rsrc_t rs, u32 off) {
    MEMFENCE();
    u32x4 x = ld_dev(rs, off);
    MEMFENCE();
    return x;
}
__device__ __forceinline__ void poll_finish(u32x4& x, rsrc_t rs, u32 off) {
    u32 spins = 0;
    while (true) {
        u32 mx = x[0] > x[1] ? x[0] : x[1];
        mx = mx > x[2] ? mx : x[2];
        mx = mx > x[3] ? mx : x[3];
        const bool bad = mx == SENT;
        if (__builtin_amdgcn_ballot_w64(bad) == 0ull) break;
        if (++spins > SPIN_CAP) break;
        __builtin_amdgcn_s_sleep(1);
        MEMFENCE();
        if (bad) x = ld_dev(rs, off);
        MEMFENCE();
    }
}

__device__ __forceinline__ void flags_finish(u32x4& fl, rsrc_t rs, u32 off, u32 epoch) {
    u32 spins = 0;
    while (__builtin_amdgcn_ballot_w64(fl[0] != epoch || fl[1] != epoch || fl[2] != epoch || fl[3] != epoch) != 0ull) {
        if (++spins > SPIN_CAP) break;
        __builtin_amdgcn_s_sleep(2);
        fl = poll_issue(rs, off);
    }
}

__device__ __forceinline__ void row_stats(const float (&x)[8], float* red, int lane, int wave, float& mean, float& rstd) {
    u32 a[8], b[8];
#pragma unroll
    for (int j = 0; j < 8; ++j) { a[j] = as_u(x[j]); b[j] = as_u(x[j] * x[j]); asm volatile("" : "+v"(a[j]), "+v"(b[j])); }
    SB();
    float r[8];
#pragma unroll
    for (int j = 0; j < 8; ++j) {
        const auto s = __builtin_amdgcn_permlane32_swap(a[j], b[j], false, false);
        a[j] = s[0]; b[j] = s[1];
    }
    SB();
#pragma unroll
    for (int j = 0; j < 8; ++j) r[j] = as_f(a[j]) + as_f(b[j]);
    SB();
    float s4[4];
#pragma unroll
    for (int j = 0; j < 4; ++j) {
        const auto s = __builtin_amdgcn_permlane16_swap(as_u(r[j]), as_u(r[j + 4]), false, false);
        a[j] = s[0]; b[j] = s[1];
    }
    SB();
#pragma unroll
    for (int j = 0; j < 4; ++j) s4[j] = as_f(a[j]) + as_f(b[j]);
    SB();
#pragma unroll
    for (int j = 0; j < 4; ++j) s4[j] += dppf<0x128>(s4[j]);
    const float t0 = (lane & 8) ? s4[2] : s4[0], t1 = (lane & 8) ? s4[3] : s4[1];
    const float c0 = t0 + dppf<0x141>(t0), c1 = t1 + dppf<0x141>(t1);
    float w = (lane & 4) ? c1 : c0;
    w += dppf<0xB1>(w);
    w += dppf<0x4E>(w);
    if ((lane & 3) == 0) red[(lane >> 2) * 8 + wave] = w;
    BAR();
    const f32x4 p0 = *(const f32x4*)(red + (lane & 15) * 8), p1 = *(const f32x4*)(red + (lane & 15) * 8 + 4);
    const f32x4 p = p0 + p1;
    const float m = ((p[0] + p[1]) + (p[2] + p[3])) * (1.0f / DM);
    const float ex2 = dppf<0x128>(m);
    const float var = fmaxf(fmaf(-m, m, ex2), 0.f);
    const float sd = rbf(__builtin_amdgcn_sqrtf(rbf(rbf(var) + 1e-5f)));
    mean = rbf(m);
    rstd = __builtin_amdgcn_rcpf(sd);
}
__device__ __forceinline__ void layer_norm(const float (&x)[8], float (&u)[8], float* red, int lane, int wave) {
    float mean, rstd;
    row_stats(x, red, lane, wave, mean, rstd);
    asm volatile("s_nop 1\n\t"
                 "v_subrev_f32_dpp %0, %8, %10 row_newbcast:0 row_mask:0xf bank_mask:0xf\n\t"
                 "v_subrev_f32_dpp %1, %8, %11 row_newbcast:1 row_mask:0xf bank_mask:0xf\n\t"
                 "v_subrev_f32_dpp %2, %8, %12 row_newbcast:2 row_mask:0xf bank_mask:0xf\n\t"
                 "v_subrev_f32_dpp %3, %8, %13 row_newbcast:3 row_mask:0xf bank_mask:0xf\n\t"
                 "v_subrev_f32_dpp %4, %8, %14 row_newbcast:4 row_mask:0xf bank_mask:0xf\n\t"
                 "v_subrev_f32_dpp %5, %8, %15 row_newbcast:5 row_mask:0xf bank_mask:0xf\n\t"
                 "v_subrev_f32_dpp %6, %8, %16 row_newbcast:6 row_mask:0xf bank_mask:0xf\n\t"
                 "v_subrev_f32_dpp %7, %8, %17 row_newbcast:7 row_mask:0xf bank_mask:0xf\n\t"
                 "v_mul_f32_dpp %0, %9, %0 row_newbcast:0 row_mask:0xf bank_mask:0xf\n\t"
                 "v_mul_f32_dpp %1, %9, %1 row_newbcast:1 row_mask:0xf bank_mask:0xf\n\t"
                 "v_mul_f32_dpp %2, %9, %2 row_newbcast:2 row_mask:0xf bank_mask:0xf\n\t"
                 "v_mul_f32_dpp %3, %9, %3 row_newbcast:3 row_mask:0xf bank_mask:0xf\n\t"
                 "v_mul_f32_dpp %4, %9, %4 row_newbcast:4 row_mask:0xf bank_mask:0xf\n\t"
                 "v_mul_f32_dpp %5, %9, %5 row_newbcast:5 row_mask:0xf bank_mask:0xf\n\t"
                 "v_mul_f32_dpp %6, %9, %6 row_newbcast:6 row_mask:0xf bank_mask:0xf\n\t"
                 "v_mul_f32_dpp %7, %9, %7 row_newbcast:7 row_mask:0xf bank_mask:0xf"
                 : "=&v"(u[0]), "=&v"(u[1]), "=&v"(u[2]), "=&v"(u[3]), "=&v"(u[4]), "=&v"(u[5]), "=&v"(u[6]), "=&v"(u[7])
                 : "v"(mean), "v"(rstd), "v"(x[0]), "v"(x[1]), "v"(x[2]), "v"(x[3]), "v"(x[4]), "v"(x[5]), "v"(x[6]), "v"(x[7]));
}

__device__ __forceinline__ void stage_taps(char* smem, int tid, u32x4 prev, u32x4 own) {
    const u32 D[8] = {prev[0], prev[1], prev[2], prev[3], own[0], own[1], own[2], own[3]};
    u32 E[8], O[8];
#pragma unroll
    for (int y = 1; y < 8; ++y) {
        E[y] = __builtin_amdgcn_alignbit(D[y], D[y], 16);
        O[y] = (D[y] & 0xffffu) | (D[y - 1] & 0xffff0000u);
    }
    char* base = smem + OFF_T + (tid >> 8) * T_CH + (255 - (tid & 255)) * 16;
#pragma unroll
    for (int s = 0; s < 8; ++s) {
        const int y = 7 - (s >> 1);
        const u32x4 v = (s & 1) ? (u32x4){O[y], O[y - 1], O[y - 2], O[y - 3]} : (u32x4){E[y], E[y - 1], E[y - 2], E[y - 3]};
        *(u32x4*)(base + s * T_COPY) = v;
    }
}

struct cplx2 { f32x2 r, i; };
__device__ __forceinline__ cplx2 csq(cplx2 a) { return {a.r * a.r - a.i * a.i, (a.r + a.r) * a.i}; }
__device__ __forceinline__ cplx2 cmul(cplx2 a, cplx2 b) { return {a.r * b.r - a.i * b.i, a.r * b.i + a.i * b.r}; }
__device__ __forceinline__ f32x4 mfma16(u32x4 a, u32x4 b, f32x4 c) {
    return __builtin_amdgcn_mfma_f32_16x16x32_bf16(__builtin_bit_cast(bf16x8, a), __builtin_bit_cast(bf16x8, b), c, 0, 0, 0);
}
__device__ __forceinline__ void kgen_step(const f32x4* P, int lane, f32x4 (&acc)[4], float& k2048) {
#pragma clang fp contract(fast)
    const int g = lane >> 4;
    const float pm = (float)(lane & 15);
    cplx2 b[2], W[2];
#pragma unroll
    for (int h = 0; h < 2; ++h) {
        const f32x4 p0 = P[4 * g + 2 * h], p1 = P[4 * g + 2 * h + 1];
        const f32x2 ex = (f32x2){p0[0], p1[0]} * pm, an = (f32x2){p0[1], p1[1]} * pm;
        const f32x2 mag = {__builtin_amdgcn_exp2f(ex[0]), __builtin_amdgcn_exp2f(ex[1])};
        const float f0 = __builtin_amdgcn_fractf(an[0]), f1 = __builtin_amdgcn_fractf(an[1]);
        b[h].r = mag * (f32x2){__builtin_amdgcn_cosf(f0), __builtin_amdgcn_cosf(f1)};
        b[h].i = mag * (f32x2){__builtin_amdgcn_sinf(f0), __builtin_amdgcn_sinf(f1)};
        W[h].r = (f32x2){p0[2], p1[2]};
        W[h].i = (f32x2){p0[3], p1[3]};
    }
    u32x4 A[4], B[4];
#pragma unroll
    for (int e = 0; e < 4; ++e) {
#pragma unroll
        for (int h = 0; h < 2; ++h) {
            B[e][2 * h] = pk_bf(b[h].r[0], -b[h].i[0]);
            B[e][2 * h + 1] = pk_bf(b[h].r[1], -b[h].i[1]);
            b[h] = csq(b[h]);
        }
    }
#pragma unroll
    for (int e = 0; e < 4; ++e) {
#pragma unroll
        for (int h = 0; h < 2; ++h) {
            const cplx2 x = cmul(W[h], b[h]);
            A[e][2 * h] = pk_bf(x.r[0], x.i[0]);
            A[e][2 * h + 1] = pk_bf(x.r[1], x.i[1]);
            if (e == 3) {
                const f32x2 z = x.r * b[h].r - x.i * b[h].i;
                k2048 += z[0] + z[1];
            } else b[h] = csq(b[h]);
        }
        acc[e] = mfma16(A[e], B[e], acc[e]);
    }
}

struct KState { f32x4 acc[4]; float k2048; };
__device__ __forceinline__ const f32x4* kgen_params(char* smem, int wave) {
    return (const f32x4*)(smem + OFF_P) + (wave >> 2) * 128 + (wave & 3) * 32;
}
__device__ __forceinline__ void kgen_save(char* smem, int wave, int lane, const KState& ks) {
    char* p = smem + OFF_KP + wave * KP_WAVE;
    *(f32x4*)(p + lane * 16) = ks.acc[0];
    if (lane >= 32) {
#pragma unroll
        for (int e = 1; e < 4; ++e) *(f32x4*)(p + 512 * e + lane * 16) = ks.acc[e];
    }
    const auto s = __builtin_amdgcn_permlane32_swap(as_u(ks.k2048), as_u(ks.k2048), false, false);
    const float h = as_f(s[0]) + as_f(s[1]);
    const auto t = __builtin_amdgcn_permlane16_swap(as_u(h), as_u(h), false, false);
    const float z = as_f(t[0]) + as_f(t[1]);
    if (lane == 8) *(float*)(p + 2560) = z;
}
__device__ __forceinline__ void kgen_load(const char* smem, int wave, int lane, KState& ks) {
    const char* p = smem + OFF_KP + wave * KP_WAVE;
    ks.acc[0] = *(const f32x4*)(p + lane * 16);
#pragma unroll
    for (int e = 1; e < 4; ++e) ks.acc[e] = *(const f32x4*)(p + 512 * e + (lane | 32) * 16);
    ks.k2048 = lane == 8 ? *(const float*)(p + 2560) : 0.f;
}
__device__ __forceinline__ void kgen_reduce(char* smem, int wave, int lane, u32 dskip) {
    const int c = wave >> 2, e = wave & 3, m = lane & 15, g = lane >> 4;
    const char* p = smem + OFF_KP + c * 4 * KP_WAVE + (e ? 512 * e + (lane | 32) * 16 : lane * 16);
    f32x4 s = (*(const f32x4*)p + *(const f32x4*)(p + KP_WAVE)) + (*(const f32x4*)(p + 2 * KP_WAVE) + *(const f32x4*)(p + 3 * KP_WAVE));
    u16* KC = (u16*)(smem + OFF_KC) + c * 648;
    if (e == 0 && lane == 0) s[0] += c ? bf_hi(dskip) : bf_lo(dskip);
    const u32 lo = pk_bf(s[0], s[1]), hi = pk_bf(s[2], s[3]);
    if (e == 0 || g >= 2) {
        u16* o = KC + 128 * e + 64 * g + m;
        o[0] = (u16)lo; o[16] = (u16)(lo >> 16); o[32] = (u16)hi; o[48] = (u16)(hi >> 16);
    }
    if (e == 3 && lane == 0) {
        const char* z = smem + OFF_KP + c * 4 * KP_WAVE + 2560;
        const float k = (*(const float*)z + *(const float*)(z + KP_WAVE)) + (*(const float*)(z + 2 * KP_WAVE) + *(const float*)(z + 3 * KP_WAVE));
        KC[640] = (u16)pk_bf(k, 0.f);
    }
}

struct Frags { u32x4 A[8], B[8]; };
template <bool INIT> __device__ __forceinline__ void mma(f32x4& acc, const u32x4& a, const u32x4& b) {
    if constexpr (INIT) asm volatile("v_mfma_f32_16x16x32_bf16 %0, %1, %2, 0" : "=&v"(acc) : "v"(a), "v"(b));
    else asm volatile("v_mfma_f32_16x16x32_bf16 %0, %1, %2, %0" : "+v"(acc) : "v"(a), "v"(b));
}
template <int I> __device__ __forceinline__ void load_one(Frags& f, const char* ap, const char* bp) {
    if constexpr (I == 0) f.A[0] = *(const u32x4*)(ap + 512 * 7);
    else if constexpr (I <= 8) f.B[I - 1] = *(const u32x4*)(bp + 512 * (I - 1));
    else f.A[I - 8] = *(const u32x4*)(ap + 512 * (7 - (I - 8)));
}
template <int I> __device__ __forceinline__ void load_all(Frags& f, const char* ap, const char* bp) {
    if constexpr (I < 16) { load_one<I>(f, ap, bp); SB(); load_all<I + 1>(f, ap, bp); }
}
template <int T0, int T1, int T2, int T3> struct TileSet {
    static constexpr int tile(int i) { return i == 0 ? T0 : i == 1 ? T1 : i == 2 ? T2 : T3; }
    static constexpr int walk(int I, bool want_tile) {
        int n = 0;
        for (int a = 0; a < 8; ++a)
            for (int j = 0; j < 4; ++j)
                if (tile(j) >= a) { if (n == I) return want_tile ? tile(j) : a; ++n; }
        return -1;
    }
};
constexpr int tri_a(int I) { int a = 0; while (I >= 8 - a) { I -= 8 - a; ++a; } return a; }
constexpr int tri_b(int I) { int a = 0; while (I >= 8 - a) { I -= 8 - a; ++a; } return I; }
template <int I, bool FIRST> __device__ __forceinline__ void class_step(const Frags& cur, Frags& nxt, f32x4 (&acc)[8], const char* ap, const char* bp) {
    if constexpr (I < 36) {
        constexpr int a = tri_a(I), b = tri_b(I);
        mma<FIRST && a == 0>(acc[a + b], cur.A[a], cur.B[b]);
        if constexpr ((I & 1) == 1 && (I >> 1) < 16) load_one<(I >> 1)>(nxt, ap, bp);
        SB();
        class_step<I + 1, FIRST>(cur, nxt, acc, ap, bp);
    }
}
template <class TS, int I, int TO, bool PRE> __device__ __forceinline__ void set_step(const Frags& cur, Frags& nxt, f32x4 (&acc)[8], const char* ap, const char* bp) {
    if constexpr (I < TO) {
        constexpr int t = TS::walk(I, true), a = TS::walk(I, false);
        mma<false>(acc[t], cur.A[a], cur.B[t - a]);
        if constexpr (PRE && I < 16) load_one<I>(nxt, ap, bp);
        SB();
        set_step<TS, I + 1, TO, PRE>(cur, nxt, acc, ap, bp);
    }
}
template <class TS, int J> __device__ __forceinline__ void tile_step(const Frags& cur, f32x4 (&acc)[8]) {
    constexpr int t = TS::tile(J);
#pragma unroll
    for (int a = 0; a <= t; ++a) { mma<false>(acc[t], cur.A[a], cur.B[t - a]); SB(); }
}
template <class TS, int J> __device__ __forceinline__ void tile_out(char* Y, f32x4 (&acc)[8], const f32x4 (&o)[4]) {
    constexpr int t = TS::tile(J);
    const f32x4 s = acc[t] + o[J];
    const u32x2 pk = {pk_bf(s[0], s[1]), pk_bf(s[2], s[3])};
    *(u32x2*)(Y + 512 * t) = pk;
    SB();
}
template <class SEND, class KEEP>
__device__ __forceinline__ void conv_half(char* smem, const u32x4 (&A0)[8], const char* ap, const char* bp, int wave, int lane, int ch) {
    f32x4 acc[8];
    Frags f0, f1;
#pragma unroll
    for (int a = 0; a < 8; ++a) f0.A[a] = A0[a];
#pragma unroll
    for (int b = 0; b < 8; ++b) { f0.B[b] = *(const u32x4*)(bp + 512 * b); SB(); }
    class_step<0, true>(f0, f1, acc, ap - 64, bp - 64);
    class_step<0, false>(f1, f0, acc, ap - 128, bp - 128);
    set_step<SEND, 0, 18, true>(f0, f1, acc, ap - 192, bp - 192);
    set_step<SEND, 0, 18, false>(f1, f0, acc, ap, bp);
    set_step<KEEP, 0, 4, false>(f0, f1, acc, ap, bp);
    asm volatile("s_nop 3" : "+v"(acc[SEND::tile(0)]), "+v"(acc[SEND::tile(1)]), "+v"(acc[SEND::tile(2)]), "+v"(acc[SEND::tile(3)]));
    f32x4* X = (f32x4*)(smem + OFF_X);
#pragma unroll
    for (int i = 0; i < 4; ++i) X[(wave * 4 + i) * 64 + lane] = acc[SEND::tile(i)];
    SB();
    set_step<KEEP, 4, 18, false>(f0, f1, acc, ap, bp);
    BAR();
    SB();
    f32x4 o[4];
#pragma unroll
    for (int i = 0; i < 4; ++i) o[i] = X[((wave ^ 1) * 4 + i) * 64 + lane];
    SB();
    char* Y = smem + OFF_Y + ch * 4096 + (16 * (lane & 15) + 4 * (lane >> 4)) * 2;
    tile_step<KEEP, 0>(f1, acc);
    tile_step<KEEP, 1>(f1, acc);
    tile_step<KEEP, 2>(f1, acc);
    asm volatile("s_nop 3" : "+v"(acc[KEEP::tile(0)]), "+v"(acc[KEEP::tile(1)]));
    tile_out<KEEP, 0>(Y, acc, o);
    tile_out<KEEP, 1>(Y, acc, o);
    tile_step<KEEP, 3>(f1, acc);
    asm volatile("" : "+v"(acc[KEEP::tile(2)]));
    tile_out<KEEP, 2>(Y, acc, o);
    asm volatile("s_nop 7" : "+v"(acc[KEEP::tile(3)]));
    tile_out<KEEP, 3>(Y, acc, o);
}
struct ConvAddr { const char* ap; const char* bp; };
__device__ __forceinline__ ConvAddr conv_addr(char* smem, int wave, int lane) {
    const int ch = (wave >> 1) & 1, hf = wave & 1;
    const int m = lane & 15, g = lane >> 4;
    ConvAddr c;
    c.ap = smem + OFF_T + ch * T_CH + ((7 - m) & 7) * T_COPY + (m < 8 ? 4048 : 4032) + 16 * g - 512 * 7 - 256 * hf;
    c.bp = smem + OFF_U + ch * U_STRIDE + 448 + 32 * m + 16 * g - 256 * hf;
    return c;
}
__device__ __forceinline__ void conv_preload(u32x4 (&A0)[8], const ConvAddr& c) {
#pragma unroll
    for (int a = 0; a < 8; ++a) A0[a] = *(const u32x4*)(c.ap + 512 * (7 - a));
}
template <bool FLAGS>
__device__ __forceinline__ void conv(char* smem, const u32x4 (&A0)[8], const ConvAddr& c, int wave, int lane, rsrc_t rs, int id, int fill0,
                                     u32 epoch) {
    if (wave < 4) {
        if ((wave & 1) == 0) conv_half<TileSet<1, 3, 4, 6>, TileSet<0, 2, 5, 7>>(smem, A0, c.ap, c.bp, wave, lane, wave >> 1);
        else conv_half<TileSet<0, 2, 5, 7>, TileSet<1, 3, 4, 6>>(smem, A0, c.ap, c.bp, wave, lane, wave >> 1);
    } else {
        if (fill0 >= 0) {
            const u32x4 sent = {SENT, SENT, SENT, SENT};
            const u32 off = WS_H + fill0 * WS_HS + id * 8192 + ((wave - 4) * 64 + lane) * 16;
            st_dev(rs, off, sent); st_dev(rs, off + 4096, sent);
            st_dev(rs, off + WS_HS, sent); st_dev(rs, off + WS_HS + 4096, sent);
        }
        BAR();
        if (FLAGS && wave == 7) {
            const u32 flag_off = WS_FLAGS + 32768 + (blockIdx.x & 31) * 1024 + lane * 16;
            u32x4 fl = poll_issue(rs, flag_off);
            flags_finish(fl, rs, flag_off, epoch);
        }
        if (fill0 >= 0) asm volatile("s_waitcnt vmcnt(0)" ::: "memory");
    }
    BAR();
}

__global__ void __launch_bounds__(512, 2)
h3s4_kernel(const char* __restrict__ q, const char* __restrict__ k, const char* __restrict__ v,
            const u32x2* __restrict__ shiftk, const u32x4* __restrict__ ssmk, const u32* __restrict__ ssmD,
            const u16* __restrict__ Are, const u16* __restrict__ Aim, const u16* __restrict__ Bre, const u16* __restrict__ Bim,
            const u16* __restrict__ Cre, const u16* __restrict__ Cim, const u16* __restrict__ logdt, const u32* __restrict__ Dskip,
            u16* __restrict__ out, char* ws
            ) {
    __shared__ __attribute__((aligned(16))) char smem[LDS_BYTES];
    const int tid = threadIdx.x, lane = tid & 63, wave = __builtin_amdgcn_readfirstlane(tid >> 6);
    const int id = (blockIdx.x & 7) * 32 + (blockIdx.x >> 3);
    const rsrc_t rs = __builtin_amdgcn_make_buffer_rsrc(ws, 0, WS_USED, 0x00020000);
    float* red = (float*)(smem + OFF_RED);
    STAMP(0);

    u32 code_pf = 0;
    {
        u64 pc; u32 len;
        asm volatile("s_getpc_b64 %0\nh3s4_text_a:\n\ts_mov_b32 %1, h3s4_text_b - h3s4_text_a" : "=s"(pc), "=s"(len));
        const u32 off = ((blockIdx.x >> 3) * 8 + wave) * 256 + lane * 4;
        if (off + 4 <= len) code_pf = *(const GAS u32*)(pc + off);
    }
    const u32x4 sent = {SENT, SENT, SENT, SENT};
    const u32 own_off = id * 8192 + tid * 16;
    u32 epoch;
    asm volatile("s_load_dword %0, %1, 0x0" : "=s"(epoch) : "s"(ws + WS_EPOCH));
    const u32 dA = ssmD[id], dB = Dskip[id];
    MEMFENCE(); SB();
    st_dev(rs, WS_H + 0 * WS_HS + own_off, sent);
    MEMFENCE(); SB();
#pragma unroll
    for (int s = 0; s < 4; ++s) {
        const int slot = wave + 8 * s;
        if (slot < 28) {
            int row = 8 * id + (slot < 12 ? slot - 4 : slot < 20 ? slot - 12 : slot - 20);
            row = row < 0 ? 0 : row;
            const char* src = slot < 12 ? k : slot < 20 ? v : q;
            lds_dma16(src + row * 1024 + lane * 16, (u32)(size_t)(LAS char*)(smem + OFF_R) + slot * R_ROW);
        }
    }
    const u32x2 sk = shiftk[tid];
    const int tch = tid >> 8, ti = tid & 255;
    u32x4 tap_own = ssmk[(2 * id + tch) * 256 + ti];
    u32x4 tap_prev = ssmk[(2 * id + tch) * 256 + (ti ? ti - 1 : 0)];
    const int pd = 2 * id + ((tid >> 7) & 1), po = pd * NS + (tid & 127);
    const u16 r_dt = logdt[pd], r_are = Are[po], r_aim = Aim[po], r_bre = Bre[po], r_bim = Bim[po], r_cre = Cre[po], r_cim = Cim[po];
    MEMFENCE(); SB();
    st_dev(rs, WS_H + 1 * WS_HS + own_off, sent);
    MEMFENCE(); SB();
    asm volatile("s_waitcnt vmcnt(13) lgkmcnt(0)\n\ts_barrier" : "+s"(epoch) :: "memory");
    epoch += 1u;
    if (tid < 32) __hip_atomic_store((GAS u32*)(ws + WS_FLAGS) + tid * 256 + id, epoch, RLX_AGENT);
    STAMP(19);
    if (tid < 60) *(u32x4*)(smem + OFF_U + (tid / 30) * U_STRIDE + (tid % 30) * 16) = (u32x4){0, 0, 0, 0};
    else if (tid >= 64 && tid < 80) *(u32x4*)(smem + OFF_T + (tid - 64) * T_COPY + 4096) = (u32x4){0, 0, 0, 0};
    if (wave == 0) WAIT_VM(2); else WAIT_VM(1);
    asm volatile("" :: "v"(code_pf));
    asm volatile("" :: "v"(sk), "v"(tap_own), "v"(tap_prev), "v"(r_dt), "v"(r_are), "v"(r_aim), "v"(r_bre), "v"(r_bim), "v"(r_cre), "v"(r_cim));
    BAR();
    STAMP(1);

    float x[8];
    u32x4 xq;
    {
        const u32 rp = (u32)(size_t)(LAS char*)(smem + OFF_R + ((lane & 15) >> 2) * R_ROW + (64 * wave + 16 * (lane >> 4) + 4 * (lane & 3)) * 2);
        u32x2 k0, k1, k2, v0, v1, q0, q1;
        static_assert(4 * R_ROW == 4352, "offsets below");
        asm volatile("ds_read_b64_tr_b16 %0, %7\n\tds_read_b64_tr_b16 %1, %7 offset:4352\n\tds_read_b64_tr_b16 %2, %7 offset:8704\n\t"
                     "ds_read_b64_tr_b16 %3, %7 offset:13056\n\tds_read_b64_tr_b16 %4, %7 offset:17408\n\t"
                     "ds_read_b64_tr_b16 %5, %7 offset:21760\n\tds_read_b64_tr_b16 %6, %7 offset:26112\n\ts_waitcnt lgkmcnt(0)"
                     : "=&v"(k0), "=&v"(k1), "=&v"(k2), "=&v"(v0), "=&v"(v1), "=&v"(q0), "=&v"(q1) : "v"(rp) : "memory");
        const u32 first = id ? 0xffffffffu : 0u;
        const u32 kp = k0[0] & first, kq = k0[1] & first;
        const float kf[12] = {0.f, bf_hi(kp), bf_lo(kq), bf_hi(kq), bf_lo(k1[0]), bf_hi(k1[0]),
                              bf_lo(k1[1]), bf_hi(k1[1]), bf_lo(k2[0]), bf_hi(k2[0]), bf_lo(k2[1]), bf_hi(k2[1])};
        float vf[8];
        unpack8((u32x4){v0[0], v0[1], v1[0], v1[1]}, vf);
        const float w0 = bf_lo(sk[0]), w1 = bf_hi(sk[0]), w2 = bf_lo(sk[1]), w3 = bf_hi(sk[1]);
        float kv[8];
#pragma unroll
        for (int j = 0; j < 8; ++j) kv[j] = fmaf(kf[j + 4], w0, fmaf(kf[j + 3], w1, fmaf(kf[j + 2], w2, kf[j + 1] * w3))) * vf[j];
        publish(rs, WS_H + 0 * WS_HS + own_off, pack8(kv));
        xq = (u32x4){q0[0], q0[1], q1[0], q1[1]};
    }
    u64 t_s = __builtin_amdgcn_s_memrealtime();
    STAMP(2);
    u32 touch = 0;
    if (wave == 6) {
        if (lane < 4) touch = __builtin_amdgcn_raw_buffer_load_b32(rs, WS_H + (2 + lane) * WS_HS + id * 8192, 0, 16);
        else if (lane == 4) touch = ((const u32*)out)[id * 2048];
    }

    const u32 flag_off = WS_FLAGS + (blockIdx.x & 31) * 1024 + lane * 16;
    u32x4 fl = {epoch, epoch, epoch, epoch};
    if (wave == 7) fl = poll_issue(rs, flag_off);
    if (ti == 0) {
        tap_prev = (u32x4){0, 0, 0, 0};
        tap_own[0] = (tap_own[0] & 0xffff0000u) | (pk_bf(bf_lo(tap_own[0]) + (tch ? bf_hi(dA) : bf_lo(dA)), 0.f) & 0xffffu);
    }
    stage_taps(smem, tid, tap_prev, tap_own);
    f32x4 prm;
    {
        const float dt = rbf(expf(bf_u16(r_dt)));
        const float are = dt * bf_u16(r_are), aim = dt * bf_u16(r_aim);
        const float br = dt * bf_u16(r_bre), bi = dt * bf_u16(r_bim);
        const float cr = bf_u16(r_cre), ci = bf_u16(r_cim);
        prm = (f32x4){are * 1.4426950408889634f, aim * 0.15915494309189535f, cr * br - ci * bi, cr * bi + ci * br};
        asm volatile("" : "+v"(prm));
    }
    if (wave == 7) {
        flags_finish(fl, rs, flag_off, epoch);
        if (blockIdx.x == 0 && lane == 0) __hip_atomic_store((GAS u32*)(ws + WS_EPOCH), epoch, RLX_AGENT);
    }
    BAR();
    if (tid < 256) ((f32x4*)(smem + OFF_P))[tid] = prm;
    STAMP(3);

    const u32 col_in = (tid >> 1) * 8192 + (2 * id + (tid & 1)) * 16;
    const u32 row_in = (tid >> 1) * 8192 + id * 32 + (tid & 1) * 16;
    char* const u_dst = smem + OFF_U + (tid & 1) * U_STRIDE + 480 + (tid >> 1) * 16;
    const char* const y_src = smem + OFF_Y + (tid & 1) * 4096 + (tid >> 1) * 16;
    const ConvAddr ca = conv_addr(smem, wave, lane);
    u32x4 A0[8];

    wait_until(t_s + POLL_DELAY);
    u32x4 pc = poll_issue(rs, WS_H + 0 * WS_HS + col_in);
    BAR();
    {
        KState ks;
#pragma unroll
        for (int e = 0; e < 4; ++e) ks.acc[e] = (f32x4){0, 0, 0, 0};
        ks.k2048 = 0.f;
        kgen_step(kgen_params(smem, wave), lane, ks.acc, ks.k2048);
        kgen_save(smem, wave, lane, ks);
    }
    STAMP(4);
    if (wave < 4) conv_preload(A0, ca);
    poll_finish(pc, rs, WS_H + 0 * WS_HS + col_in);
    *(u32x4*)u_dst = pc;
    asm volatile("" :: "v"(touch));
    BAR();
    if (wave == 7 && lane < 32) __hip_atomic_store((GAS u32*)(ws + WS_FLAGS + 32768) + lane * 256 + id, epoch, RLX_AGENT);
    STAMP(5);

    conv<true>(smem, A0, ca, wave, lane, rs, id, 2, epoch);
    STAMP(6);
    publish(rs, WS_H + 1 * WS_HS + own_off, *(const u32x4*)y_src);
    t_s = __builtin_amdgcn_s_memrealtime();

    float u[8];
    unpack8(xq, x);
    layer_norm(x, u, red, lane, wave);
    const u32x4 u0q = pack8(u);
    wait_until(t_s + POLL_DELAY);
    u32x4 pr = poll_issue(rs, WS_H + 1 * WS_HS + row_in);
    {
        KState ks;
        kgen_load(smem, wave, lane, ks);
        kgen_step(kgen_params(smem, wave) + 16, lane, ks.acc, ks.k2048);
        kgen_save(smem, wave, lane, ks);
    }
    STAMP(7);

    poll_finish(pr, rs, WS_H + 1 * WS_HS + row_in);
    STAMP(8);
    float yA[8];
    unpack8(pr, yA);
    unpack8(u0q, u);
#pragma unroll
    for (int j = 0; j < 8; ++j) u[j] = fmaf(u[j], yA[j], x[j]);
    unpack8(pack8(u), x);
    layer_norm(x, u, red + 128, lane, wave);
    publish(rs, WS_H + 2 * WS_HS + own_off, pack8(u));
    t_s = __builtin_amdgcn_s_memrealtime();
    STAMP(9);

    {
        kgen_reduce(smem, wave, lane, dB);
        BAR();
        const u16* KC = (const u16*)(smem + OFF_KC);
        u32 d[8];
#pragma unroll
        for (int j = 0; j < 8; ++j) {
            const int l0 = 8 * ti - 8 + 2 * j;
            const u32 bits = pk_bf((float)l0, (float)(l0 + 1));
            const int i0 = l0 < 128 ? (l0 < 0 ? 0 : l0) : (int)(bits & 0xffffu) - 17024;
            const int i1 = l0 < 128 ? (l0 < 0 ? 0 : l0 + 1) : (int)(bits >> 16) - 17024;
            const u32 a = KC[tch * 648 + i0], b = KC[tch * 648 + i1];
            d[j] = l0 < 0 ? 0u : (a | (b << 16));
        }
        wait_until(t_s + POLL_DELAY);
        pc = poll_issue(rs, WS_H + 2 * WS_HS + col_in);
        stage_taps(smem, tid, (u32x4){d[0], d[1], d[2], d[3]}, (u32x4){d[4], d[5], d[6], d[7]});
        BAR();
    }

    if (wave < 4) conv_preload(A0, ca);
    poll_finish(pc, rs, WS_H + 2 * WS_HS + col_in);
    *(u32x4*)u_dst = pc;
    BAR();
    STAMP(10);
    conv<false>(smem, A0, ca, wave, lane, rs, id, 4, epoch);
    STAMP(11);
    publish(rs, WS_H + 3 * WS_HS + own_off, *(const u32x4*)y_src);
    t_s = __builtin_amdgcn_s_memrealtime();

    wait_until(t_s + POLL_DELAY);
    pr = poll_issue(rs, WS_H + 3 * WS_HS + row_in);
    poll_finish(pr, rs, WS_H + 3 * WS_HS + row_in);
    STAMP(12);
    {
        float y[8];
        unpack8(pr, y);
#pragma unroll
        for (int j = 0; j < 8; ++j) y[j] += x[j];
        unpack8(pack8(y), x);
        layer_norm(x, u, red, lane, wave);
#pragma unroll
        for (int j = 0; j < 8; ++j) u[j] = fmaf(u[j], yA[j], x[j]);
        unpack8(pack8(u), x);
        layer_norm(x, u, red + 128, lane, wave);
    }
    publish(rs, WS_H + 4 * WS_HS + own_off, pack8(u));
    t_s = __builtin_amdgcn_s_memrealtime();
    STAMP(13);

    wait_until(t_s + POLL_DELAY);
    pc = poll_issue(rs, WS_H + 4 * WS_HS + col_in);
    if (wave < 4) conv_preload(A0, ca);
    poll_finish(pc, rs, WS_H + 4 * WS_HS + col_in);
    *(u32x4*)u_dst = pc;
    BAR();
    STAMP(14);
    conv<false>(smem, A0, ca, wave, lane, rs, id, -1, epoch);
    STAMP(15);
    publish(rs, WS_H + 5 * WS_HS + own_off, *(const u32x4*)y_src);
    t_s = __builtin_amdgcn_s_memrealtime();

    wait_until(t_s + POLL_DELAY);
    pr = poll_issue(rs, WS_H + 5 * WS_HS + row_in);
    poll_finish(pr, rs, WS_H + 5 * WS_HS + row_in);
    STAMP(16);
    {
        float y[8];
        unpack8(pr, y);
#pragma unroll
        for (int j = 0; j < 8; ++j) y[j] += x[j];
        const u32x4 o = pack8(y);
        u16* dst = out + (8 * id) * DM + tid;
#pragma unroll
        for (int j = 0; j < 4; ++j) {
            dst[(2 * j) * DM] = (u16)o[j];
            dst[(2 * j + 1) * DM] = (u16)(o[j] >> 16);
        }
    }
    STAMP(17);
    asm volatile("h3s4_text_b:");
}

extern "C" void kernel_launch(void* const* d_in, const int* in_sizes, int n_in,
                              void* d_out, int out_size, void* d_ws, size_t ws_size,
                              hipStream_t stream) {
    h3s4_kernel<<<256, 512, 0, stream>>>(
        (const char*)d_in[0], (const char*)d_in[1], (const char*)d_in[2], (const u32x2*)d_in[3], (const u32x4*)d_in[4],
        (const u32*)d_in[5], (const u16*)d_in[7], (const u16*)d_in[8], (const u16*)d_in[9], (const u16*)d_in[10],
        (const u16*)d_in[11], (const u16*)d_in[12], (const u16*)d_in[13], (const u32*)d_in[14], (u16*)d_out, (char*)d_ws
        );
}
```

```cpp
#define DIAG 0
#define DIAG_A 0
#define DIAG_B 0
#include <hip/hip_runtime.h>
#include <hip/hip_bf16.h>
#include <stdint.h>

typedef unsigned int u32;
typedef unsigned short u16;
typedef unsigned long long u64;
typedef __attribute__((ext_vector_type(4))) float f32x4;
typedef __attribute__((ext_vector_type(2))) float f32x2;
typedef __attribute__((ext_vector_type(4))) u32 u32x4;
typedef __attribute__((ext_vector_type(2))) u32 u32x2;
typedef __attribute__((ext_vector_type(4))) short s16x4;
typedef __attribute__((ext_vector_type(8))) __bf16 bf16x8;
typedef __attribute__((ext_vector_type(2))) __bf16 bf16x2;
#define LAS __attribute__((address_space(3)))
#define GAS __attribute__((address_space(1)))
#define RLX_AGENT __ATOMIC_RELAXED, __HIP_MEMORY_SCOPE_AGENT
#define SB() __builtin_amdgcn_sched_barrier(0)
#define BAR() asm volatile("s_waitcnt lgkmcnt(0)\n\ts_barrier" ::: "memory")
#define MEMFENCE() asm volatile("" ::: "memory")
#define WAIT_VM(n) do { MEMFENCE(); __builtin_amdgcn_s_waitcnt(0x0F70 | ((n) & 15) | (((n) >> 4) << 14)); MEMFENCE(); } while (0)

constexpr int DM = 512;
constexpr int NS = 128;
constexpr u32 SENT = 0xffffffffu;
constexpr u32 SPIN_CAP = 20000;
#define POLL_DELAY 50

constexpr u32 WS_EPOCH = 0;
constexpr u32 WS_FLAGS = 4096;
constexpr u32 WS_H = 1u << 20;
constexpr u32 WS_HS = 2u << 20;
constexpr u32 WS_USED = WS_H + 6 * WS_HS;

constexpr int T_COPY = 4160, T_CH = 8 * T_COPY;
constexpr int U_STRIDE = 4672;
constexpr int R_ROW = 1088;
constexpr int OFF_T = 0;
constexpr int OFF_U = OFF_T + 2 * T_CH;
constexpr int OFF_X = OFF_U + 2 * U_STRIDE;
constexpr int OFF_Y = OFF_X + 16384;
constexpr int OFF_R = OFF_Y + 8192;
constexpr int OFF_P = OFF_R;
constexpr int OFF_KP = OFF_R + 4096;
constexpr int KP_WAVE = 2576;
constexpr int OFF_KC = OFF_KP + 8 * KP_WAVE;
constexpr int OFF_RED = OFF_R + 28 * R_ROW;
constexpr int OFF_ST = OFF_RED + 1024;
constexpr int LDS_BYTES = OFF_ST + 256;
static_assert(OFF_KC + 2 * 648 * 2 <= OFF_RED, "table alias");

#define STAMP(i) do { if (threadIdx.x == 0) ((u64*)(smem + OFF_ST))[i] = __builtin_amdgcn_s_memrealtime(); } while (0)

__device__ __forceinline__ float bf_lo(u32 p) { return __builtin_bit_cast(float, p << 16); }
__device__ __forceinline__ float bf_hi(u32 p) { return __builtin_bit_cast(float, p & 0xffff0000u); }
__device__ __forceinline__ float bf_u16(u16 p) { return __builtin_bit_cast(float, (u32)p << 16); }
__device__ __forceinline__ u32 pk_bf(float lo, float hi) {
    bf16x2 v = {(__bf16)lo, (__bf16)hi};
    return __builtin_bit_cast(u32, v);
}
__device__ __forceinline__ float as_f(u32 x) { return __builtin_bit_cast(float, x); }
__device__ __forceinline__ u32 as_u(float x) { return __builtin_bit_cast(u32, x); }
__device__ __forceinline__ float rbf(float x) { return bf_lo(pk_bf(x, x)); }
__device__ __forceinline__ void unpack8(u32x4 p, float (&f)[8]) {
#pragma unroll
    for (int i = 0; i < 4; ++i) { f[2 * i] = bf_lo(p[i]); f[2 * i + 1] = bf_hi(p[i]); }
}
__device__ __forceinline__ u32x4 pack8(const float (&f)[8]) {
    return (u32x4){pk_bf(f[0], f[1]), pk_bf(f[2], f[3]), pk_bf(f[4], f[5]), pk_bf(f[6], f[7])};
}
template <int CTRL> __device__ __forceinline__ float dppf(float v) {
    return __builtin_bit_cast(float, __builtin_amdgcn_update_dpp(0, __builtin_bit_cast(int, v), CTRL, 0xf, 0xf, true));
}

__device__ __forceinline__ void lds_dma16(const char* src, u32 lds) {
    u32 keep;
    asm volatile("s_mov_b32 %0, m0\n\ts_mov_b32 m0, %2\n\ts_nop 0\n\tglobal_load_lds_dwordx4 %1, off\n\ts_mov_b32 m0, %0"
                 : "=&s"(keep) : "v"(src), "s"(lds) : "memory");
}

typedef __amdgpu_buffer_rsrc_t rsrc_t;
__device__ __forceinline__ u32x4 ld_dev(rsrc_t rs, u32 off) { return __builtin_amdgcn_raw_buffer_load_b128(rs, off, 0, 16); }
__device__ __forceinline__ void st_dev(rsrc_t rs, u32 off, u32x4 v) { __builtin_amdgcn_raw_buffer_store_b128(v, rs, off, 0, 16); }
__device__ __forceinline__ void publish(rsrc_t rs, u32 off, u32x4 v) {
#pragma unroll
    for (int i = 0; i < 4; ++i) v[i] = v[i] < SENT - 1u ? v[i] : SENT - 1u;
    st_dev(rs, off, v);
}
__device__ __forceinline__ void wait_until(u64 t) {
    while (__builtin_amdgcn_s_memrealtime() < t) __builtin_amdgcn_s_sleep(1);
}
__device__ __forceinline__ u32x4 poll_issue(rsrc_t rs, u32 off) {
    MEMFENCE();
    u32x4 x = ld_dev(rs, off);
    MEMFENCE();
    return x;
}
__device__ __forceinline__ void poll_finish(u32x4& x, rsrc_t rs, u32 off) {
    u32 spins = 0;
    while (true) {
        u32 mx = x[0] > x[1] ? x[0] : x[1];
        mx = mx > x[2] ? mx : x[2];
        mx = mx > x[3] ? mx : x[3];
        const bool bad = mx == SENT;
        if (__builtin_amdgcn_ballot_w64(bad) == 0ull) break;
        if (++spins > SPIN_CAP) break;
        __builtin_amdgcn_s_sleep(1);
        MEMFENCE();
        if (bad) x = ld_dev(rs, off);
        MEMFENCE();
    }
}

__device__ __forceinline__ void flags_finish(u32x4& fl, rsrc_t rs, u32 off, u32 epoch) {
    u32 spins = 0;
    while (__builtin_amdgcn_ballot_w64(fl[0] != epoch || fl[1] != epoch || fl[2] != epoch || fl[3] != epoch) != 0ull) {
        if (++spins > SPIN_CAP) break;
        __builtin_amdgcn_s_sleep(2);
        fl = poll_issue(rs, off);
    }
}

__device__ __forceinline__ void row_stats(const float (&x)[8], float* red, int lane, int wave, float& mean, float& rstd) {
    u32 a[8], b[8];
#pragma unroll
    for (int j = 0; j < 8; ++j) { a[j] = as_u(x[j]); b[j] = as_u(x[j] * x[j]); asm volatile("" : "+v"(a[j]), "+v"(b[j])); }
    SB();
    float r[8];
#pragma unroll
    for (int j = 0; j < 8; ++j) {
        const auto s = __builtin_amdgcn_permlane32_swap(a[j], b[j], false, false);
        a[j] = s[0]; b[j] = s[1];
    }
    SB();
#pragma unroll
    for (int j = 0; j < 8; ++j) r[j] = as_f(a[j]) + as_f(b[j]);
    SB();
    float s4[4];
#pragma unroll
    for (int j = 0; j < 4; ++j) {
        const auto s = __builtin_amdgcn_permlane16_swap(as_u(r[j]), as_u(r[j + 4]), false, false);
        a[j] = s[0]; b[j] = s[1];
    }
    SB();
#pragma unroll
    for (int j = 0; j < 4; ++j) s4[j] = as_f(a[j]) + as_f(b[j]);
    SB();
#pragma unroll
    for (int j = 0; j < 4; ++j) s4[j] += dppf<0x128>(s4[j]);
    const float t0 = (lane & 8) ? s4[2] : s4[0], t1 = (lane & 8) ? s4[3] : s4[1];
    const float c0 = t0 + dppf<0x141>(t0), c1 = t1 + dppf<0x141>(t1);
    float w = (lane & 4) ? c1 : c0;
    w += dppf<0xB1>(w);
    w += dppf<0x4E>(w);
    if ((lane & 3) == 0) red[(lane >> 2) * 8 + wave] = w;
    BAR();
    const f32x4 p0 = *(const f32x4*)(red + (lane & 15) * 8), p1 = *(const f32x4*)(red + (lane & 15) * 8 + 4);
    const f32x4 p = p0 + p1;
    const float m = ((p[0] + p[1]) + (p[2] + p[3])) * (1.0f / DM);
    const float ex2 = dppf<0x128>(m);
    const float var = fmaxf(fmaf(-m, m, ex2), 0.f);
    const float sd = rbf(__builtin_amdgcn_sqrtf(rbf(rbf(var) + 1e-5f)));
    mean = rbf(m);
    rstd = __builtin_amdgcn_rcpf(sd);
}
__device__ __forceinline__ void layer_norm(const float (&x)[8], float (&u)[8], float* red, int lane, int wave) {
    float mean, rstd;
    row_stats(x, red, lane, wave, mean, rstd);
    asm volatile("s_nop 1\n\t"
                 "v_subrev_f32_dpp %0, %8, %10 row_newbcast:0 row_mask:0xf bank_mask:0xf\n\t"
                 "v_subrev_f32_dpp %1, %8, %11 row_newbcast:1 row_mask:0xf bank_mask:0xf\n\t"
                 "v_subrev_f32_dpp %2, %8, %12 row_newbcast:2 row_mask:0xf bank_mask:0xf\n\t"
                 "v_subrev_f32_dpp %3, %8, %13 row_newbcast:3 row_mask:0xf bank_mask:0xf\n\t"
                 "v_subrev_f32_dpp %4, %8, %14 row_newbcast:4 row_mask:0xf bank_mask:0xf\n\t"
                 "v_subrev_f32_dpp %5, %8, %15 row_newbcast:5 row_mask:0xf bank_mask:0xf\n\t"
                 "v_subrev_f32_dpp %6, %8, %16 row_newbcast:6 row_mask:0xf bank_mask:0xf\n\t"
                 "v_subrev_f32_dpp %7, %8, %17 row_newbcast:7 row_mask:0xf bank_mask:0xf\n\t"
                 "v_mul_f32_dpp %0, %9, %0 row_newbcast:0 row_mask:0xf bank_mask:0xf\n\t"
                 "v_mul_f32_dpp %1, %9, %1 row_newbcast:1 row_mask:0xf bank_mask:0xf\n\t"
                 "v_mul_f32_dpp %2, %9, %2 row_newbcast:2 row_mask:0xf bank_mask:0xf\n\t"
                 "v_mul_f32_dpp %3, %9, %3 row_newbcast:3 row_mask:0xf bank_mask:0xf\n\t"
                 "v_mul_f32_dpp %4, %9, %4 row_newbcast:4 row_mask:0xf bank_mask:0xf\n\t"
                 "v_mul_f32_dpp %5, %9, %5 row_newbcast:5 row_mask:0xf bank_mask:0xf\n\t"
                 "v_mul_f32_dpp %6, %9, %6 row_newbcast:6 row_mask:0xf bank_mask:0xf\n\t"
                 "v_mul_f32_dpp %7, %9, %7 row_newbcast:7 row_mask:0xf bank_mask:0xf"
                 : "=&v"(u[0]), "=&v"(u[1]), "=&v"(u[2]), "=&v"(u[3]), "=&v"(u[4]), "=&v"(u[5]), "=&v"(u[6]), "=&v"(u[7])
                 : "v"(mean), "v"(rstd), "v"(x[0]), "v"(x[1]), "v"(x[2]), "v"(x[3]), "v"(x[4]), "v"(x[5]), "v"(x[6]), "v"(x[7]));
}

__device__ __forceinline__ void stage_taps(char* smem, int tid, u32x4 prev, u32x4 own) {
    const u32 D[8] = {prev[0], prev[1], prev[2], prev[3], own[0], own[1], own[2], own[3]};
    u32 E[8], O[8];
#pragma unroll
    for (int y = 1; y < 8; ++y) {
        E[y] = __builtin_amdgcn_alignbit(D[y], D[y], 16);
        O[y] = (D[y] & 0xffffu) | (D[y - 1] & 0xffff0000u);
    }
    char* base = smem + OFF_T + (tid >> 8) * T_CH + (255 - (tid & 255)) * 16;
#pragma unroll
    for (int s = 0; s < 8; ++s) {
        const int y = 7 - (s >> 1);
        const u32x4 v = (s & 1) ? (u32x4){O[y], O[y - 1], O[y - 2], O[y - 3]} : (u32x4){E[y], E[y - 1], E[y - 2], E[y - 3]};
        *(u32x4*)(base + s * T_COPY) = v;
    }
}

struct cplx2 { f32x2 r, i; };
__device__ __forceinline__ cplx2 csq(cplx2 a) { return {a.r * a.r - a.i * a.i, (a.r + a.r) * a.i}; }
__device__ __forceinline__ cplx2 cmul(cplx2 a, cplx2 b) { return {a.r * b.r - a.i * b.i, a.r * b.i + a.i * b.r}; }
__device__ __forceinline__ f32x4 mfma16(u32x4 a, u32x4 b, f32x4 c) {
    return __builtin_amdgcn_mfma_f32_16x16x32_bf16(__builtin_bit_cast(bf16x8, a), __builtin_bit_cast(bf16x8, b), c, 0, 0, 0);
}
__device__ __forceinline__ void kgen_step(const f32x4* P, int lane, f32x4 (&acc)[4], float& k2048) {
#pragma clang fp contract(fast)
    const int g = lane >> 4;
    const float pm = (float)(lane & 15);
    cplx2 b[2], W[2];
#pragma unroll
    for (int h = 0; h < 2; ++h) {
        const f32x4 p0 = P[4 * g + 2 * h], p1 = P[4 * g + 2 * h + 1];
        const f32x2 ex = (f32x2){p0[0], p1[0]} * pm, an = (f32x2){p0[1], p1[1]} * pm;
        const f32x2 mag = {__builtin_amdgcn_exp2f(ex[0]), __builtin_amdgcn_exp2f(ex[1])};
        const float f0 = __builtin_amdgcn_fractf(an[0]), f1 = __builtin_amdgcn_fractf(an[1]);
        b[h].r = mag * (f32x2){__builtin_amdgcn_cosf(f0), __builtin_amdgcn_cosf(f1)};
        b[h].i = mag * (f32x2){__builtin_amdgcn_sinf(f0), __builtin_amdgcn_sinf(f1)};
        W[h].r = (f32x2){p0[2], p1[2]};
        W[h].i = (f32x2){p0[3], p1[3]};
    }
    u32x4 A[4], B[4];
#pragma unroll
    for (int e = 0; e < 4; ++e) {
#pragma unroll
        for (int h = 0; h < 2; ++h) {
            B[e][2 * h] = pk_bf(b[h].r[0], -b[h].i[0]);
            B[e][2 * h + 1] = pk_bf(b[h].r[1], -b[h].i[1]);
            b[h] = csq(b[h]);
        }
    }
#pragma unroll
    for (int e = 0; e < 4; ++e) {
#pragma unroll
        for (int h = 0; h < 2; ++h) {
            const cplx2 x = cmul(W[h], b[h]);
            A[e][2 * h] = pk_bf(x.r[0], x.i[0]);
            A[e][2 * h + 1] = pk_bf(x.r[1], x.i[1]);
            if (e == 3) {
                const f32x2 z = x.r * b[h].r - x.i * b[h].i;
                k2048 += z[0] + z[1];
            } else b[h] = csq(b[h]);
        }
        acc[e] = mfma16(A[e], B[e], acc[e]);
    }
}

struct KState { f32x4 acc[4]; float k2048; };
__device__ __forceinline__ const f32x4* kgen_params(char* smem, int wave) {
    return (const f32x4*)(smem + OFF_P) + (wave >> 2) * 128 + (wave & 3) * 32;
}
__device__ __forceinline__ void kgen_save(char* smem, int wave, int lane, const KState& ks) {
    char* p = smem + OFF_KP + wave * KP_WAVE;
    *(f32x4*)(p + lane * 16) = ks.acc[0];
    if (lane >= 32) {
#pragma unroll
        for (int e = 1; e < 4; ++e) *(f32x4*)(p + 512 * e + lane * 16) = ks.acc[e];
    }
    const auto s = __builtin_amdgcn_permlane32_swap(as_u(ks.k2048), as_u(ks.k2048), false, false);
    const float h = as_f(s[0]) + as_f(s[1]);
    const auto t = __builtin_amdgcn_permlane16_swap(as_u(h), as_u(h), false, false);
    const float z = as_f(t[0]) + as_f(t[1]);
    if (lane == 8) *(float*)(p + 2560) = z;
}
__device__ __forceinline__ void kgen_load(const char* smem, int wave, int lane, KState& ks) {
    const char* p = smem + OFF_KP + wave * KP_WAVE;
    ks.acc[0] = *(const f32x4*)(p + lane * 16);
#pragma unroll
    for (int e = 1; e < 4; ++e) ks.acc[e] = *(const f32x4*)(p + 512 * e + (lane | 32) * 16);
    ks.k2048 = lane == 8 ? *(const float*)(p + 2560) : 0.f;
}
__device__ __forceinline__ void kgen_reduce(char* smem, int wave, int lane, u32 dskip) {
    const int c = wave >> 2, e = wave & 3, m = lane & 15, g = lane >> 4;
    const char* p = smem + OFF_KP + c * 4 * KP_WAVE + (e ? 512 * e + (lane | 32) * 16 : lane * 16);
    f32x4 s = (*(const f32x4*)p + *(const f32x4*)(p + KP_WAVE)) + (*(const f32x4*)(p + 2 * KP_WAVE) + *(const f32x4*)(p + 3 * KP_WAVE));
    u16* KC = (u16*)(smem + OFF_KC) + c * 648;
    if (e == 0 && lane == 0) s[0] += c ? bf_hi(dskip) : bf_lo(dskip);
    const u32 lo = pk_bf(s[0], s[1]), hi = pk_bf(s[2], s[3]);
    if (e == 0 || g >= 2) {
        u16* o = KC + 128 * e + 64 * g + m;
        o[0] = (u16)lo; o[16] = (u16)(lo >> 16); o[32] = (u16)hi; o[48] = (u16)(hi >> 16);
    }
    if (e == 3 && lane == 0) {
        const char* z = smem + OFF_KP + c * 4 * KP_WAVE + 2560;
        const float k = (*(const float*)z + *(const float*)(z + KP_WAVE)) + (*(const float*)(z + 2 * KP_WAVE) + *(const float*)(z + 3 * KP_WAVE));
        KC[640] = (u16)pk_bf(k, 0.f);
    }
}

struct Frags { u32x4 A[8], B[8]; };
template <bool INIT> __device__ __forceinline__ void mma(f32x4& acc, const u32x4& a, const u32x4& b) {
    if constexpr (INIT) asm volatile("v_mfma_f32_16x16x32_bf16 %0, %1, %2, 0" : "=&v"(acc) : "v"(a), "v"(b));
    else asm volatile("v_mfma_f32_16x16x32_bf16 %0, %1, %2, %0" : "+v"(acc) : "v"(a), "v"(b));
}
template <int I> __device__ __forceinline__ void load_one(Frags& f, const char* ap, const char* bp) {
    if constexpr (I == 0) f.A[0] = *(const u32x4*)(ap + 512 * 7);
    else if constexpr (I <= 8) f.B[I - 1] = *(const u32x4*)(bp + 512 * (I - 1));
    else f.A[I - 8] = *(const u32x4*)(ap + 512 * (7 - (I - 8)));
}
template <int I> __device__ __forceinline__ void load_all(Frags& f, const char* ap, const char* bp) {
    if constexpr (I < 16) { load_one<I>(f, ap, bp); SB(); load_all<I + 1>(f, ap, bp); }
}
template <int T0, int T1, int T2, int T3> struct TileSet {
    static constexpr int tile(int i) { return i == 0 ? T0 : i == 1 ? T1 : i == 2 ? T2 : T3; }
    static constexpr int walk(int I, bool want_tile) {
        int n = 0;
        for (int a = 0; a < 8; ++a)
            for (int j = 0; j < 4; ++j)
                if (tile(j) >= a) { if (n == I) return want_tile ? tile(j) : a; ++n; }
        return -1;
    }
};
constexpr int tri_a(int I) { int a = 0; while (I >= 8 - a) { I -= 8 - a; ++a; } return a; }
constexpr int tri_b(int I) { int a = 0; while (I >= 8 - a) { I -= 8 - a; ++a; } return I; }
template <int I, bool FIRST> __device__ __forceinline__ void class_step(const Frags& cur, Frags& nxt, f32x4 (&acc)[8], const char* ap, const char* bp) {
    if constexpr (I < 36) {
        constexpr int a = tri_a(I), b = tri_b(I);
        mma<FIRST && a == 0>(acc[a + b], cur.A[a], cur.B[b]);
        if constexpr ((I & 1) == 1 && (I >> 1) < 16) load_one<(I >> 1)>(nxt, ap, bp);
        SB();
        class_step<I + 1, FIRST>(cur, nxt, acc, ap, bp);
    }
}
template <class TS, int I, int TO, bool PRE> __device__ __forceinline__ void set_step(const Frags& cur, Frags& nxt, f32x4 (&acc)[8], const char* ap, const char* bp) {
    if constexpr (I < TO) {
        constexpr int t = TS::walk(I, true), a = TS::walk(I, false);
        mma<false>(acc[t], cur.A[a], cur.B[t - a]);
        if constexpr (PRE && I < 16) load_one<I>(nxt, ap, bp);
        SB();
        set_step<TS, I + 1, TO, PRE>(cur, nxt, acc, ap, bp);
    }
}
template <class TS, int J> __device__ __forceinline__ void tile_step(const Frags& cur, f32x4 (&acc)[8]) {
    constexpr int t = TS::tile(J);
#pragma unroll
    for (int a = 0; a <= t; ++a) { mma<false>(acc[t], cur.A[a], cur.B[t - a]); SB(); }
}
template <class TS, int J> __device__ __forceinline__ void tile_out(char* Y, f32x4 (&acc)[8], const f32x4 (&o)[4]) {
    constexpr int t = TS::tile(J);
    const f32x4 s = acc[t] + o[J];
    const u32x2 pk = {pk_bf(s[0], s[1]), pk_bf(s[2], s[3])};
    *(u32x2*)(Y + 512 * t) = pk;
    SB();
}
template <class SEND, class KEEP>
__device__ __forceinline__ void conv_half(char* smem, const u32x4 (&A0)[8], const char* ap, const char* bp, int wave, int lane, int ch) {
    f32x4 acc[8];
    Frags f0, f1;
#pragma unroll
    for (int a = 0; a < 8; ++a) f0.A[a] = A0[a];
#pragma unroll
    for (int b = 0; b < 8; ++b) { f0.B[b] = *(const u32x4*)(bp + 512 * b); SB(); }
    class_step<0, true>(f0, f1, acc, ap - 64, bp - 64);
    class_step<0, false>(f1, f0, acc, ap - 128, bp - 128);
    set_step<SEND, 0, 18, true>(f0, f1, acc, ap - 192, bp - 192);
    set_step<SEND, 0, 18, false>(f1, f0, acc, ap, bp);
    set_step<KEEP, 0, 4, false>(f0, f1, acc, ap, bp);
    asm volatile("s_nop 3" : "+v"(acc[SEND::tile(0)]), "+v"(acc[SEND::tile(1)]), "+v"(acc[SEND::tile(2)]), "+v"(acc[SEND::tile(3)]));
    f32x4* X = (f32x4*)(smem + OFF_X);
#pragma unroll
    for (int i = 0; i < 4; ++i) X[(wave * 4 + i) * 64 + lane] = acc[SEND::tile(i)];
    SB();
    set_step<KEEP, 4, 18, false>(f0, f1, acc, ap, bp);
    BAR();
    SB();
    f32x4 o[4];
#pragma unroll
    for (int i = 0; i < 4; ++i) o[i] = X[((wave ^ 1) * 4 + i) * 64 + lane];
    SB();
    char* Y = smem + OFF_Y + ch * 4096 + (16 * (lane & 15) + 4 * (lane >> 4)) * 2;
    tile_step<KEEP, 0>(f1, acc);
    tile_step<KEEP, 1>(f1, acc);
    tile_step<KEEP, 2>(f1, acc);
    asm volatile("s_nop 3" : "+v"(acc[KEEP::tile(0)]), "+v"(acc[KEEP::tile(1)]));
    tile_out<KEEP, 0>(Y, acc, o);
    tile_out<KEEP, 1>(Y, acc, o);
    tile_step<KEEP, 3>(f1, acc);
    asm volatile("" : "+v"(acc[KEEP::tile(2)]));
    tile_out<KEEP, 2>(Y, acc, o);
    asm volatile("s_nop 7" : "+v"(acc[KEEP::tile(3)]));
    tile_out<KEEP, 3>(Y, acc, o);
}
struct ConvAddr { const char* ap; const char* bp; };
__device__ __forceinline__ ConvAddr conv_addr(char* smem, int wave, int lane) {
    const int ch = (wave >> 1) & 1, hf = wave & 1;
    const int m = lane & 15, g = lane >> 4;
    ConvAddr c;
    c.ap = smem + OFF_T + ch * T_CH + ((7 - m) & 7) * T_COPY + (m < 8 ? 4048 : 4032) + 16 * g - 512 * 7 - 256 * hf;
    c.bp = smem + OFF_U + ch * U_STRIDE + 448 + 32 * m + 16 * g - 256 * hf;
    return c;
}
__device__ __forceinline__ void conv_preload(u32x4 (&A0)[8], const ConvAddr& c) {
#pragma unroll
    for (int a = 0; a < 8; ++a) A0[a] = *(const u32x4*)(c.ap + 512 * (7 - a));
}
template <bool FLAGS>
__device__ __forceinline__ void conv(char* smem, const u32x4 (&A0)[8], const ConvAddr& c, int wave, int lane, rsrc_t rs, int id, int fill0,
                                     u32 epoch) {
    if (wave < 4) {
        if ((wave & 1) == 0) conv_half<TileSet<1, 3, 4, 6>, TileSet<0, 2, 5, 7>>(smem, A0, c.ap, c.bp, wave, lane, wave >> 1);
        else conv_half<TileSet<0, 2, 5, 7>, TileSet<1, 3, 4, 6>>(smem, A0, c.ap, c.bp, wave, lane, wave >> 1);
    } else {
        if (fill0 >= 0) {
            const u32x4 sent = {SENT, SENT, SENT, SENT};
            const u32 off = WS_H + fill0 * WS_HS + id * 8192 + ((wave - 4) * 64 + lane) * 16;
            st_dev(rs, off, sent); st_dev(rs, off + 4096, sent);
            st_dev(rs, off + WS_HS, sent); st_dev(rs, off + WS_HS + 4096, sent);
        }
        BAR();
        if (FLAGS && wave == 7) {
            const u32 flag_off = WS_FLAGS + 32768 + (blockIdx.x & 31) * 1024 + lane * 16;
            u32x4 fl = poll_issue(rs, flag_off);
            flags_finish(fl, rs, flag_off, epoch);
        }
        if (fill0 >= 0) asm volatile("s_waitcnt vmcnt(0)" ::: "memory");
    }
    BAR();
}

__global__ void __launch_bounds__(512, 2)
h3s4_kernel(const char* __restrict__ q, const char* __restrict__ k, const char* __restrict__ v,
            const u32x2* __restrict__ shiftk, const u32x4* __restrict__ ssmk, const u32* __restrict__ ssmD,
            const u16* __restrict__ Are, const u16* __restrict__ Aim, const u16* __restrict__ Bre, const u16* __restrict__ Bim,
            const u16* __restrict__ Cre, const u16* __restrict__ Cim, const u16* __restrict__ logdt, const u32* __restrict__ Dskip,
            u16* __restrict__ out, char* ws
            ) {
    __shared__ __attribute__((aligned(16))) char smem[LDS_BYTES];
    const int tid = threadIdx.x, lane = tid & 63, wave = __builtin_amdgcn_readfirstlane(tid >> 6);
    const int id = (blockIdx.x & 7) * 32 + (blockIdx.x >> 3);
    const rsrc_t rs = __builtin_amdgcn_make_buffer_rsrc(ws, 0, WS_USED, 0x00020000);
    float* red = (float*)(smem + OFF_RED);
    STAMP(0);

    u32 code_pf = 0;
    {
        u64 pc; u32 len;
        asm volatile("s_getpc_b64 %0\nh3s4_text_a:\n\ts_mov_b32 %1, h3s4_text_b - h3s4_text_a" : "=s"(pc), "=s"(len));
        const u32 off = ((blockIdx.x >> 3) * 8 + wave) * 256 + lane * 4;
        if (off + 4 <= len) code_pf = *(const GAS u32*)(pc + off);
    }
    const u32x4 sent = {SENT, SENT, SENT, SENT};
    const u32 own_off = id * 8192 + tid * 16;
    u32 epoch;
    asm volatile("s_load_dword %0, %1, 0x0" : "=s"(epoch) : "s"(ws + WS_EPOCH));
    const u32 dA = ssmD[id], dB = Dskip[id];
    MEMFENCE(); SB();
    st_dev(rs, WS_H + 0 * WS_HS + own_off, sent);
    MEMFENCE(); SB();
#pragma unroll
    for (int s = 0; s < 4; ++s) {
        const int slot = wave + 8 * s;
        if (slot < 28) {
            int row = 8 * id + (slot < 12 ? slot - 4 : slot < 20 ? slot - 12 : slot - 20);
            row = row < 0 ? 0 : row;
            const char* src = slot < 12 ? k : slot < 20 ? v : q;
            lds_dma16(src + row * 1024 + lane * 16, (u32)(size_t)(LAS char*)(smem + OFF_R) + slot * R_ROW);
        }
    }
    const u32x2 sk = shiftk[tid];
    const int tch = tid >> 8, ti = tid & 255;
    u32x4 tap_own = ssmk[(2 * id + tch) * 256 + ti];
    u32x4 tap_prev = ssmk[(2 * id + tch) * 256 + (ti ? ti - 1 : 0)];
    const int pd = 2 * id + ((tid >> 7) & 1), po = pd * NS + (tid & 127);
    const u16 r_dt = logdt[pd], r_are = Are[po], r_aim = Aim[po], r_bre = Bre[po], r_bim = Bim[po], r_cre = Cre[po], r_cim = Cim[po];
    MEMFENCE(); SB();
    st_dev(rs, WS_H + 1 * WS_HS + own_off, sent);
    MEMFENCE(); SB();
    asm volatile("s_waitcnt vmcnt(13) lgkmcnt(0)\n\ts_barrier" : "+s"(epoch) :: "memory");
    epoch += 1u;
    if (tid < 32) __hip_atomic_store((GAS u32*)(ws + WS_FLAGS) + tid * 256 + id, epoch, RLX_AGENT);
    STAMP(19);
    if (tid < 60) *(u32x4*)(smem + OFF_U + (tid / 30) * U_STRIDE + (tid % 30) * 16) = (u32x4){0, 0, 0, 0};
    else if (tid >= 64 && tid < 80) *(u32x4*)(smem + OFF_T + (tid - 64) * T_COPY + 4096) = (u32x4){0, 0, 0, 0};
    if (wave == 0) WAIT_VM(2); else WAIT_VM(1);
    asm volatile("" :: "v"(code_pf));
    asm volatile("" :: "v"(sk), "v"(tap_own), "v"(tap_prev), "v"(r_dt), "v"(r_are), "v"(r_aim), "v"(r_bre), "v"(r_bim), "v"(r_cre), "v"(r_cim));
    BAR();
    STAMP(1);

    float x[8];
    u32x4 xq;
    {
        const u32 rp = (u32)(size_t)(LAS char*)(smem + OFF_R + ((lane & 15) >> 2) * R_ROW + (64 * wave + 16 * (lane >> 4) + 4 * (lane & 3)) * 2);
        u32x2 k0, k1, k2, v0, v1, q0, q1;
        static_assert(4 * R_ROW == 4352, "offsets below");
        asm volatile("ds_read_b64_tr_b16 %0, %7\n\tds_read_b64_tr_b16 %1, %7 offset:4352\n\tds_read_b64_tr_b16 %2, %7 offset:8704\n\t"
                     "ds_read_b64_tr_b16 %3, %7 offset:13056\n\tds_read_b64_tr_b16 %4, %7 offset:17408\n\t"
                     "ds_read_b64_tr_b16 %5, %7 offset:21760\n\tds_read_b64_tr_b16 %6, %7 offset:26112\n\ts_waitcnt lgkmcnt(0)"
                     : "=&v"(k0), "=&v"(k1), "=&v"(k2), "=&v"(v0), "=&v"(v1), "=&v"(q0), "=&v"(q1) : "v"(rp) : "memory");
        const u32 first = id ? 0xffffffffu : 0u;
        const u32 kp = k0[0] & first, kq = k0[1] & first;
        const float kf[12] = {0.f, bf_hi(kp), bf_lo(kq), bf_hi(kq), bf_lo(k1[0]), bf_hi(k1[0]),
                              bf_lo(k1[1]), bf_hi(k1[1]), bf_lo(k2[0]), bf_hi(k2[0]), bf_lo(k2[1]), bf_hi(k2[1])};
        float vf[8];
        unpack8((u32x4){v0[0], v0[1], v1[0], v1[1]}, vf);
        const float w0 = bf_lo(sk[0]), w1 = bf_hi(sk[0]), w2 = bf_lo(sk[1]), w3 = bf_hi(sk[1]);
        float kv[8];
#pragma unroll
        for (int j = 0; j < 8; ++j) kv[j] = fmaf(kf[j + 4], w0, fmaf(kf[j + 3], w1, fmaf(kf[j + 2], w2, kf[j + 1] * w3))) * vf[j];
        publish(rs, WS_H + 0 * WS_HS + own_off, pack8(kv));
        xq = (u32x4){q0[0], q0[1], q1[0], q1[1]};
    }
    u64 t_s = __builtin_amdgcn_s_memrealtime();
    STAMP(2);
    u32 touch = 0;
    if (wave == 6) {
        if (lane < 4) touch = __builtin_amdgcn_raw_buffer_load_b32(rs, WS_H + (2 + lane) * WS_HS + id * 8192, 0, 16);
        else if (lane == 4) touch = ((const u32*)out)[id * 2048];
    }

    const u32 flag_off = WS_FLAGS + (blockIdx.x & 31) * 1024 + lane * 16;
    u32x4 fl = {epoch, epoch, epoch, epoch};
    if (wave == 7) fl = poll_issue(rs, flag_off);
    if (ti == 0) {
        tap_prev = (u32x4){0, 0, 0, 0};
        tap_own[0] = (tap_own[0] & 0xffff0000u) | (pk_bf(bf_lo(tap_own[0]) + (tch ? bf_hi(dA) : bf_lo(dA)), 0.f) & 0xffffu);
    }
    stage_taps(smem, tid, tap_prev, tap_own);
    f32x4 prm;
    {
        const float dt = rbf(expf(bf_u16(r_dt)));
        const float are = dt * bf_u16(r_are), aim = dt * bf_u16(r_aim);
        const float br = dt * bf_u16(r_bre), bi = dt * bf_u16(r_bim);
        const float cr = bf_u16(r_cre), ci = bf_u16(r_cim);
        prm = (f32x4){are * 1.4426950408889634f, aim * 0.15915494309189535f, cr * br - ci * bi, cr * bi + ci * br};
        asm volatile("" : "+v"(prm));
    }
    if (wave == 7) {
        flags_finish(fl, rs, flag_off, epoch);
        if (blockIdx.x == 0 && lane == 0) __hip_atomic_store((GAS u32*)(ws + WS_EPOCH), epoch, RLX_AGENT);
    }
    BAR();
    if (tid < 256) ((f32x4*)(smem + OFF_P))[tid] = prm;
    STAMP(3);

    const u32 col_in = (tid >> 1) * 8192 + (2 * id + (tid & 1)) * 16;
    const u32 row_in = (tid >> 1) * 8192 + id * 32 + (tid & 1) * 16;
    char* const u_dst = smem + OFF_U + (tid & 1) * U_STRIDE + 480 + (tid >> 1) * 16;
    const char* const y_src = smem + OFF_Y + (tid & 1) * 4096 + (tid >> 1) * 16;
    const ConvAddr ca = conv_addr(smem, wave, lane);
    u32x4 A0[8];

    wait_until(t_s + POLL_DELAY);
    u32x4 pc = poll_issue(rs, WS_H + 0 * WS_HS + col_in);
    BAR();
    {
        KState ks;
#pragma unroll
        for (int e = 0; e < 4; ++e) ks.acc[e] = (f32x4){0, 0, 0, 0};
        ks.k2048 = 0.f;
        kgen_step(kgen_params(smem, wave), lane, ks.acc, ks.k2048);
        kgen_save(smem, wave, lane, ks);
    }
    STAMP(4);
    if (wave < 4) conv_preload(A0, ca);
    poll_finish(pc, rs, WS_H + 0 * WS_HS + col_in);
    *(u32x4*)u_dst = pc;
    asm volatile("" :: "v"(touch));
    BAR();
    if (wave == 7 && lane < 32) __hip_atomic_store((GAS u32*)(ws + WS_FLAGS + 32768) + lane * 256 + id, epoch, RLX_AGENT);
    STAMP(5);

    conv<true>(smem, A0, ca, wave, lane, rs, id, 2, epoch);
    STAMP(6);
    publish(rs, WS_H + 1 * WS_HS + own_off, *(const u32x4*)y_src);
    t_s = __builtin_amdgcn_s_memrealtime();

    float u[8];
    unpack8(xq, x);
    layer_norm(x, u, red, lane, wave);
    const u32x4 u0q = pack8(u);
    wait_until(t_s + POLL_DELAY);
    u32x4 pr = poll_issue(rs, WS_H + 1 * WS_HS + row_in);
    {
        KState ks;
        kgen_load(smem, wave, lane, ks);
        kgen_step(kgen_params(smem, wave) + 16, lane, ks.acc, ks.k2048);
        kgen_save(smem, wave, lane, ks);
    }
    STAMP(7);

    poll_finish(pr, rs, WS_H + 1 * WS_HS + row_in);
    STAMP(8);
    float yA[8];
    unpack8(pr, yA);
    unpack8(u0q, u);
#pragma unroll
    for (int j = 0; j < 8; ++j) u[j] = fmaf(u[j], yA[j], x[j]);
    unpack8(pack8(u), x);
    layer_norm(x, u, red + 128, lane, wave);
    publish(rs, WS_H + 2 * WS_HS + own_off, pack8(u));
    t_s = __builtin_amdgcn_s_memrealtime();
    STAMP(9);

    {
        kgen_reduce(smem, wave, lane, dB);
        BAR();
        const u16* KC = (const u16*)(smem + OFF_KC);
        u32 d[8];
#pragma unroll
        for (int j = 0; j < 8; ++j) {
            const int l0 = 8 * ti - 8 + 2 * j;
            const u32 bits = pk_bf((float)l0, (float)(l0 + 1));
            const int i0 = l0 < 128 ? (l0 < 0 ? 0 : l0) : (int)(bits & 0xffffu) - 17024;
            const int i1 = l0 < 128 ? (l0 < 0 ? 0 : l0 + 1) : (int)(bits >> 16) - 17024;
            const u32 a = KC[tch * 648 + i0], b = KC[tch * 648 + i1];
            d[j] = l0 < 0 ? 0u : (a | (b << 16));
        }
        wait_until(t_s + POLL_DELAY);
        pc = poll_issue(rs, WS_H + 2 * WS_HS + col_in);
        stage_taps(smem, tid, (u32x4){d[0], d[1], d[2], d[3]}, (u32x4){d[4], d[5], d[6], d[7]});
        BAR();
    }

    if (wave < 4) conv_preload(A0, ca);
    poll_finish(pc, rs, WS_H + 2 * WS_HS + col_in);
    *(u32x4*)u_dst = pc;
    BAR();
    STAMP(10);
    conv<false>(smem, A0, ca, wave, lane, rs, id, 4, epoch);
    STAMP(11);
    publish(rs, WS_H + 3 * WS_HS + own_off, *(const u32x4*)y_src);
    t_s = __builtin_amdgcn_s_memrealtime();

    wait_until(t_s + POLL_DELAY);
    pr = poll_issue(rs, WS_H + 3 * WS_HS + row_in);
    poll_finish(pr, rs, WS_H + 3 * WS_HS + row_in);
    STAMP(12);
    {
        float y[8];
        unpack8(pr, y);
#pragma unroll
        for (int j = 0; j < 8; ++j) y[j] += x[j];
        unpack8(pack8(y), x);
        layer_norm(x, u, red, lane, wave);
#pragma unroll
        for (int j = 0; j < 8; ++j) u[j] = fmaf(u[j], yA[j], x[j]);
        unpack8(pack8(u), x);
        layer_norm(x, u, red + 128, lane, wave);
    }
    publish(rs, WS_H + 4 * WS_HS + own_off, pack8(u));
    t_s = __builtin_amdgcn_s_memrealtime();
    STAMP(13);

    wait_until(t_s + POLL_DELAY);
    pc = poll_issue(rs, WS_H + 4 * WS_HS + col_in);
    if (wave < 4) conv_preload(A0, ca);
    poll_finish(pc, rs, WS_H + 4 * WS_HS + col_in);
    *(u32x4*)u_dst = pc;
    BAR();
    STAMP(14);
    conv<false>(smem, A0, ca, wave, lane, rs, id, -1, epoch);
    STAMP(15);
    publish(rs, WS_H + 5 * WS_HS + own_off, *(const u32x4*)y_src);
    t_s = __builtin_amdgcn_s_memrealtime();

    wait_until(t_s + POLL_DELAY);
    pr = poll_issue(rs, WS_H + 5 * WS_HS + row_in);
    poll_finish(pr, rs, WS_H + 5 * WS_HS + row_in);
    STAMP(16);
    {
        float y[8];
        unpack8(pr, y);
#pragma unroll
        for (int j = 0; j < 8; ++j) y[j] += x[j];
        const u32x4 o = pack8(y);
        u16* dst = out + (8 * id) * DM + tid;
#pragma unroll
        for (int j = 0; j < 4; ++j) {
            dst[(2 * j) * DM] = (u16)o[j];
            dst[(2 * j + 1) * DM] = (u16)(o[j] >> 16);
        }
    }
    STAMP(17);
    asm volatile("h3s4_text_b:");
    asm volatile("s_waitcnt vmcnt(0)" ::: "memory");
    STAMP(18);
    BAR();
    if (tid == 0) {
        const u64* st = (const u64*)(smem + OFF_ST);
        const u64 dur = st[DIAG_B] - st[DIAG_A];
        const u64 until = __builtin_amdgcn_s_memrealtime() + DIAG * dur;
        while (__builtin_amdgcn_s_memrealtime() < until) __builtin_amdgcn_s_sleep(2);
    }
}

extern "C" void kernel_launch(void* const* d_in, const int* in_sizes, int n_in,
                              void* d_out, int out_size, void* d_ws, size_t ws_size,
                              hipStream_t stream) {
    h3s4_kernel<<<256, 512, 0, stream>>>(
        (const char*)d_in[0], (const char*)d_in[1], (const char*)d_in[2], (const u32x2*)d_in[3], (const u32x4*)d_in[4],
        (const u32*)d_in[5], (const u16*)d_in[7], (const u16*)d_in[8], (const u16*)d_in[9], (const u16*)d_in[10],
        (const u16*)d_in[11], (const u16*)d_in[12], (const u16*)d_in[13], (const u32*)d_in[14], (u16*)d_out, (char*)d_ws
        );
}
```

```cpp
#include <hip/hip_runtime.h>
#include <hip/hip_bf16.h>
#include <stdint.h>

typedef unsigned int u32;
typedef unsigned short u16;
typedef unsigned long long u64;
typedef __attribute__((ext_vector_type(4))) float f32x4;
typedef __attribute__((ext_vector_type(2))) float f32x2;
typedef __attribute__((ext_vector_type(4))) u32 u32x4;
typedef __attribute__((ext_vector_type(2))) u32 u32x2;
typedef __attribute__((ext_vector_type(4))) short s16x4;
typedef __attribute__((ext_vector_type(8))) __bf16 bf16x8;
typedef __attribute__((ext_vector_type(2))) __bf16 bf16x2;
#define LAS __attribute__((address_space(3)))
#define GAS __attribute__((address_space(1)))
#define RLX_AGENT __ATOMIC_RELAXED, __HIP_MEMORY_SCOPE_AGENT
#define SB() __builtin_amdgcn_sched_barrier(0)
#define BAR() asm volatile("s_waitcnt lgkmcnt(0)\n\ts_barrier" ::: "memory")
#define MEMFENCE() asm volatile("" ::: "memory")
#define WAIT_VM(n) do { MEMFENCE(); __builtin_amdgcn_s_waitcnt(0x0F70 | ((n) & 15) | (((n) >> 4) << 14)); MEMFENCE(); } while (0)

constexpr int DM = 512;
constexpr int NS = 128;
constexpr u32 SENT = 0xffffffffu;
constexpr u32 SPIN_CAP = 20000;
#define FILL_GAP 70
#define R_STEPS 1
#define POLL_DELAY 50

constexpr u32 WS_EPOCH = 0;
constexpr u32 WS_FLAGS = 4096;
constexpr u32 WS_H = 1u << 20;
constexpr u32 WS_HS = 2u << 20;
constexpr u32 WS_USED = WS_H + 6 * WS_HS;

constexpr int T_COPY = 4160, T_CH = 8 * T_COPY;
constexpr int U_STRIDE = 4672;
constexpr int R_ROW = 1088;
constexpr int KP_WAVE = 2816;
constexpr int OFF_T = 0;
constexpr int OFF_U = OFF_T + 2 * T_CH;
constexpr int OFF_X = OFF_U + 2 * U_STRIDE;
constexpr int OFF_Y = OFF_X + 16384;
constexpr int OFF_R = OFF_Y + 8192;
constexpr int OFF_P = OFF_R + 28 * R_ROW;
constexpr int OFF_KP = OFF_P + 4096;
constexpr int OFF_KC = OFF_KP + 8 * KP_WAVE;
constexpr int OFF_RED = OFF_KC + 2 * 648 * 2;
constexpr int OFF_CNT = OFF_RED + 1024;
constexpr int OFF_ST = OFF_CNT + 64;
constexpr int LDS_BYTES = OFF_ST + 512;
static_assert(LDS_BYTES <= 163840, "LDS");

#define STAMP(i)

__device__ __forceinline__ float bf_lo(u32 p) { return __builtin_bit_cast(float, p << 16); }
__device__ __forceinline__ float bf_hi(u32 p) { return __builtin_bit_cast(float, p & 0xffff0000u); }
__device__ __forceinline__ float bf_u16(u16 p) { return __builtin_bit_cast(float, (u32)p << 16); }
__device__ __forceinline__ u32 pk_bf(float lo, float hi) {
    bf16x2 v = {(__bf16)lo, (__bf16)hi};
    return __builtin_bit_cast(u32, v);
}
__device__ __forceinline__ float as_f(u32 x) { return __builtin_bit_cast(float, x); }
__device__ __forceinline__ u32 as_u(float x) { return __builtin_bit_cast(u32, x); }
__device__ __forceinline__ float rbf(float x) { return bf_lo(pk_bf(x, x)); }
__device__ __forceinline__ void unpack8(u32x4 p, float (&f)[8]) {
#pragma unroll
    for (int i = 0; i < 4; ++i) { f[2 * i] = bf_lo(p[i]); f[2 * i + 1] = bf_hi(p[i]); }
}
__device__ __forceinline__ u32x4 pack8(const float (&f)[8]) {
    return (u32x4){pk_bf(f[0], f[1]), pk_bf(f[2], f[3]), pk_bf(f[4], f[5]), pk_bf(f[6], f[7])};
}
__device__ __forceinline__ u32x4 reverse8(u32x4 p) {
    return (u32x4){__builtin_amdgcn_alignbit(p[3], p[3], 16), __builtin_amdgcn_alignbit(p[2], p[2], 16),
                   __builtin_amdgcn_alignbit(p[1], p[1], 16), __builtin_amdgcn_alignbit(p[0], p[0], 16)};
}
template <int CTRL> __device__ __forceinline__ float dppf(float v) {
    return __builtin_bit_cast(float, __builtin_amdgcn_update_dpp(0, __builtin_bit_cast(int, v), CTRL, 0xf, 0xf, true));
}

__device__ __forceinline__ void lds_dma16(const char* src, u32 lds) {
    u32 keep;
    asm volatile("s_mov_b32 %0, m0\n\ts_mov_b32 m0, %2\n\ts_nop 0\n\tglobal_load_lds_dwordx4 %1, off\n\ts_mov_b32 m0, %0"
                 : "=&s"(keep) : "v"(src), "s"(lds) : "memory");
}

__device__ __forceinline__ u32 ld_u16(const void* base, u32 off) {
    u32 r;
    asm volatile("global_load_ushort %0, %1, %2" : "=v"(r) : "v"(off), "s"(base) : "memory");
    return r;
}
__device__ __forceinline__ u32x2 ld_b64(const void* base, u32 off) {
    u32x2 r;
    asm volatile("global_load_dwordx2 %0, %1, %2" : "=v"(r) : "v"(off), "s"(base) : "memory");
    return r;
}
__device__ __forceinline__ u32x4 ld_b128(const void* base, u32 off) {
    u32x4 r;
    asm volatile("global_load_dwordx4 %0, %1, %2" : "=v"(r) : "v"(off), "s"(base) : "memory");
    return r;
}

typedef __amdgpu_buffer_rsrc_t rsrc_t;
__device__ __forceinline__ u32x4 ld_dev(rsrc_t rs, u32 off) { return __builtin_amdgcn_raw_buffer_load_b128(rs, off, 0, 16); }
__device__ __forceinline__ void st_dev(rsrc_t rs, u32 off, u32x4 v) { __builtin_amdgcn_raw_buffer_store_b128(v, rs, off, 0, 16); }
__device__ __forceinline__ void publish(rsrc_t rs, u32 off, u32x4 v) {
#pragma unroll
    for (int i = 0; i < 4; ++i) v[i] = v[i] < SENT - 1u ? v[i] : SENT - 1u;
    st_dev(rs, off, v);
}
__device__ __forceinline__ u32 launch_tag(const char* ws) {
    u32 e;
    asm volatile("s_load_dword %0, %1, 0x0\n\ts_waitcnt lgkmcnt(0)" : "=&s"(e) : "s"(ws + WS_EPOCH) : "memory");
    return e + 1u;
}
__device__ __forceinline__ void wait_until(u64 t) {
    while (__builtin_amdgcn_s_memrealtime() < t) __builtin_amdgcn_s_sleep(1);
}
__device__ __forceinline__ u32x4 poll_issue(rsrc_t rs, u32 off) {
    MEMFENCE();
    u32x4 x = ld_dev(rs, off);
    MEMFENCE();
    return x;
}
struct Piece2 { u32x4 a, b; };
__device__ __forceinline__ Piece2 poll2_issue(rsrc_t rs, u32 off_a, u32 off_b) {
    MEMFENCE();
    Piece2 p = {ld_dev(rs, off_a), ld_dev(rs, off_b)};
    MEMFENCE();
    return p;
}
__device__ __forceinline__ bool has_sentinel(const u32x4& x) {
    u32 mx = x[0] > x[1] ? x[0] : x[1];
    mx = mx > x[2] ? mx : x[2];
    mx = mx > x[3] ? mx : x[3];
    return mx == SENT;
}
__device__ __forceinline__ void poll2_finish(Piece2& p, u32x4 desc, u32 off_a, u32 off_b) {
#pragma nounroll
    for (u32 spins = 0; spins < SPIN_CAP; ++spins) {
        const bool bad = has_sentinel(p.a) || has_sentinel(p.b);
        if (__builtin_amdgcn_ballot_w64(bad) == 0ull) break;
        __builtin_amdgcn_s_sleep(1);
        if (bad) asm volatile("buffer_load_dwordx4 %0, %2, %4, 0 offen sc1\n\tbuffer_load_dwordx4 %1, %3, %4, 0 offen sc1\n\ts_waitcnt vmcnt(0)"
                              : "+v"(p.a), "+v"(p.b) : "v"(off_a), "v"(off_b), "s"(desc) : "memory");
    }
}
__device__ __forceinline__ void flags_finish(u32x4& fl, rsrc_t rs, u32 off, u32 epoch) {
    u32 spins = 0;
    while (__builtin_amdgcn_ballot_w64(fl[0] != epoch || fl[1] != epoch || fl[2] != epoch || fl[3] != epoch) != 0ull) {
        if (++spins > SPIN_CAP) break;
        __builtin_amdgcn_s_sleep(2);
        fl = poll_issue(rs, off);
    }
}

__device__ __forceinline__ void cnt_arrive(char* smem, int k, int lane) {
    asm volatile("s_waitcnt lgkmcnt(0)" ::: "memory");
    if (lane == 0) __hip_atomic_fetch_add((LAS u32*)(smem + OFF_CNT) + k, 1u, __ATOMIC_RELAXED, __HIP_MEMORY_SCOPE_WORKGROUP);
}
__device__ __forceinline__ void cnt_wait(char* smem, int k, u32 target) {
    u32 spins = 0;
    while (__hip_atomic_load((LAS u32*)(smem + OFF_CNT) + k, __ATOMIC_RELAXED, __HIP_MEMORY_SCOPE_WORKGROUP) < target) {
        if (++spins > 50 * SPIN_CAP) break;
        __builtin_amdgcn_s_sleep(1);
    }
    MEMFENCE();
}

struct Rows { f32x2 a[4], b[4]; };
__device__ __forceinline__ void unpack_rows(u32x4 p, f32x2 (&f)[4]) {
#pragma unroll
    for (int i = 0; i < 4; ++i) f[i] = (f32x2){bf_lo(p[i]), bf_hi(p[i])};
}
__device__ __forceinline__ u32x4 pack_rows(const f32x2 (&f)[4]) {
    return (u32x4){pk_bf(f[0][0], f[0][1]), pk_bf(f[1][0], f[1][1]), pk_bf(f[2][0], f[2][1]), pk_bf(f[3][0], f[3][1])};
}
__device__ __forceinline__ u32x4 round_rows(f32x2 (&f)[4]) {
    u32x4 p = pack_rows(f);
    asm("" : "+v"(p));
    unpack_rows(p, f);
    return p;
}
__device__ __forceinline__ void layer_norm(const Rows& x, Rows& u, float* red, int lane, int w) {
#pragma clang fp contract(fast)
    u32 a[8], b[8];
#pragma unroll
    for (int i = 0; i < 4; ++i) {
        const f32x2 s1 = x.a[i] + x.b[i], s2 = x.a[i] * x.a[i] + x.b[i] * x.b[i];
        a[2 * i] = as_u(s1[0]); a[2 * i + 1] = as_u(s1[1]);
        b[2 * i] = as_u(s2[0]); b[2 * i + 1] = as_u(s2[1]);
    }
    SB();
#pragma unroll
    for (int j = 0; j < 8; ++j) {
        const auto s = __builtin_amdgcn_permlane32_swap(a[j], b[j], false, false);
        a[j] = s[0]; b[j] = s[1];
    }
    SB();
    float r[8];
#pragma unroll
    for (int j = 0; j < 8; ++j) r[j] = as_f(a[j]) + as_f(b[j]);
    SB();
#pragma unroll
    for (int j = 0; j < 4; ++j) {
        const auto s = __builtin_amdgcn_permlane16_swap(as_u(r[j]), as_u(r[j + 4]), false, false);
        a[j] = s[0]; b[j] = s[1];
    }
    SB();
    float s4[4];
#pragma unroll
    for (int j = 0; j < 4; ++j) s4[j] = as_f(a[j]) + as_f(b[j]);
    SB();
#pragma unroll
    for (int j = 0; j < 4; ++j) s4[j] += dppf<0x128>(s4[j]);
    const float t0 = (lane & 8) ? s4[2] : s4[0], t1 = (lane & 8) ? s4[3] : s4[1];
    const float c0 = t0 + dppf<0x141>(t0), c1 = t1 + dppf<0x141>(t1);
    float s = (lane & 4) ? c1 : c0;
    s += dppf<0xB1>(s);
    s += dppf<0x4E>(s);
    const u32 sb = as_u(s) < SENT - 1u ? as_u(s) : SENT - 1u;
    if ((lane & 3) == 0) ((volatile LAS u32*)(LAS char*)(char*)red)[(lane >> 2) * 4 + w] = sb;
    u32x4 p;
    u32 spins = 0;
    while (true) {
        p = *(volatile LAS u32x4*)((LAS char*)(char*)red + (lane & 15) * 16);
        u32 mx = p[0] > p[1] ? p[0] : p[1];
        mx = mx > p[2] ? mx : p[2];
        mx = mx > p[3] ? mx : p[3];
        if (__builtin_amdgcn_ballot_w64(mx == SENT) == 0ull) break;
        if (++spins > 50 * SPIN_CAP) break;
    }
    const float m = ((as_f(p[0]) + as_f(p[1])) + (as_f(p[2]) + as_f(p[3]))) * (1.0f / DM);
    const float ex2 = dppf<0x128>(m);
    const float rstd = __builtin_amdgcn_rsqf(fmaxf(fmaf(-m, m, ex2), 0.f) + 1e-5f);
    const float off = -m * rstd;
    const f32x2 R[4] = {{dppf<0x150>(rstd), dppf<0x151>(rstd)}, {dppf<0x152>(rstd), dppf<0x153>(rstd)},
                        {dppf<0x154>(rstd), dppf<0x155>(rstd)}, {dppf<0x156>(rstd), dppf<0x157>(rstd)}};
    const f32x2 O[4] = {{dppf<0x150>(off), dppf<0x151>(off)}, {dppf<0x152>(off), dppf<0x153>(off)},
                        {dppf<0x154>(off), dppf<0x155>(off)}, {dppf<0x156>(off), dppf<0x157>(off)}};
#pragma unroll
    for (int i = 0; i < 4; ++i) { u.a[i] = x.a[i] * R[i] + O[i]; u.b[i] = x.b[i] * R[i] + O[i]; }
}

__device__ __forceinline__ void stage_taps(char* smem, int ch, int i, u32x4 prev, u32x4 own) {
    const u32 D[8] = {prev[0], prev[1], prev[2], prev[3], own[0], own[1], own[2], own[3]};
    u32 O[7];
#pragma unroll
    for (int y = 0; y < 7; ++y) O[y] = __builtin_amdgcn_alignbit(D[y + 1], D[y], 16);
    char* base = smem + OFF_T + ch * T_CH + (i + 1) * 16;
#pragma unroll
    for (int c = 0; c < 8; ++c) {
        const int y = (c + 1) >> 1;
        const u32x4 v = (c & 1) ? (u32x4){D[y], D[y + 1], D[y + 2], D[y + 3]} : (u32x4){O[y], O[y + 1], O[y + 2], O[y + 3]};
        *(u32x4*)(base + c * T_COPY) = v;
    }
}

__device__ __forceinline__ f32x4 mfma16(u32x4 a, u32x4 b, f32x4 c) {
    return __builtin_amdgcn_mfma_f32_16x16x32_bf16(__builtin_bit_cast(bf16x8, a), __builtin_bit_cast(bf16x8, b), c, 0, 0, 0);
}
struct KState { f32x4 acc[3]; };
__device__ __forceinline__ void kgen_init(KState& ks) {
#pragma unroll
    for (int e = 0; e < 3; ++e) ks.acc[e] = (f32x4){0, 0, 0, 0};
}
#define OPQ(x) asm volatile("" : "+v"(x))
template <int NS_, bool PK>
__device__ __forceinline__ void kgen_steps(const f32x4* P, int lane, KState& ks) {
#pragma clang fp contract(fast)
    const int g = lane >> 4, m = lane & 15;
    const float lo = (float)(8 * m);
    const float hi[3] = {(float)(128 * (m >> 3) + (m & 7)),
                         (float)(m < 8 ? 256 + 128 * (m >> 2) + 2 * (m & 3) : 512 + 128 * ((m - 8) >> 1) + 4 * (m & 1)),
                         (float)(m < 8 ? 1024 + 128 * m : 2048)};
    auto eval = [&](f32x2 ex, f32x2 an, bool conj, u32& o0, u32& o1) {
        if (!PK) { OPQ(ex[0]); OPQ(ex[1]); OPQ(an[0]); OPQ(an[1]); }
        const float m0 = __builtin_amdgcn_exp2f(ex[0]), m1 = __builtin_amdgcn_exp2f(ex[1]);
        const float f0 = __builtin_amdgcn_fractf(an[0]), f1 = __builtin_amdgcn_fractf(an[1]);
        const float c0 = __builtin_amdgcn_cosf(f0), c1 = __builtin_amdgcn_cosf(f1);
        const float s0 = __builtin_amdgcn_sinf(f0), s1 = __builtin_amdgcn_sinf(f1);
        float r0, r1, i0, i1;
        if (PK) {
            const f32x2 r = (f32x2){c0, c1} * (f32x2){m0, m1}, i = (f32x2){s0, s1} * (f32x2){m0, m1};
            r0 = r[0]; r1 = r[1]; i0 = i[0]; i1 = i[1];
        } else {
            r0 = c0 * m0; OPQ(r0); i0 = s0 * m0; OPQ(i0); r1 = c1 * m1; OPQ(r1); i1 = s1 * m1; OPQ(i1);
        }
        o0 = pk_bf(r0, conj ? -i0 : i0);
        o1 = pk_bf(r1, conj ? -i1 : i1);
    };
    auto affine = [&](f32x2 a, float p, f32x2 b) -> f32x2 {
        if (PK) return a * p + b;
        float x = fmaf(a[0], p, b[0]), y = fmaf(a[1], p, b[1]);
        OPQ(x); OPQ(y);
        return (f32x2){x, y};
    };
#pragma unroll
    for (int s = 0; s < NS_; ++s) {
        u32x4 B, A[3];
#pragma unroll
        for (int h = 0; h < 2; ++h) {
            const f32x4 p0 = P[16 * s + 4 * g + 2 * h], p1 = P[16 * s + 4 * g + 2 * h + 1];
            u32 o0, o1;
            eval(affine(p0.xy, lo, (f32x2){0.f, 0.f}), affine(p0.zw, lo, (f32x2){0.f, 0.f}), true, o0, o1);
            B[2 * h] = o0; B[2 * h + 1] = o1;
#pragma unroll
            for (int e = 0; e < 3; ++e) {
                eval(affine(p0.xy, hi[e], p1.xy), affine(p0.zw, hi[e], p1.zw), false, o0, o1);
                A[e][2 * h] = o0; A[e][2 * h + 1] = o1;
            }
        }
#pragma unroll
        for (int e = 0; e < 3; ++e) ks.acc[e] = mfma16(A[e], B, ks.acc[e]);
    }
}
__device__ __forceinline__ const f32x4* kgen_params(char* smem, int wave, int step) {
    return (const f32x4*)(smem + OFF_P) + ((wave >> 1) & 1) * 128 + ((wave & 1) * 4 + step) * 16;
}
__device__ __forceinline__ void kgen_save(char* smem, int wave, int lane, const KState& ks) {
    char* p = smem + OFF_KP + wave * KP_WAVE + lane * 16;
    *(f32x4*)p = ks.acc[0];
    *(f32x4*)(p + 1024) = ks.acc[1];
    if (lane < 48) *(f32x4*)(p + 2048) = ks.acc[2];
}
__device__ __forceinline__ f32x4 kgen_sum(const char* p) {
    return (*(const f32x4*)p + *(const f32x4*)(p + KP_WAVE)) + (*(const f32x4*)(p + 4 * KP_WAVE) + *(const f32x4*)(p + 5 * KP_WAVE));
}
__device__ __forceinline__ void kgen_reduce(char* smem, int wave, int lane, u32 dskip) {
    const int c = wave >> 1, j = lane & 15, g = lane >> 4;
    char* KC = smem + OFF_KC + c * 1296;
    const char* p = smem + OFF_KP + 2 * c * KP_WAVE + lane * 16;
    if ((wave & 1) == 0) {
        f32x4 s = kgen_sum(p);
        const f32x4 t = kgen_sum(smem + OFF_KP + 2 * c * KP_WAVE + 2048 + (lane < 48 ? lane : 0) * 16);
        if (lane == 0) s[0] += c ? bf_hi(dskip) : bf_lo(dskip);
        *(u32x2*)(KC + 2 * (128 * (g >> 1) + 4 * (g & 1) + 8 * j)) = (u32x2){pk_bf(s[0], s[1]), pk_bf(s[2], s[3])};
        const u32 lo = pk_bf(t[0], t[1]), hi = pk_bf(t[2], t[3]);
        u16* o = (u16*)KC + 512 + 64 * g + j;
        if (g < 2) { o[0] = (u16)lo; o[16] = (u16)(lo >> 16); o[32] = (u16)hi; o[48] = (u16)(hi >> 16); }
        else if (lane == 32) { ((u16*)KC)[640] = (u16)lo; ((u16*)KC)[641] = 0; }
    } else {
        const f32x4 s = kgen_sum(p + 1024);
        const u32 lo = pk_bf(s[0], s[1]), hi = pk_bf(s[2], s[3]);
        if (g < 2) *(u32x2*)(KC + 2 * (256 + 64 * g + 4 * j)) = (u32x2){lo, hi};
        else {
            u32* o = (u32*)(KC + 2 * (384 + 64 * (g - 2) + 2 * j));
            o[0] = lo; o[16] = hi;
        }
    }
}

struct TapIndex {
    u16 off[2048 + 8];
    constexpr TapIndex() : off() {
        for (int i = 0; i < 8; ++i) off[i] = 641 * 2;
        for (int l = 0; l < 2048; ++l) {
            int e = 0;
            while ((l >> e) >= 256) ++e;
            int v = l >> e;
            if (e > 0) {
                const int rem = l & ((1 << e) - 1), half = 1 << (e - 1);
                if (rem > half || (rem == half && (v & 1))) ++v;
            }
            off[8 + l] = (u16)(2 * (128 * e + v));
        }
    }
};
__device__ const TapIndex tap_index = TapIndex();

struct Frags { u32x4 A[8], B[8]; };
template <bool INIT> __device__ __forceinline__ void mma(f32x4& acc, const u32x4& a, const u32x4& b) {
    if constexpr (INIT) asm volatile("v_mfma_f32_16x16x32_bf16 %0, %1, %2, 0" : "=&v"(acc) : "v"(a), "v"(b));
    else asm volatile("v_mfma_f32_16x16x32_bf16 %0, %1, %2, %0" : "+v"(acc) : "v"(a), "v"(b));
}
template <int I> __device__ __forceinline__ void load_one(Frags& f, const char* ap, const char* bp) {
    if constexpr (I == 0) f.A[0] = *(const u32x4*)ap;
    else if constexpr (I <= 8) f.B[I - 1] = *(const u32x4*)(bp + 512 * (8 - I));
    else f.A[I - 8] = *(const u32x4*)(ap + 512 * (I - 8));
}
template <int I> __device__ __forceinline__ void load_all(Frags& f, const char* ap, const char* bp) {
    if constexpr (I < 16) { load_one<I>(f, ap, bp); SB(); load_all<I + 1>(f, ap, bp); }
}
template <int T0, int T1, int T2, int T3> struct TileSet {
    static constexpr int tile(int i) { return i == 0 ? T0 : i == 1 ? T1 : i == 2 ? T2 : T3; }
    static constexpr int walk(int I, bool want_tile) {
        int n = 0;
        for (int a = 0; a < 8; ++a)
            for (int j = 0; j < 4; ++j)
                if (tile(j) >= a) { if (n == I) return want_tile ? tile(j) : a; ++n; }
        return -1;
    }
};
constexpr int tri_a(int I) { int a = 0; while (I >= 8 - a) { I -= 8 - a; ++a; } return a; }
constexpr int tri_b(int I) { int a = 0; while (I >= 8 - a) { I -= 8 - a; ++a; } return I; }
template <int I, bool FIRST> __device__ __forceinline__ void class_step(const Frags& cur, Frags& nxt, f32x4 (&acc)[8], const char* ap, const char* bp) {
    if constexpr (I < 36) {
        constexpr int a = tri_a(I), b = tri_b(I);
        mma<FIRST && a == 0>(acc[a + b], cur.A[a], cur.B[b]);
        if constexpr ((I & 1) == 1 && (I >> 1) < 16) load_one<(I >> 1)>(nxt, ap, bp);
        SB();
        class_step<I + 1, FIRST>(cur, nxt, acc, ap, bp);
    }
}
template <class TS, int I, int TO, int PRE> __device__ __forceinline__ void set_step(const Frags& cur, Frags& nxt, f32x4 (&acc)[8], const char* ap, const char* bp) {
    if constexpr (I < TO) {
        constexpr int t = TS::walk(I, true), a = TS::walk(I, false);
        mma<false>(acc[t], cur.A[a], cur.B[t - a]);
        if constexpr (PRE == 1 && (I * 11) / 18 != ((I + 1) * 11) / 18) load_one<(I * 11) / 18>(nxt, ap, bp);
        if constexpr (PRE == 2 && I < 5) load_one<11 + I>(nxt, ap, bp);
        SB();
        set_step<TS, I + 1, TO, PRE>(cur, nxt, acc, ap, bp);
    }
}
template <class TS, int J> __device__ __forceinline__ void tile_step(const Frags& cur, f32x4 (&acc)[8]) {
    constexpr int t = TS::tile(J);
#pragma unroll
    for (int a = 0; a <= t; ++a) { mma<false>(acc[t], cur.A[a], cur.B[t - a]); SB(); }
}
template <class TS, int J> __device__ __forceinline__ void tile_out(char* Y, f32x4 (&acc)[8], const f32x4 (&o)[4]) {
    constexpr int t = TS::tile(J);
    const f32x4 s = acc[t] + o[J];
    const u32x2 pk = {pk_bf(s[0], s[1]), pk_bf(s[2], s[3])};
    *(u32x2*)(Y + 512 * t) = pk;
    SB();
}
template <class SEND, class KEEP>
__device__ __forceinline__ void conv_half(char* smem, const u32x4 (&A0)[8], const char* ap, const char* bp, int wave, int lane, int ch, u32 seq) {
    f32x4 acc[8];
    Frags f0, f1;
#pragma unroll
    for (int a = 0; a < 8; ++a) f0.A[a] = A0[a];
#pragma unroll
    for (int b = 0; b < 8; ++b) { f0.B[b] = *(const u32x4*)(bp + 512 * (7 - b)); SB(); }
    class_step<0, true>(f0, f1, acc, ap + 64, bp + 64);
    class_step<0, false>(f1, f0, acc, ap + 128, bp + 128);
    set_step<SEND, 0, 18, 1>(f0, f1, acc, ap + 192, bp + 192);
    set_step<SEND, 0, 18, 2>(f1, f1, acc, ap + 192, bp + 192);
    set_step<KEEP, 0, 4, 0>(f0, f1, acc, ap, bp);
    asm volatile("s_nop 3" : "+v"(acc[SEND::tile(0)]), "+v"(acc[SEND::tile(1)]), "+v"(acc[SEND::tile(2)]), "+v"(acc[SEND::tile(3)]));
    f32x4* X = (f32x4*)(smem + OFF_X);
#pragma unroll
    for (int i = 0; i < 4; ++i) X[(wave * 4 + i) * 64 + lane] = acc[SEND::tile(i)];
    volatile LAS u32* xf = (volatile LAS u32*)(LAS char*)(smem + OFF_CNT + 32);
    xf[wave] = seq;
    SB();
    set_step<KEEP, 4, 14, 0>(f0, f1, acc, ap, bp);
    u32 seen = xf[wave ^ 1];
    SB();
    set_step<KEEP, 14, 18, 0>(f0, f1, acc, ap, bp);
    for (u32 spins = 0; seen < seq && spins < 50 * SPIN_CAP; ++spins) seen = xf[wave ^ 1];
    SB();
    f32x4 o[4];
#pragma unroll
    for (int i = 0; i < 4; ++i) o[i] = X[((wave ^ 1) * 4 + i) * 64 + lane];
    SB();
    char* Y = smem + OFF_Y + ch * 4096 + (16 * (lane & 15) + 4 * (lane >> 4)) * 2;
    tile_step<KEEP, 0>(f1, acc);
    tile_step<KEEP, 1>(f1, acc);
    tile_step<KEEP, 2>(f1, acc);
    asm volatile("s_nop 3" : "+v"(acc[KEEP::tile(0)]), "+v"(acc[KEEP::tile(1)]));
    tile_out<KEEP, 0>(Y, acc, o);
    tile_out<KEEP, 1>(Y, acc, o);
    tile_step<KEEP, 3>(f1, acc);
    asm volatile("" : "+v"(acc[KEEP::tile(2)]));
    tile_out<KEEP, 2>(Y, acc, o);
    asm volatile("s_nop 7" : "+v"(acc[KEEP::tile(3)]));
    tile_out<KEEP, 3>(Y, acc, o);
}
struct ConvAddr { const char* ap; const char* bp; };
__device__ __forceinline__ ConvAddr conv_addr(char* smem, int wave, int lane) {
    const int ch = (wave >> 1) & 1, hf = wave & 1;
    const int m = lane & 15, g = lane >> 4;
    ConvAddr c;
    c.ap = smem + OFF_T + ch * T_CH + (m & 7) * T_COPY + 16 * (m >> 3) + 16 * g + 256 * hf;
    c.bp = smem + OFF_U + ch * U_STRIDE + 4064 - 32 * m + 16 * g + 256 * hf - 512 * 7;
    return c;
}
__device__ __forceinline__ void conv_preload(u32x4 (&A0)[8], const ConvAddr& c) {
#pragma unroll
    for (int a = 0; a < 8; ++a) A0[a] = *(const u32x4*)(c.ap + 512 * a);
}
template <bool EARLY>
__device__ __forceinline__ void conv_col(char* smem, const ConvAddr& c, int wave, int lane, u32 seq) {
    u32x4 A0[8];
    if (EARLY) conv_preload(A0, c);
    BAR();
    if (!EARLY) conv_preload(A0, c);
    if ((wave & 1) == 0) conv_half<TileSet<1, 3, 4, 6>, TileSet<0, 2, 5, 7>>(smem, A0, c.ap, c.bp, wave, lane, wave >> 1, seq);
    else conv_half<TileSet<0, 2, 5, 7>, TileSet<1, 3, 4, 6>>(smem, A0, c.ap, c.bp, wave, lane, wave >> 1, seq);
    STAMP(26 + seq);
    BAR();
}
template <class PRE>
__device__ __forceinline__ void conv_row(char* smem, int wave, int lane, rsrc_t rs, int id, int fill0, int st, PRE pre) {
    const u32x4 sent = {SENT, SENT, SENT, SENT};
    const u32 off = WS_H + (fill0 < 0 ? 0 : fill0) * WS_HS + id * 8192 + ((wave - 4) * 64 + lane) * 16;
    const u64 t0 = __builtin_amdgcn_s_memrealtime();
    if (fill0 >= 0) { st_dev(rs, off, sent); st_dev(rs, off + 4096, sent); }
    pre();
    if (fill0 >= 0) {
        wait_until(t0 + FILL_GAP);
        st_dev(rs, off + WS_HS, sent); st_dev(rs, off + WS_HS + 4096, sent);
        WAIT_VM(2);
    }
    STAMP(st);
    BAR();
}

__global__ void __launch_bounds__(512, 2)
h3s4_kernel(const char* __restrict__ q, const char* __restrict__ k, const char* __restrict__ v,
            const u32x2* __restrict__ shiftk, const u32x4* __restrict__ ssmk, const u32* __restrict__ ssmD,
            const u16* __restrict__ Are, const u16* __restrict__ Aim, const u16* __restrict__ Bre, const u16* __restrict__ Bim,
            const u16* __restrict__ Cre, const u16* __restrict__ Cim, const u16* __restrict__ logdt, const u32* __restrict__ Dskip,
            u16* __restrict__ out, char* ws
            ) {
    __shared__ __attribute__((aligned(16))) char smem[LDS_BYTES];
    const int tid = threadIdx.x, lane = tid & 63, wave = __builtin_amdgcn_readfirstlane(tid >> 6);
    const int id = (blockIdx.x & 7) * 32 + (blockIdx.x >> 3);
    const rsrc_t rs = __builtin_amdgcn_make_buffer_rsrc(ws, 0, WS_USED, 0x00020000);
    const u32x4 desc = {(u32)(size_t)ws, (u32)((size_t)ws >> 32) & 0xffffu, WS_USED, 0x00020000u};
    STAMP(0);

    u32 code_pf = 0;
    {
        u64 pc; u32 len;
        asm volatile("s_getpc_b64 %0\nh3s4_text_a:\n\ts_mov_b32 %1, h3s4_text_b - h3s4_text_a" : "=s"(pc), "=s"(len));
        const u32 off = ((blockIdx.x >> 3) * 8 + wave) * 256 + lane * 4;
        if (off + 4 <= len) asm volatile("global_load_dword %0, %1, %2" : "=v"(code_pf) : "v"(off), "s"(pc) : "memory");
    }
    const u32x4 sent = {SENT, SENT, SENT, SENT};
    const u32 dA = ssmD[id], dB = Dskip[id];
    MEMFENCE(); SB();
    st_dev(rs, WS_H + 0 * WS_HS + id * 8192 + tid * 16, sent);
    MEMFENCE(); SB();
#pragma unroll
    for (int s = 0; s < 4; ++s) {
        const int slot = wave + 8 * s;
        if (slot < 28) {
            int row = 8 * id + (slot < 12 ? slot - 4 : slot < 20 ? slot - 12 : slot - 20);
            row = row < 0 ? 0 : row;
            const char* src = slot < 12 ? k : slot < 20 ? v : q;
            lds_dma16(src + row * 1024 + lane * 16, (u32)(size_t)(LAS char*)(smem + OFF_R) + slot * R_ROW);
        }
    }

    if (wave < 4) {
        u32x4 tap_own = ld_b128(ssmk, (2 * id) * 4096 + tid * 16), tap_prev = ld_b128(ssmk, (2 * id) * 4096 + (tid ? tid - 1 : 0) * 16);
        const int pd = 2 * id + (tid >> 7), po = (pd * NS + (tid & 127)) * 2;
        u32 r_dt = ld_u16(logdt, pd * 2), r_are = ld_u16(Are, po), r_aim = ld_u16(Aim, po), r_bre = ld_u16(Bre, po);
        u32 r_bim = ld_u16(Bim, po), r_cre = ld_u16(Cre, po), r_cim = ld_u16(Cim, po);
        MEMFENCE(); SB();
        st_dev(rs, WS_H + 1 * WS_HS + id * 8192 + tid * 16, sent);
        MEMFENCE(); SB();
        const u32 epoch = launch_tag(ws);
        asm volatile("s_waitcnt vmcnt(13)\n\ts_barrier" ::: "memory");
        if (tid < 32) __hip_atomic_store((GAS u32*)(ws + WS_FLAGS) + tid * 256 + id, epoch, RLX_AGENT);
        STAMP(19);
        if (tid < 60) *(u32x4*)(smem + OFF_U + (tid / 30) * U_STRIDE + 4096 + (tid % 30) * 16) = (u32x4){0, 0, 0, 0};
        else if (tid >= 64 && tid < 80) *(u32x4*)(smem + OFF_T + (tid - 64) * T_COPY) = (u32x4){0, 0, 0, 0};
        asm volatile("s_waitcnt vmcnt(1)" : "+v"(tap_own), "+v"(tap_prev), "+v"(r_dt), "+v"(r_are), "+v"(r_aim), "+v"(r_bre), "+v"(r_bim), "+v"(r_cre), "+v"(r_cim) :: "memory");
        BAR();
        STAMP(1);

        {
            const float dt = rbf(__builtin_amdgcn_exp2f(bf_lo(r_dt) * 1.4426950408889634f));
            const float are = dt * bf_lo(r_are), aim = dt * bf_lo(r_aim);
            const float br = dt * bf_lo(r_bre), bi = dt * bf_lo(r_bim);
            const float cr = bf_lo(r_cre), ci = bf_lo(r_cim);
            const float wr = cr * br - ci * bi, wi = cr * bi + ci * br;
            const float mx = fmaxf(fabsf(wr), fabsf(wi)), mn = fminf(fabsf(wr), fabsf(wi));
            const float ratio = mx > 0.f ? mn / mx : 0.f;
            float* p = (float*)(smem + OFF_P) + (tid >> 1) * 8 + (tid & 1);
            p[0] = are * 1.4426950408889634f;
            p[2] = aim * 0.15915494309189535f;
            p[4] = __builtin_amdgcn_logf(mx) + 0.5f * __builtin_amdgcn_logf(fmaf(ratio, ratio, 1.0f));
            p[6] = atan2f(wi, wr) * 0.15915494309189535f;
        }
        if (tid == 0) {
            tap_prev = (u32x4){0, 0, 0, 0};
            tap_own[0] = (tap_own[0] & 0xffff0000u) | (pk_bf(bf_lo(tap_own[0]) + bf_lo(dA), 0.f) & 0xffffu);
        }
        stage_taps(smem, 0, tid, tap_prev, tap_own);
        cnt_arrive(smem, 0, lane);
        STAMP(2);

        const ConvAddr ca = conv_addr(smem, wave, lane);
        WAIT_VM(0);
        asm volatile("" :: "v"(code_pf));
        cnt_wait(smem, 0, 8);
        STAMP(4);
        conv_col<true>(smem, ca, wave, lane, 1);
        STAMP(6);

        const u32x4 tix0 = ((const u32x4*)tap_index.off)[tid], tix1 = ((const u32x4*)tap_index.off)[tid + 1];
        {
            KState ks;
            kgen_init(ks);
            kgen_steps<4 - R_STEPS, true>(kgen_params(smem, wave, 0), lane, ks);
            kgen_save(smem, wave, lane, ks);
        }
        STAMP(7);
        cnt_arrive(smem, 1, lane);
        cnt_wait(smem, 1, 8);
        kgen_reduce(smem, wave, lane, dB);
        cnt_arrive(smem, 2, lane);
        cnt_wait(smem, 2, 4);
        STAMP(23);
        {
            u32 d[2][8];
#pragma unroll
            for (int c = 0; c < 2; ++c) {
                const char* kc = smem + OFF_KC + c * 1296;
#pragma unroll
                for (int j = 0; j < 8; ++j) {
                    const u32 o = j < 4 ? tix0[j] : tix1[j - 4];
                    d[c][j] = (u32)*(const u16*)(kc + (o & 0xffffu)) | ((u32)*(const u16*)(kc + (o >> 16)) << 16);
                }
            }
#pragma unroll
            for (int c = 0; c < 2; ++c)
                stage_taps(smem, c, tid, (u32x4){d[c][0], d[c][1], d[c][2], d[c][3]}, (u32x4){d[c][4], d[c][5], d[c][6], d[c][7]});
        }
        cnt_arrive(smem, 3, lane);
        cnt_wait(smem, 3, 4);
        STAMP(25);
        conv_col<true>(smem, ca, wave, lane, 2);
        STAMP(11);
        conv_col<true>(smem, ca, wave, lane, 3);
        STAMP(15);
    } else {
        const int r = tid - 256, w = wave - 4;
        u32x2 skA = ld_b64(shiftk, r * 8), skB = ld_b64(shiftk, (r + 256) * 8);
        u32x4 tap_own = ld_b128(ssmk, (2 * id + 1) * 4096 + r * 16), tap_prev = ld_b128(ssmk, (2 * id + 1) * 4096 + (r ? r - 1 : 0) * 16);
        MEMFENCE(); SB();
        st_dev(rs, WS_H + 1 * WS_HS + id * 8192 + tid * 16, sent);
        MEMFENCE(); SB();
        const u32 epoch = launch_tag(ws);
        asm volatile("s_waitcnt vmcnt(7)\n\ts_barrier" ::: "memory");
        __builtin_amdgcn_s_setprio(2);
        ((u32*)(smem + OFF_RED))[r] = SENT;
        if (r < 16) ((u32*)(smem + OFF_CNT))[r] = 0u;
        asm volatile("s_waitcnt vmcnt(1)" : "+v"(skA), "+v"(skB), "+v"(tap_own), "+v"(tap_prev) :: "memory");
        BAR();
        STAMP(1);

        const u32 own_a = id * 8192 + r * 16, own_b = own_a + 4096;
        const u32 col_a = (r >> 1) * 8192 + (2 * id + (r & 1)) * 16, col_b = col_a + 128 * 8192;
        const u32 row_a = (r >> 1) * 8192 + id * 32 + (r & 1) * 16, row_b = row_a + 128 * 8192;
        char* const u_dst = smem + OFF_U + (r & 1) * U_STRIDE + 4080 - (r >> 1) * 16;
        const char* const y_src = smem + OFF_Y + (r & 1) * 4096 + (r >> 1) * 16;
        float* red = (float*)(smem + OFF_RED);

        const u32 flag_off = WS_FLAGS + (blockIdx.x & 31) * 1024 + lane * 16;
        u32x4 fl = {epoch, epoch, epoch, epoch};
        if (wave == 7) fl = poll_issue(rs, flag_off);

        Rows x, u0;
        {
            const u32 rp = (u32)(size_t)(LAS char*)(smem + OFF_R + ((lane & 15) >> 2) * R_ROW + (64 * w + 16 * (lane >> 4) + 4 * (lane & 3)) * 2);
            u32x2 kk[2][3], vv[2][2], qq[2][2];
            static_assert(4 * R_ROW == 4352, "offsets below");
            asm volatile("ds_read_b64_tr_b16 %0, %10\n\tds_read_b64_tr_b16 %1, %10 offset:4352\n\tds_read_b64_tr_b16 %2, %10 offset:8704\n\t"
                         "ds_read_b64_tr_b16 %3, %10 offset:13056\n\tds_read_b64_tr_b16 %4, %10 offset:17408\n\t"
                         "ds_read_b64_tr_b16 %5, %10 offset:512\n\tds_read_b64_tr_b16 %6, %10 offset:4864\n\tds_read_b64_tr_b16 %7, %10 offset:9216\n\t"
                         "ds_read_b64_tr_b16 %8, %10 offset:13568\n\tds_read_b64_tr_b16 %9, %10 offset:17920\n\ts_waitcnt lgkmcnt(0)"
                         : "=&v"(kk[0][0]), "=&v"(kk[0][1]), "=&v"(kk[0][2]), "=&v"(vv[0][0]), "=&v"(vv[0][1]),
                           "=&v"(kk[1][0]), "=&v"(kk[1][1]), "=&v"(kk[1][2]), "=&v"(vv[1][0]), "=&v"(vv[1][1]) : "v"(rp) : "memory");
            const u32 first = id ? 0xffffffffu : 0u;
            u32x4 o[2];
#pragma unroll
            for (int c = 0; c < 2; ++c) {
                const u32x2 sk = c ? skB : skA;
                const u32 kp = kk[c][0][0] & first, kq = kk[c][0][1] & first;
                const float kf[12] = {0.f, bf_hi(kp), bf_lo(kq), bf_hi(kq), bf_lo(kk[c][1][0]), bf_hi(kk[c][1][0]),
                                      bf_lo(kk[c][1][1]), bf_hi(kk[c][1][1]), bf_lo(kk[c][2][0]), bf_hi(kk[c][2][0]),
                                      bf_lo(kk[c][2][1]), bf_hi(kk[c][2][1])};
                float vf[8];
                unpack8((u32x4){vv[c][0][0], vv[c][0][1], vv[c][1][0], vv[c][1][1]}, vf);
                const float w0 = bf_lo(sk[0]), w1 = bf_hi(sk[0]), w2 = bf_lo(sk[1]), w3 = bf_hi(sk[1]);
                float kv[8];
#pragma unroll
                for (int j = 0; j < 8; ++j) kv[j] = fmaf(kf[j + 4], w0, fmaf(kf[j + 3], w1, fmaf(kf[j + 2], w2, kf[j + 1] * w3))) * vf[j];
                o[c] = pack8(kv);
            }
            publish(rs, WS_H + 0 * WS_HS + own_a, o[0]);
            publish(rs, WS_H + 0 * WS_HS + own_b, o[1]);
            asm volatile("ds_read_b64_tr_b16 %0, %4 offset:21760\n\tds_read_b64_tr_b16 %1, %4 offset:26112\n\t"
                         "ds_read_b64_tr_b16 %2, %4 offset:22272\n\tds_read_b64_tr_b16 %3, %4 offset:26624\n\ts_waitcnt lgkmcnt(0)"
                         : "=&v"(qq[0][0]), "=&v"(qq[0][1]), "=&v"(qq[1][0]), "=&v"(qq[1][1]) : "v"(rp) : "memory");
            unpack_rows((u32x4){qq[0][0][0], qq[0][0][1], qq[0][1][0], qq[0][1][1]}, x.a);
            unpack_rows((u32x4){qq[1][0][0], qq[1][0][1], qq[1][1][0], qq[1][1][1]}, x.b);
        }
        u64 t_s = __builtin_amdgcn_s_memrealtime();
        STAMP(2);
        u32 touch = 0;
        if (wave == 6) {
            if (lane < 4) touch = __builtin_amdgcn_raw_buffer_load_b32(rs, WS_H + (2 + lane) * WS_HS + id * 8192, 0, 16);
            else if (lane == 4) touch = ((const u32*)out)[id * 2048];
        }
        if (r == 0) {
            tap_prev = (u32x4){0, 0, 0, 0};
            tap_own[0] = (tap_own[0] & 0xffff0000u) | (pk_bf(bf_lo(tap_own[0]) + bf_hi(dA), 0.f) & 0xffffu);
        }
        stage_taps(smem, 1, r, tap_prev, tap_own);
        cnt_arrive(smem, 0, lane);
        if (wave == 7) {
            flags_finish(fl, rs, flag_off, epoch);
            if (blockIdx.x == 0 && lane == 0) __hip_atomic_store((GAS u32*)(ws + WS_EPOCH), epoch, RLX_AGENT);
            cnt_arrive(smem, 4, lane);
        }
        cnt_wait(smem, 4, 1);
        STAMP(3);

        wait_until(t_s + POLL_DELAY);
        Piece2 pc = poll2_issue(rs, WS_H + 0 * WS_HS + col_a, WS_H + 0 * WS_HS + col_b);
        layer_norm(x, u0, red, lane, w);
        poll2_finish(pc, desc, WS_H + 0 * WS_HS + col_a, WS_H + 0 * WS_HS + col_b);
        STAMP(21);
        *(u32x4*)u_dst = reverse8(pc.a);
        *(u32x4*)(u_dst - 2048) = reverse8(pc.b);
        asm volatile("" :: "v"(touch));
        asm volatile("" :: "v"(code_pf));
        BAR();
        STAMP(5);
        if (wave == 7 && lane < 32) __hip_atomic_store((GAS u32*)(ws + WS_FLAGS + 32768) + lane * 256 + id, epoch, RLX_AGENT);
        conv_row(smem, wave, lane, rs, id, 2, 27, [&] {
            __builtin_amdgcn_s_setprio(0);
            KState ks;
            kgen_init(ks);
            kgen_steps<R_STEPS, false>(kgen_params(smem, wave, 4 - R_STEPS), lane, ks);
            kgen_save(smem, wave, lane, ks);
            cnt_arrive(smem, 1, lane);
            __builtin_amdgcn_s_setprio(2);
            if (wave == 7) fl = poll_issue(rs, flag_off + 32768);
        });
        publish(rs, WS_H + 1 * WS_HS + own_a, *(const u32x4*)y_src);
        publish(rs, WS_H + 1 * WS_HS + own_b, *(const u32x4*)(y_src + 2048));
        t_s = __builtin_amdgcn_s_memrealtime();
        STAMP(6);
        if (wave == 7) {
            flags_finish(fl, rs, flag_off + 32768, epoch);
            cnt_arrive(smem, 5, lane);
        }

        wait_until(t_s + POLL_DELAY);
        cnt_wait(smem, 5, 1);
        Piece2 pr = poll2_issue(rs, WS_H + 1 * WS_HS + row_a, WS_H + 1 * WS_HS + row_b);
        STAMP(7);

        poll2_finish(pr, desc, WS_H + 1 * WS_HS + row_a, WS_H + 1 * WS_HS + row_b);
        STAMP(8);
        Rows yA, u;
        unpack_rows(pr.a, yA.a);
        unpack_rows(pr.b, yA.b);
#pragma unroll
        for (int i = 0; i < 4; ++i) { x.a[i] += u0.a[i] * yA.a[i]; x.b[i] += u0.b[i] * yA.b[i]; }
        round_rows(x.a);
        round_rows(x.b);
        layer_norm(x, u, red + 64, lane, w);
        publish(rs, WS_H + 2 * WS_HS + own_a, pack_rows(u.a));
        publish(rs, WS_H + 2 * WS_HS + own_b, pack_rows(u.b));
        t_s = __builtin_amdgcn_s_memrealtime();
        STAMP(9);

        wait_until(t_s + POLL_DELAY);
        pc = poll2_issue(rs, WS_H + 2 * WS_HS + col_a, WS_H + 2 * WS_HS + col_b);
        poll2_finish(pc, desc, WS_H + 2 * WS_HS + col_a, WS_H + 2 * WS_HS + col_b);
        STAMP(26);
        *(u32x4*)u_dst = reverse8(pc.a);
        *(u32x4*)(u_dst - 2048) = reverse8(pc.b);
        BAR();
        STAMP(10);
        conv_row(smem, wave, lane, rs, id, 4, 28, [] {});
        publish(rs, WS_H + 3 * WS_HS + own_a, *(const u32x4*)y_src);
        publish(rs, WS_H + 3 * WS_HS + own_b, *(const u32x4*)(y_src + 2048));
        t_s = __builtin_amdgcn_s_memrealtime();
        STAMP(11);

        wait_until(t_s + POLL_DELAY);
        pr = poll2_issue(rs, WS_H + 3 * WS_HS + row_a, WS_H + 3 * WS_HS + row_b);
        poll2_finish(pr, desc, WS_H + 3 * WS_HS + row_a, WS_H + 3 * WS_HS + row_b);
        STAMP(12);
        {
            Rows y;
            unpack_rows(pr.a, y.a);
            unpack_rows(pr.b, y.b);
#pragma unroll
            for (int i = 0; i < 4; ++i) { x.a[i] += y.a[i]; x.b[i] += y.b[i]; }
            round_rows(x.a);
            round_rows(x.b);
            layer_norm(x, u, red + 128, lane, w);
#pragma unroll
            for (int i = 0; i < 4; ++i) { x.a[i] += u.a[i] * yA.a[i]; x.b[i] += u.b[i] * yA.b[i]; }
            round_rows(x.a);
            round_rows(x.b);
            layer_norm(x, u, red + 192, lane, w);
        }
        publish(rs, WS_H + 4 * WS_HS + own_a, pack_rows(u.a));
        publish(rs, WS_H + 4 * WS_HS + own_b, pack_rows(u.b));
        t_s = __builtin_amdgcn_s_memrealtime();
        STAMP(13);

        wait_until(t_s + POLL_DELAY);
        pc = poll2_issue(rs, WS_H + 4 * WS_HS + col_a, WS_H + 4 * WS_HS + col_b);
        poll2_finish(pc, desc, WS_H + 4 * WS_HS + col_a, WS_H + 4 * WS_HS + col_b);
        *(u32x4*)u_dst = reverse8(pc.a);
        *(u32x4*)(u_dst - 2048) = reverse8(pc.b);
        BAR();
        STAMP(14);
        conv_row(smem, wave, lane, rs, id, -1, 29, [] {});
        publish(rs, WS_H + 5 * WS_HS + own_a, *(const u32x4*)y_src);
        publish(rs, WS_H + 5 * WS_HS + own_b, *(const u32x4*)(y_src + 2048));
        t_s = __builtin_amdgcn_s_memrealtime();
        STAMP(15);

        wait_until(t_s + POLL_DELAY);
        pr = poll2_issue(rs, WS_H + 5 * WS_HS + row_a, WS_H + 5 * WS_HS + row_b);
        poll2_finish(pr, desc, WS_H + 5 * WS_HS + row_a, WS_H + 5 * WS_HS + row_b);
        STAMP(16);
        {
            Rows y;
            unpack_rows(pr.a, y.a);
            unpack_rows(pr.b, y.b);
#pragma unroll
            for (int i = 0; i < 4; ++i) { y.a[i] += x.a[i]; y.b[i] += x.b[i]; }
            const u32x4 oa = pack_rows(y.a), ob = pack_rows(y.b);
            u16* dst = out + (8 * id) * DM + r;
#pragma unroll
            for (int j = 0; j < 4; ++j) {
                dst[(2 * j) * DM] = (u16)oa[j];
                dst[(2 * j) * DM + 256] = (u16)ob[j];
                dst[(2 * j + 1) * DM] = (u16)(oa[j] >> 16);
                dst[(2 * j + 1) * DM + 256] = (u16)(ob[j] >> 16);
            }
        }
        STAMP(17);
    }
    asm volatile("h3s4_text_b:");
}

extern "C" void kernel_launch(void* const* d_in, const int* in_sizes, int n_in,
                              void* d_out, int out_size, void* d_ws, size_t ws_size,
                              hipStream_t stream) {
    h3s4_kernel<<<256, 512, 0, stream>>>(
        (const char*)d_in[0], (const char*)d_in[1], (const char*)d_in[2], (const u32x2*)d_in[3], (const u32x4*)d_in[4],
        (const u32*)d_in[5], (const u16*)d_in[7], (const u16*)d_in[8], (const u16*)d_in[9], (const u16*)d_in[10],
        (const u16*)d_in[11], (const u16*)d_in[12], (const u16*)d_in[13], (const u32*)d_in[14], (u16*)d_out, (char*)d_ws
        );
}
```

```cpp
#define DIAG 0
#define DIAG_A 0
#define DIAG_B 0
#include <hip/hip_runtime.h>
#include <hip/hip_bf16.h>
#include <stdint.h>

typedef unsigned int u32;
typedef unsigned short u16;
typedef unsigned long long u64;
typedef __attribute__((ext_vector_type(4))) float f32x4;
typedef __attribute__((ext_vector_type(2))) float f32x2;
typedef __attribute__((ext_vector_type(4))) u32 u32x4;
typedef __attribute__((ext_vector_type(2))) u32 u32x2;
typedef __attribute__((ext_vector_type(4))) short s16x4;
typedef __attribute__((ext_vector_type(8))) __bf16 bf16x8;
typedef __attribute__((ext_vector_type(2))) __bf16 bf16x2;
#define LAS __attribute__((address_space(3)))
#define GAS __attribute__((address_space(1)))
#define RLX_AGENT __ATOMIC_RELAXED, __HIP_MEMORY_SCOPE_AGENT
#define SB() __builtin_amdgcn_sched_barrier(0)
#define BAR() asm volatile("s_waitcnt lgkmcnt(0)\n\ts_barrier" ::: "memory")
#define MEMFENCE() asm volatile("" ::: "memory")
#define WAIT_VM(n) do { MEMFENCE(); __builtin_amdgcn_s_waitcnt(0x0F70 | ((n) & 15) | (((n) >> 4) << 14)); MEMFENCE(); } while (0)

constexpr int DM = 512;
constexpr int NS = 128;
constexpr u32 SENT = 0xffffffffu;
constexpr u32 SPIN_CAP = 20000;
#define FILL_GAP 70
#define R_STEPS 1
#define POLL_DELAY 50

constexpr u32 WS_EPOCH = 0;
constexpr u32 WS_FLAGS = 4096;
constexpr u32 WS_H = 1u << 20;
constexpr u32 WS_HS = 2u << 20;
constexpr u32 WS_USED = WS_H + 6 * WS_HS;

constexpr int T_COPY = 4160, T_CH = 8 * T_COPY;
constexpr int U_STRIDE = 4672;
constexpr int R_ROW = 1088;
constexpr int KP_WAVE = 2816;
constexpr int OFF_T = 0;
constexpr int OFF_U = OFF_T + 2 * T_CH;
constexpr int OFF_X = OFF_U + 2 * U_STRIDE;
constexpr int OFF_Y = OFF_X + 16384;
constexpr int OFF_R = OFF_Y + 8192;
constexpr int OFF_P = OFF_R + 28 * R_ROW;
constexpr int OFF_KP = OFF_P + 4096;
constexpr int OFF_KC = OFF_KP + 8 * KP_WAVE;
constexpr int OFF_RED = OFF_KC + 2 * 648 * 2;
constexpr int OFF_CNT = OFF_RED + 1024;
constexpr int OFF_ST = OFF_CNT + 64;
constexpr int LDS_BYTES = OFF_ST + 512;
static_assert(LDS_BYTES <= 163840, "LDS");

#define STAMP(i) do { if ((threadIdx.x & 255) == 0) ((u64*)(smem + OFF_ST))[(i) + (threadIdx.x ? 32 : 0)] = __builtin_amdgcn_s_memrealtime(); } while (0)

__device__ __forceinline__ float bf_lo(u32 p) { return __builtin_bit_cast(float, p << 16); }
__device__ __forceinline__ float bf_hi(u32 p) { return __builtin_bit_cast(float, p & 0xffff0000u); }
__device__ __forceinline__ float bf_u16(u16 p) { return __builtin_bit_cast(float, (u32)p << 16); }
__device__ __forceinline__ u32 pk_bf(float lo, float hi) {
    bf16x2 v = {(__bf16)lo, (__bf16)hi};
    return __builtin_bit_cast(u32, v);
}
__device__ __forceinline__ float as_f(u32 x) { return __builtin_bit_cast(float, x); }
__device__ __forceinline__ u32 as_u(float x) { return __builtin_bit_cast(u32, x); }
__device__ __forceinline__ float rbf(float x) { return bf_lo(pk_bf(x, x)); }
__device__ __forceinline__ void unpack8(u32x4 p, float (&f)[8]) {
#pragma unroll
    for (int i = 0; i < 4; ++i) { f[2 * i] = bf_lo(p[i]); f[2 * i + 1] = bf_hi(p[i]); }
}
__device__ __forceinline__ u32x4 pack8(const float (&f)[8]) {
    return (u32x4){pk_bf(f[0], f[1]), pk_bf(f[2], f[3]), pk_bf(f[4], f[5]), pk_bf(f[6], f[7])};
}
__device__ __forceinline__ u32x4 reverse8(u32x4 p) {
    return (u32x4){__builtin_amdgcn_alignbit(p[3], p[3], 16), __builtin_amdgcn_alignbit(p[2], p[2], 16),
                   __builtin_amdgcn_alignbit(p[1], p[1], 16), __builtin_amdgcn_alignbit(p[0], p[0], 16)};
}
template <int CTRL> __device__ __forceinline__ float dppf(float v) {
    return __builtin_bit_cast(float, __builtin_amdgcn_update_dpp(0, __builtin_bit_cast(int, v), CTRL, 0xf, 0xf, true));
}

__device__ __forceinline__ void lds_dma16(const char* src, u32 lds) {
    u32 keep;
    asm volatile("s_mov_b32 %0, m0\n\ts_mov_b32 m0, %2\n\ts_nop 0\n\tglobal_load_lds_dwordx4 %1, off\n\ts_mov_b32 m0, %0"
                 : "=&s"(keep) : "v"(src), "s"(lds) : "memory");
}

__device__ __forceinline__ u32 ld_u16(const void* base, u32 off) {
    u32 r;
    asm volatile("global_load_ushort %0, %1, %2" : "=v"(r) : "v"(off), "s"(base) : "memory");
    return r;
}
__device__ __forceinline__ u32x2 ld_b64(const void* base, u32 off) {
    u32x2 r;
    asm volatile("global_load_dwordx2 %0, %1, %2" : "=v"(r) : "v"(off), "s"(base) : "memory");
    return r;
}
__device__ __forceinline__ u32x4 ld_b128(const void* base, u32 off) {
    u32x4 r;
    asm volatile("global_load_dwordx4 %0, %1, %2" : "=v"(r) : "v"(off), "s"(base) : "memory");
    return r;
}

typedef __amdgpu_buffer_rsrc_t rsrc_t;
__device__ __forceinline__ u32x4 ld_dev(rsrc_t rs, u32 off) { return __builtin_amdgcn_raw_buffer_load_b128(rs, off, 0, 16); }
__device__ __forceinline__ void st_dev(rsrc_t rs, u32 off, u32x4 v) { __builtin_amdgcn_raw_buffer_store_b128(v, rs, off, 0, 16); }
__device__ __forceinline__ void publish(rsrc_t rs, u32 off, u32x4 v) {
#pragma unroll
    for (int i = 0; i < 4; ++i) v[i] = v[i] < SENT - 1u ? v[i] : SENT - 1u;
    st_dev(rs, off, v);
}
__device__ __forceinline__ u32 launch_tag(const char* ws) {
    u32 e;
    asm volatile("s_load_dword %0, %1, 0x0\n\ts_waitcnt lgkmcnt(0)" : "=&s"(e) : "s"(ws + WS_EPOCH) : "memory");
    return e + 1u;
}
__device__ __forceinline__ void wait_until(u64 t) {
    while (__builtin_amdgcn_s_memrealtime() < t) __builtin_amdgcn_s_sleep(1);
}
__device__ __forceinline__ u32x4 poll_issue(rsrc_t rs, u32 off) {
    MEMFENCE();
    u32x4 x = ld_dev(rs, off);
    MEMFENCE();
    return x;
}
struct Piece2 { u32x4 a, b; };
__device__ __forceinline__ Piece2 poll2_issue(rsrc_t rs, u32 off_a, u32 off_b) {
    MEMFENCE();
    Piece2 p = {ld_dev(rs, off_a), ld_dev(rs, off_b)};
    MEMFENCE();
    return p;
}
__device__ __forceinline__ bool has_sentinel(const u32x4& x) {
    u32 mx = x[0] > x[1] ? x[0] : x[1];
    mx = mx > x[2] ? mx : x[2];
    mx = mx > x[3] ? mx : x[3];
    return mx == SENT;
}
__device__ __forceinline__ void poll2_finish(Piece2& p, u32x4 desc, u32 off_a, u32 off_b) {
#pragma nounroll
    for (u32 spins = 0; spins < SPIN_CAP; ++spins) {
        const bool bad = has_sentinel(p.a) || has_sentinel(p.b);
        if (__builtin_amdgcn_ballot_w64(bad) == 0ull) break;
        __builtin_amdgcn_s_sleep(1);
        if (bad) asm volatile("buffer_load_dwordx4 %0, %2, %4, 0 offen sc1\n\tbuffer_load_dwordx4 %1, %3, %4, 0 offen sc1\n\ts_waitcnt vmcnt(0)"
                              : "+v"(p.a), "+v"(p.b) : "v"(off_a), "v"(off_b), "s"(desc) : "memory");
    }
}
__device__ __forceinline__ void flags_finish(u32x4& fl, rsrc_t rs, u32 off, u32 epoch) {
    u32 spins = 0;
    while (__builtin_amdgcn_ballot_w64(fl[0] != epoch || fl[1] != epoch || fl[2] != epoch || fl[3] != epoch) != 0ull) {
        if (++spins > SPIN_CAP) break;
        __builtin_amdgcn_s_sleep(2);
        fl = poll_issue(rs, off);
    }
}

__device__ __forceinline__ void cnt_arrive(char* smem, int k, int lane) {
    asm volatile("s_waitcnt lgkmcnt(0)" ::: "memory");
    if (lane == 0) __hip_atomic_fetch_add((LAS u32*)(smem + OFF_CNT) + k, 1u, __ATOMIC_RELAXED, __HIP_MEMORY_SCOPE_WORKGROUP);
}
__device__ __forceinline__ void cnt_wait(char* smem, int k, u32 target) {
    u32 spins = 0;
    while (__hip_atomic_load((LAS u32*)(smem + OFF_CNT) + k, __ATOMIC_RELAXED, __HIP_MEMORY_SCOPE_WORKGROUP) < target) {
        if (++spins > 50 * SPIN_CAP) break;
        __builtin_amdgcn_s_sleep(1);
    }
    MEMFENCE();
}

struct Rows { f32x2 a[4], b[4]; };
__device__ __forceinline__ void unpack_rows(u32x4 p, f32x2 (&f)[4]) {
#pragma unroll
    for (int i = 0; i < 4; ++i) f[i] = (f32x2){bf_lo(p[i]), bf_hi(p[i])};
}
__device__ __forceinline__ u32x4 pack_rows(const f32x2 (&f)[4]) {
    return (u32x4){pk_bf(f[0][0], f[0][1]), pk_bf(f[1][0], f[1][1]), pk_bf(f[2][0], f[2][1]), pk_bf(f[3][0], f[3][1])};
}
__device__ __forceinline__ u32x4 round_rows(f32x2 (&f)[4]) {
    u32x4 p = pack_rows(f);
    asm("" : "+v"(p));
    unpack_rows(p, f);
    return p;
}
__device__ __forceinline__ void layer_norm(const Rows& x, Rows& u, float* red, int lane, int w) {
#pragma clang fp contract(fast)
    u32 a[8], b[8];
#pragma unroll
    for (int i = 0; i < 4; ++i) {
        const f32x2 s1 = x.a[i] + x.b[i], s2 = x.a[i] * x.a[i] + x.b[i] * x.b[i];
        a[2 * i] = as_u(s1[0]); a[2 * i + 1] = as_u(s1[1]);
        b[2 * i] = as_u(s2[0]); b[2 * i + 1] = as_u(s2[1]);
    }
    SB();
#pragma unroll
    for (int j = 0; j < 8; ++j) {
        const auto s = __builtin_amdgcn_permlane32_swap(a[j], b[j], false, false);
        a[j] = s[0]; b[j] = s[1];
    }
    SB();
    float r[8];
#pragma unroll
    for (int j = 0; j < 8; ++j) r[j] = as_f(a[j]) + as_f(b[j]);
    SB();
#pragma unroll
    for (int j = 0; j < 4; ++j) {
        const auto s = __builtin_amdgcn_permlane16_swap(as_u(r[j]), as_u(r[j + 4]), false, false);
        a[j] = s[0]; b[j] = s[1];
    }
    SB();
    float s4[4];
#pragma unroll
    for (int j = 0; j < 4; ++j) s4[j] = as_f(a[j]) + as_f(b[j]);
    SB();
#pragma unroll
    for (int j = 0; j < 4; ++j) s4[j] += dppf<0x128>(s4[j]);
    const float t0 = (lane & 8) ? s4[2] : s4[0], t1 = (lane & 8) ? s4[3] : s4[1];
    const float c0 = t0 + dppf<0x141>(t0), c1 = t1 + dppf<0x141>(t1);
    float s = (lane & 4) ? c1 : c0;
    s += dppf<0xB1>(s);
    s += dppf<0x4E>(s);
    const u32 sb = as_u(s) < SENT - 1u ? as_u(s) : SENT - 1u;
    if ((lane & 3) == 0) ((volatile LAS u32*)(LAS char*)(char*)red)[(lane >> 2) * 4 + w] = sb;
    u32x4 p;
    u32 spins = 0;
    while (true) {
        p = *(volatile LAS u32x4*)((LAS char*)(char*)red + (lane & 15) * 16);
        u32 mx = p[0] > p[1] ? p[0] : p[1];
        mx = mx > p[2] ? mx : p[2];
        mx = mx > p[3] ? mx : p[3];
        if (__builtin_amdgcn_ballot_w64(mx == SENT) == 0ull) break;
        if (++spins > 50 * SPIN_CAP) break;
    }
    const float m = ((as_f(p[0]) + as_f(p[1])) + (as_f(p[2]) + as_f(p[3]))) * (1.0f / DM);
    const float ex2 = dppf<0x128>(m);
    const float rstd = __builtin_amdgcn_rsqf(fmaxf(fmaf(-m, m, ex2), 0.f) + 1e-5f);
    const float off = -m * rstd;
    const f32x2 R[4] = {{dppf<0x150>(rstd), dppf<0x151>(rstd)}, {dppf<0x152>(rstd), dppf<0x153>(rstd)},
                        {dppf<0x154>(rstd), dppf<0x155>(rstd)}, {dppf<0x156>(rstd), dppf<0x157>(rstd)}};
    const f32x2 O[4] = {{dppf<0x150>(off), dppf<0x151>(off)}, {dppf<0x152>(off), dppf<0x153>(off)},
                        {dppf<0x154>(off), dppf<0x155>(off)}, {dppf<0x156>(off), dppf<0x157>(off)}};
#pragma unroll
    for (int i = 0; i < 4; ++i) { u.a[i] = x.a[i] * R[i] + O[i]; u.b[i] = x.b[i] * R[i] + O[i]; }
}

__device__ __forceinline__ void stage_taps(char* smem, int ch, int i, u32x4 prev, u32x4 own) {
    const u32 D[8] = {prev[0], prev[1], prev[2], prev[3], own[0], own[1], own[2], own[3]};
    u32 O[7];
#pragma unroll
    for (int y = 0; y < 7; ++y) O[y] = __builtin_amdgcn_alignbit(D[y + 1], D[y], 16);
    char* base = smem + OFF_T + ch * T_CH + (i + 1) * 16;
#pragma unroll
    for (int c = 0; c < 8; ++c) {
        const int y = (c + 1) >> 1;
        const u32x4 v = (c & 1) ? (u32x4){D[y], D[y + 1], D[y + 2], D[y + 3]} : (u32x4){O[y], O[y + 1], O[y + 2], O[y + 3]};
        *(u32x4*)(base + c * T_COPY) = v;
    }
}

__device__ __forceinline__ f32x4 mfma16(u32x4 a, u32x4 b, f32x4 c) {
    return __builtin_amdgcn_mfma_f32_16x16x32_bf16(__builtin_bit_cast(bf16x8, a), __builtin_bit_cast(bf16x8, b), c, 0, 0, 0);
}
struct KState { f32x4 acc[3]; };
__device__ __forceinline__ void kgen_init(KState& ks) {
#pragma unroll
    for (int e = 0; e < 3; ++e) ks.acc[e] = (f32x4){0, 0, 0, 0};
}
#define OPQ(x) asm volatile("" : "+v"(x))
template <int NS_, bool PK>
__device__ __forceinline__ void kgen_steps(const f32x4* P, int lane, KState& ks) {
#pragma clang fp contract(fast)
    const int g = lane >> 4, m = lane & 15;
    const float lo = (float)(8 * m);
    const float hi[3] = {(float)(128 * (m >> 3) + (m & 7)),
                         (float)(m < 8 ? 256 + 128 * (m >> 2) + 2 * (m & 3) : 512 + 128 * ((m - 8) >> 1) + 4 * (m & 1)),
                         (float)(m < 8 ? 1024 + 128 * m : 2048)};
    auto eval = [&](f32x2 ex, f32x2 an, bool conj, u32& o0, u32& o1) {
        if (!PK) { OPQ(ex[0]); OPQ(ex[1]); OPQ(an[0]); OPQ(an[1]); }
        const float m0 = __builtin_amdgcn_exp2f(ex[0]), m1 = __builtin_amdgcn_exp2f(ex[1]);
        const float f0 = __builtin_amdgcn_fractf(an[0]), f1 = __builtin_amdgcn_fractf(an[1]);
        const float c0 = __builtin_amdgcn_cosf(f0), c1 = __builtin_amdgcn_cosf(f1);
        const float s0 = __builtin_amdgcn_sinf(f0), s1 = __builtin_amdgcn_sinf(f1);
        float r0, r1, i0, i1;
        if (PK) {
            const f32x2 r = (f32x2){c0, c1} * (f32x2){m0, m1}, i = (f32x2){s0, s1} * (f32x2){m0, m1};
            r0 = r[0]; r1 = r[1]; i0 = i[0]; i1 = i[1];
        } else {
            r0 = c0 * m0; OPQ(r0); i0 = s0 * m0; OPQ(i0); r1 = c1 * m1; OPQ(r1); i1 = s1 * m1; OPQ(i1);
        }
        o0 = pk_bf(r0, conj ? -i0 : i0);
        o1 = pk_bf(r1, conj ? -i1 : i1);
    };
    auto affine = [&](f32x2 a, float p, f32x2 b) -> f32x2 {
        if (PK) return a * p + b;
        float x = fmaf(a[0], p, b[0]), y = fmaf(a[1], p, b[1]);
        OPQ(x); OPQ(y);
        return (f32x2){x, y};
    };
#pragma unroll
    for (int s = 0; s < NS_; ++s) {
        u32x4 B, A[3];
#pragma unroll
        for (int h = 0; h < 2; ++h) {
            const f32x4 p0 = P[16 * s + 4 * g + 2 * h], p1 = P[16 * s + 4 * g + 2 * h + 1];
            u32 o0, o1;
            eval(affine(p0.xy, lo, (f32x2){0.f, 0.f}), affine(p0.zw, lo, (f32x2){0.f, 0.f}), true, o0, o1);
            B[2 * h] = o0; B[2 * h + 1] = o1;
#pragma unroll
            for (int e = 0; e < 3; ++e) {
                eval(affine(p0.xy, hi[e], p1.xy), affine(p0.zw, hi[e], p1.zw), false, o0, o1);
                A[e][2 * h] = o0; A[e][2 * h + 1] = o1;
            }
        }
#pragma unroll
        for (int e = 0; e < 3; ++e) ks.acc[e] = mfma16(A[e], B, ks.acc[e]);
    }
}
__device__ __forceinline__ const f32x4* kgen_params(char* smem, int wave, int step) {
    return (const f32x4*)(smem + OFF_P) + ((wave >> 1) & 1) * 128 + ((wave & 1) * 4 + step) * 16;
}
__device__ __forceinline__ void kgen_save(char* smem, int wave, int lane, const KState& ks) {
    char* p = smem + OFF_KP + wave * KP_WAVE + lane * 16;
    *(f32x4*)p = ks.acc[0];
    *(f32x4*)(p + 1024) = ks.acc[1];
    if (lane < 48) *(f32x4*)(p + 2048) = ks.acc[2];
}
__device__ __forceinline__ f32x4 kgen_sum(const char* p) {
    return (*(const f32x4*)p + *(const f32x4*)(p + KP_WAVE)) + (*(const f32x4*)(p + 4 * KP_WAVE) + *(const f32x4*)(p + 5 * KP_WAVE));
}
__device__ __forceinline__ void kgen_reduce(char* smem, int wave, int lane, u32 dskip) {
    const int c = wave >> 1, j = lane & 15, g = lane >> 4;
    char* KC = smem + OFF_KC + c * 1296;
    const char* p = smem + OFF_KP + 2 * c * KP_WAVE + lane * 16;
    if ((wave & 1) == 0) {
        f32x4 s = kgen_sum(p);
        const f32x4 t = kgen_sum(smem + OFF_KP + 2 * c * KP_WAVE + 2048 + (lane < 48 ? lane : 0) * 16);
        if (lane == 0) s[0] += c ? bf_hi(dskip) : bf_lo(dskip);
        *(u32x2*)(KC + 2 * (128 * (g >> 1) + 4 * (g & 1) + 8 * j)) = (u32x2){pk_bf(s[0], s[1]), pk_bf(s[2], s[3])};
        const u32 lo = pk_bf(t[0], t[1]), hi = pk_bf(t[2], t[3]);
        u16* o = (u16*)KC + 512 + 64 * g + j;
        if (g < 2) { o[0] = (u16)lo; o[16] = (u16)(lo >> 16); o[32] = (u16)hi; o[48] = (u16)(hi >> 16); }
        else if (lane == 32) { ((u16*)KC)[640] = (u16)lo; ((u16*)KC)[641] = 0; }
    } else {
        const f32x4 s = kgen_sum(p + 1024);
        const u32 lo = pk_bf(s[0], s[1]), hi = pk_bf(s[2], s[3]);
        if (g < 2) *(u32x2*)(KC + 2 * (256 + 64 * g + 4 * j)) = (u32x2){lo, hi};
        else {
            u32* o = (u32*)(KC + 2 * (384 + 64 * (g - 2) + 2 * j));
            o[0] = lo; o[16] = hi;
        }
    }
}

struct TapIndex {
    u16 off[2048 + 8];
    constexpr TapIndex() : off() {
        for (int i = 0; i < 8; ++i) off[i] = 641 * 2;
        for (int l = 0; l < 2048; ++l) {
            int e = 0;
            while ((l >> e) >= 256) ++e;
            int v = l >> e;
            if (e > 0) {
                const int rem = l & ((1 << e) - 1), half = 1 << (e - 1);
                if (rem > half || (rem == half && (v & 1))) ++v;
            }
            off[8 + l] = (u16)(2 * (128 * e + v));
        }
    }
};
__device__ const TapIndex tap_index = TapIndex();

struct Frags { u32x4 A[8], B[8]; };
template <bool INIT> __device__ __forceinline__ void mma(f32x4& acc, const u32x4& a, const u32x4& b) {
    if constexpr (INIT) asm volatile("v_mfma_f32_16x16x32_bf16 %0, %1, %2, 0" : "=&v"(acc) : "v"(a), "v"(b));
    else asm volatile("v_mfma_f32_16x16x32_bf16 %0, %1, %2, %0" : "+v"(acc) : "v"(a), "v"(b));
}
template <int I> __device__ __forceinline__ void load_one(Frags& f, const char* ap, const char* bp) {
    if constexpr (I == 0) f.A[0] = *(const u32x4*)ap;
    else if constexpr (I <= 8) f.B[I - 1] = *(const u32x4*)(bp + 512 * (8 - I));
    else f.A[I - 8] = *(const u32x4*)(ap + 512 * (I - 8));
}
template <int I> __device__ __forceinline__ void load_all(Frags& f, const char* ap, const char* bp) {
    if constexpr (I < 16) { load_one<I>(f, ap, bp); SB(); load_all<I + 1>(f, ap, bp); }
}
template <int T0, int T1, int T2, int T3> struct TileSet {
    static constexpr int tile(int i) { return i == 0 ? T0 : i == 1 ? T1 : i == 2 ? T2 : T3; }
    static constexpr int walk(int I, bool want_tile) {
        int n = 0;
        for (int a = 0; a < 8; ++a)
            for (int j = 0; j < 4; ++j)
                if (tile(j) >= a) { if (n == I) return want_tile ? tile(j) : a; ++n; }
        return -1;
    }
};
constexpr int tri_a(int I) { int a = 0; while (I >= 8 - a) { I -= 8 - a; ++a; } return a; }
constexpr int tri_b(int I) { int a = 0; while (I >= 8 - a) { I -= 8 - a; ++a; } return I; }
template <int I, bool FIRST> __device__ __forceinline__ void class_step(const Frags& cur, Frags& nxt, f32x4 (&acc)[8], const char* ap, const char* bp) {
    if constexpr (I < 36) {
        constexpr int a = tri_a(I), b = tri_b(I);
        mma<FIRST && a == 0>(acc[a + b], cur.A[a], cur.B[b]);
        if constexpr ((I & 1) == 1 && (I >> 1) < 16) load_one<(I >> 1)>(nxt, ap, bp);
        SB();
        class_step<I + 1, FIRST>(cur, nxt, acc, ap, bp);
    }
}
template <class TS, int I, int TO, int PRE> __device__ __forceinline__ void set_step(const Frags& cur, Frags& nxt, f32x4 (&acc)[8], const char* ap, const char* bp) {
    if constexpr (I < TO) {
        constexpr int t = TS::walk(I, true), a = TS::walk(I, false);
        mma<false>(acc[t], cur.A[a], cur.B[t - a]);
        if constexpr (PRE == 1 && (I * 11) / 18 != ((I + 1) * 11) / 18) load_one<(I * 11) / 18>(nxt, ap, bp);
        if constexpr (PRE == 2 && I < 5) load_one<11 + I>(nxt, ap, bp);
        SB();
        set_step<TS, I + 1, TO, PRE>(cur, nxt, acc, ap, bp);
    }
}
template <class TS, int J> __device__ __forceinline__ void tile_step(const Frags& cur, f32x4 (&acc)[8]) {
    constexpr int t = TS::tile(J);
#pragma unroll
    for (int a = 0; a <= t; ++a) { mma<false>(acc[t], cur.A[a], cur.B[t - a]); SB(); }
}
template <class TS, int J> __device__ __forceinline__ void tile_out(char* Y, f32x4 (&acc)[8], const f32x4 (&o)[4]) {
    constexpr int t = TS::tile(J);
    const f32x4 s = acc[t] + o[J];
    const u32x2 pk = {pk_bf(s[0], s[1]), pk_bf(s[2], s[3])};
    *(u32x2*)(Y + 512 * t) = pk;
    SB();
}
template <class SEND, class KEEP>
__device__ __forceinline__ void conv_half(char* smem, const u32x4 (&A0)[8], const char* ap, const char* bp, int wave, int lane, int ch, u32 seq) {
    f32x4 acc[8];
    Frags f0, f1;
#pragma unroll
    for (int a = 0; a < 8; ++a) f0.A[a] = A0[a];
#pragma unroll
    for (int b = 0; b < 8; ++b) { f0.B[b] = *(const u32x4*)(bp + 512 * (7 - b)); SB(); }
    class_step<0, true>(f0, f1, acc, ap + 64, bp + 64);
    class_step<0, false>(f1, f0, acc, ap + 128, bp + 128);
    set_step<SEND, 0, 18, 1>(f0, f1, acc, ap + 192, bp + 192);
    set_step<SEND, 0, 18, 2>(f1, f1, acc, ap + 192, bp + 192);
    set_step<KEEP, 0, 4, 0>(f0, f1, acc, ap, bp);
    asm volatile("s_nop 3" : "+v"(acc[SEND::tile(0)]), "+v"(acc[SEND::tile(1)]), "+v"(acc[SEND::tile(2)]), "+v"(acc[SEND::tile(3)]));
    f32x4* X = (f32x4*)(smem + OFF_X);
#pragma unroll
    for (int i = 0; i < 4; ++i) X[(wave * 4 + i) * 64 + lane] = acc[SEND::tile(i)];
    volatile LAS u32* xf = (volatile LAS u32*)(LAS char*)(smem + OFF_CNT + 32);
    xf[wave] = seq;
    SB();
    set_step<KEEP, 4, 14, 0>(f0, f1, acc, ap, bp);
    u32 seen = xf[wave ^ 1];
    SB();
    set_step<KEEP, 14, 18, 0>(f0, f1, acc, ap, bp);
    for (u32 spins = 0; seen < seq && spins < 50 * SPIN_CAP; ++spins) seen = xf[wave ^ 1];
    SB();
    f32x4 o[4];
#pragma unroll
    for (int i = 0; i < 4; ++i) o[i] = X[((wave ^ 1) * 4 + i) * 64 + lane];
    SB();
    char* Y = smem + OFF_Y + ch * 4096 + (16 * (lane & 15) + 4 * (lane >> 4)) * 2;
    tile_step<KEEP, 0>(f1, acc);
    tile_step<KEEP, 1>(f1, acc);
    tile_step<KEEP, 2>(f1, acc);
    asm volatile("s_nop 3" : "+v"(acc[KEEP::tile(0)]), "+v"(acc[KEEP::tile(1)]));
    tile_out<KEEP, 0>(Y, acc, o);
    tile_out<KEEP, 1>(Y, acc, o);
    tile_step<KEEP, 3>(f1, acc);
    asm volatile("" : "+v"(acc[KEEP::tile(2)]));
    tile_out<KEEP, 2>(Y, acc, o);
    asm volatile("s_nop 7" : "+v"(acc[KEEP::tile(3)]));
    tile_out<KEEP, 3>(Y, acc, o);
}
struct ConvAddr { const char* ap; const char* bp; };
__device__ __forceinline__ ConvAddr conv_addr(char* smem, int wave, int lane) {
    const int ch = (wave >> 1) & 1, hf = wave & 1;
    const int m = lane & 15, g = lane >> 4;
    ConvAddr c;
    c.ap = smem + OFF_T + ch * T_CH + (m & 7) * T_COPY + 16 * (m >> 3) + 16 * g + 256 * hf;
    c.bp = smem + OFF_U + ch * U_STRIDE + 4064 - 32 * m + 16 * g + 256 * hf - 512 * 7;
    return c;
}
__device__ __forceinline__ void conv_preload(u32x4 (&A0)[8], const ConvAddr& c) {
#pragma unroll
    for (int a = 0; a < 8; ++a) A0[a] = *(const u32x4*)(c.ap + 512 * a);
}
template <bool EARLY>
__device__ __forceinline__ void conv_col(char* smem, const ConvAddr& c, int wave, int lane, u32 seq) {
    u32x4 A0[8];
    if (EARLY) conv_preload(A0, c);
    BAR();
    if (!EARLY) conv_preload(A0, c);
    if ((wave & 1) == 0) conv_half<TileSet<1, 3, 4, 6>, TileSet<0, 2, 5, 7>>(smem, A0, c.ap, c.bp, wave, lane, wave >> 1, seq);
    else conv_half<TileSet<0, 2, 5, 7>, TileSet<1, 3, 4, 6>>(smem, A0, c.ap, c.bp, wave, lane, wave >> 1, seq);
    STAMP(26 + seq);
    BAR();
}
template <class PRE>
__device__ __forceinline__ void conv_row(char* smem, int wave, int lane, rsrc_t rs, int id, int fill0, int st, PRE pre) {
    const u32x4 sent = {SENT, SENT, SENT, SENT};
    const u32 off = WS_H + (fill0 < 0 ? 0 : fill0) * WS_HS + id * 8192 + ((wave - 4) * 64 + lane) * 16;
    const u64 t0 = __builtin_amdgcn_s_memrealtime();
    if (fill0 >= 0) { st_dev(rs, off, sent); st_dev(rs, off + 4096, sent); }
    pre();
    if (fill0 >= 0) {
        wait_until(t0 + FILL_GAP);
        st_dev(rs, off + WS_HS, sent); st_dev(rs, off + WS_HS + 4096, sent);
        WAIT_VM(2);
    }
    STAMP(st);
    BAR();
}

__global__ void __launch_bounds__(512, 2)
h3s4_kernel(const char* __restrict__ q, const char* __restrict__ k, const char* __restrict__ v,
            const u32x2* __restrict__ shiftk, const u32x4* __restrict__ ssmk, const u32* __restrict__ ssmD,
            const u16* __restrict__ Are, const u16* __restrict__ Aim, const u16* __restrict__ Bre, const u16* __restrict__ Bim,
            const u16* __restrict__ Cre, const u16* __restrict__ Cim, const u16* __restrict__ logdt, const u32* __restrict__ Dskip,
            u16* __restrict__ out, char* ws
            ) {
    __shared__ __attribute__((aligned(16))) char smem[LDS_BYTES];
    const int tid = threadIdx.x, lane = tid & 63, wave = __builtin_amdgcn_readfirstlane(tid >> 6);
    const int id = (blockIdx.x & 7) * 32 + (blockIdx.x >> 3);
    const rsrc_t rs = __builtin_amdgcn_make_buffer_rsrc(ws, 0, WS_USED, 0x00020000);
    const u32x4 desc = {(u32)(size_t)ws, (u32)((size_t)ws >> 32) & 0xffffu, WS_USED, 0x00020000u};
    STAMP(0);

    u32 code_pf = 0;
    {
        u64 pc; u32 len;
        asm volatile("s_getpc_b64 %0\nh3s4_text_a:\n\ts_mov_b32 %1, h3s4_text_b - h3s4_text_a" : "=s"(pc), "=s"(len));
        const u32 off = ((blockIdx.x >> 3) * 8 + wave) * 256 + lane * 4;
        if (off + 4 <= len) asm volatile("global_load_dword %0, %1, %2" : "=v"(code_pf) : "v"(off), "s"(pc) : "memory");
    }
    const u32x4 sent = {SENT, SENT, SENT, SENT};
    const u32 dA = ssmD[id], dB = Dskip[id];
    MEMFENCE(); SB();
    st_dev(rs, WS_H + 0 * WS_HS + id * 8192 + tid * 16, sent);
    MEMFENCE(); SB();
#pragma unroll
    for (int s = 0; s < 4; ++s) {
        const int slot = wave + 8 * s;
        if (slot < 28) {
            int row = 8 * id + (slot < 12 ? slot - 4 : slot < 20 ? slot - 12 : slot - 20);
            row = row < 0 ? 0 : row;
            const char* src = slot < 12 ? k : slot < 20 ? v : q;
            lds_dma16(src + row * 1024 + lane * 16, (u32)(size_t)(LAS char*)(smem + OFF_R) + slot * R_ROW);
        }
    }

    if (wave < 4) {
        u32x4 tap_own = ld_b128(ssmk, (2 * id) * 4096 + tid * 16), tap_prev = ld_b128(ssmk, (2 * id) * 4096 + (tid ? tid - 1 : 0) * 16);
        const int pd = 2 * id + (tid >> 7), po = (pd * NS + (tid & 127)) * 2;
        u32 r_dt = ld_u16(logdt, pd * 2), r_are = ld_u16(Are, po), r_aim = ld_u16(Aim, po), r_bre = ld_u16(Bre, po);
        u32 r_bim = ld_u16(Bim, po), r_cre = ld_u16(Cre, po), r_cim = ld_u16(Cim, po);
        MEMFENCE(); SB();
        st_dev(rs, WS_H + 1 * WS_HS + id * 8192 + tid * 16, sent);
        MEMFENCE(); SB();
        const u32 epoch = launch_tag(ws);
        asm volatile("s_waitcnt vmcnt(13)\n\ts_barrier" ::: "memory");
        if (tid < 32) __hip_atomic_store((GAS u32*)(ws + WS_FLAGS) + tid * 256 + id, epoch, RLX_AGENT);
        STAMP(19);
        if (tid < 60) *(u32x4*)(smem + OFF_U + (tid / 30) * U_STRIDE + 4096 + (tid % 30) * 16) = (u32x4){0, 0, 0, 0};
        else if (tid >= 64 && tid < 80) *(u32x4*)(smem + OFF_T + (tid - 64) * T_COPY) = (u32x4){0, 0, 0, 0};
        asm volatile("s_waitcnt vmcnt(1)" : "+v"(tap_own), "+v"(tap_prev), "+v"(r_dt), "+v"(r_are), "+v"(r_aim), "+v"(r_bre), "+v"(r_bim), "+v"(r_cre), "+v"(r_cim) :: "memory");
        BAR();
        STAMP(1);

        {
            const float dt = rbf(__builtin_amdgcn_exp2f(bf_lo(r_dt) * 1.4426950408889634f));
            const float are = dt * bf_lo(r_are), aim = dt * bf_lo(r_aim);
            const float br = dt * bf_lo(r_bre), bi = dt * bf_lo(r_bim);
            const float cr = bf_lo(r_cre), ci = bf_lo(r_cim);
            const float wr = cr * br - ci * bi, wi = cr * bi + ci * br;
            const float mx = fmaxf(fabsf(wr), fabsf(wi)), mn = fminf(fabsf(wr), fabsf(wi));
            const float ratio = mx > 0.f ? mn / mx : 0.f;
            float* p = (float*)(smem + OFF_P) + (tid >> 1) * 8 + (tid & 1);
            p[0] = are * 1.4426950408889634f;
            p[2] = aim * 0.15915494309189535f;
            p[4] = __builtin_amdgcn_logf(mx) + 0.5f * __builtin_amdgcn_logf(fmaf(ratio, ratio, 1.0f));
            p[6] = atan2f(wi, wr) * 0.15915494309189535f;
        }
        if (tid == 0) {
            tap_prev = (u32x4){0, 0, 0, 0};
            tap_own[0] = (tap_own[0] & 0xffff0000u) | (pk_bf(bf_lo(tap_own[0]) + bf_lo(dA), 0.f) & 0xffffu);
        }
        stage_taps(smem, 0, tid, tap_prev, tap_own);
        cnt_arrive(smem, 0, lane);
        STAMP(2);

        const ConvAddr ca = conv_addr(smem, wave, lane);
        WAIT_VM(0);
        asm volatile("" :: "v"(code_pf));
        cnt_wait(smem, 0, 8);
        STAMP(4);
        conv_col<true>(smem, ca, wave, lane, 1);
        STAMP(6);

        const u32x4 tix0 = ((const u32x4*)tap_index.off)[tid], tix1 = ((const u32x4*)tap_index.off)[tid + 1];
        {
            KState ks;
            kgen_init(ks);
            kgen_steps<4 - R_STEPS, true>(kgen_params(smem, wave, 0), lane, ks);
            kgen_save(smem, wave, lane, ks);
        }
        STAMP(7);
        cnt_arrive(smem, 1, lane);
        cnt_wait(smem, 1, 8);
        kgen_reduce(smem, wave, lane, dB);
        cnt_arrive(smem, 2, lane);
        cnt_wait(smem, 2, 4);
        STAMP(23);
        {
            u32 d[2][8];
#pragma unroll
            for (int c = 0; c < 2; ++c) {
                const char* kc = smem + OFF_KC + c * 1296;
#pragma unroll
                for (int j = 0; j < 8; ++j) {
                    const u32 o = j < 4 ? tix0[j] : tix1[j - 4];
                    d[c][j] = (u32)*(const u16*)(kc + (o & 0xffffu)) | ((u32)*(const u16*)(kc + (o >> 16)) << 16);
                }
            }
#pragma unroll
            for (int c = 0; c < 2; ++c)
                stage_taps(smem, c, tid, (u32x4){d[c][0], d[c][1], d[c][2], d[c][3]}, (u32x4){d[c][4], d[c][5], d[c][6], d[c][7]});
        }
        cnt_arrive(smem, 3, lane);
        cnt_wait(smem, 3, 4);
        STAMP(25);
        conv_col<true>(smem, ca, wave, lane, 2);
        STAMP(11);
        conv_col<true>(smem, ca, wave, lane, 3);
        STAMP(15);
    } else {
        const int r = tid - 256, w = wave - 4;
        u32x2 skA = ld_b64(shiftk, r * 8), skB = ld_b64(shiftk, (r + 256) * 8);
        u32x4 tap_own = ld_b128(ssmk, (2 * id + 1) * 4096 + r * 16), tap_prev = ld_b128(ssmk, (2 * id + 1) * 4096 + (r ? r - 1 : 0) * 16);
        MEMFENCE(); SB();
        st_dev(rs, WS_H + 1 * WS_HS + id * 8192 + tid * 16, sent);
        MEMFENCE(); SB();
        const u32 epoch = launch_tag(ws);
        asm volatile("s_waitcnt vmcnt(7)\n\ts_barrier" ::: "memory");
        __builtin_amdgcn_s_setprio(2);
        ((u32*)(smem + OFF_RED))[r] = SENT;
        if (r < 16) ((u32*)(smem + OFF_CNT))[r] = 0u;
        asm volatile("s_waitcnt vmcnt(1)" : "+v"(skA), "+v"(skB), "+v"(tap_own), "+v"(tap_prev) :: "memory");
        BAR();
        STAMP(1);

        const u32 own_a = id * 8192 + r * 16, own_b = own_a + 4096;
        const u32 col_a = (r >> 1) * 8192 + (2 * id + (r & 1)) * 16, col_b = col_a + 128 * 8192;
        const u32 row_a = (r >> 1) * 8192 + id * 32 + (r & 1) * 16, row_b = row_a + 128 * 8192;
        char* const u_dst = smem + OFF_U + (r & 1) * U_STRIDE + 4080 - (r >> 1) * 16;
        const char* const y_src = smem + OFF_Y + (r & 1) * 4096 + (r >> 1) * 16;
        float* red = (float*)(smem + OFF_RED);

        const u32 flag_off = WS_FLAGS + (blockIdx.x & 31) * 1024 + lane * 16;
        u32x4 fl = {epoch, epoch, epoch, epoch};
        if (wave == 7) fl = poll_issue(rs, flag_off);

        Rows x, u0;
        {
            const u32 rp = (u32)(size_t)(LAS char*)(smem + OFF_R + ((lane & 15) >> 2) * R_ROW + (64 * w + 16 * (lane >> 4) + 4 * (lane & 3)) * 2);
            u32x2 kk[2][3], vv[2][2], qq[2][2];
            static_assert(4 * R_ROW == 4352, "offsets below");
            asm volatile("ds_read_b64_tr_b16 %0, %10\n\tds_read_b64_tr_b16 %1, %10 offset:4352\n\tds_read_b64_tr_b16 %2, %10 offset:8704\n\t"
                         "ds_read_b64_tr_b16 %3, %10 offset:13056\n\tds_read_b64_tr_b16 %4, %10 offset:17408\n\t"
                         "ds_read_b64_tr_b16 %5, %10 offset:512\n\tds_read_b64_tr_b16 %6, %10 offset:4864\n\tds_read_b64_tr_b16 %7, %10 offset:9216\n\t"
                         "ds_read_b64_tr_b16 %8, %10 offset:13568\n\tds_read_b64_tr_b16 %9, %10 offset:17920\n\ts_waitcnt lgkmcnt(0)"
                         : "=&v"(kk[0][0]), "=&v"(kk[0][1]), "=&v"(kk[0][2]), "=&v"(vv[0][0]), "=&v"(vv[0][1]),
                           "=&v"(kk[1][0]), "=&v"(kk[1][1]), "=&v"(kk[1][2]), "=&v"(vv[1][0]), "=&v"(vv[1][1]) : "v"(rp) : "memory");
            const u32 first = id ? 0xffffffffu : 0u;
            u32x4 o[2];
#pragma unroll
            for (int c = 0; c < 2; ++c) {
                const u32x2 sk = c ? skB : skA;
                const u32 kp = kk[c][0][0] & first, kq = kk[c][0][1] & first;
                const float kf[12] = {0.f, bf_hi(kp), bf_lo(kq), bf_hi(kq), bf_lo(kk[c][1][0]), bf_hi(kk[c][1][0]),
                                      bf_lo(kk[c][1][1]), bf_hi(kk[c][1][1]), bf_lo(kk[c][2][0]), bf_hi(kk[c][2][0]),
                                      bf_lo(kk[c][2][1]), bf_hi(kk[c][2][1])};
                float vf[8];
                unpack8((u32x4){vv[c][0][0], vv[c][0][1], vv[c][1][0], vv[c][1][1]}, vf);
                const float w0 = bf_lo(sk[0]), w1 = bf_hi(sk[0]), w2 = bf_lo(sk[1]), w3 = bf_hi(sk[1]);
                float kv[8];
#pragma unroll
                for (int j = 0; j < 8; ++j) kv[j] = fmaf(kf[j + 4], w0, fmaf(kf[j + 3], w1, fmaf(kf[j + 2], w2, kf[j + 1] * w3))) * vf[j];
                o[c] = pack8(kv);
            }
            publish(rs, WS_H + 0 * WS_HS + own_a, o[0]);
            publish(rs, WS_H + 0 * WS_HS + own_b, o[1]);
            asm volatile("ds_read_b64_tr_b16 %0, %4 offset:21760\n\tds_read_b64_tr_b16 %1, %4 offset:26112\n\t"
                         "ds_read_b64_tr_b16 %2, %4 offset:22272\n\tds_read_b64_tr_b16 %3, %4 offset:26624\n\ts_waitcnt lgkmcnt(0)"
                         : "=&v"(qq[0][0]), "=&v"(qq[0][1]), "=&v"(qq[1][0]), "=&v"(qq[1][1]) : "v"(rp) : "memory");
            unpack_rows((u32x4){qq[0][0][0], qq[0][0][1], qq[0][1][0], qq[0][1][1]}, x.a);
            unpack_rows((u32x4){qq[1][0][0], qq[1][0][1], qq[1][1][0], qq[1][1][1]}, x.b);
        }
        u64 t_s = __builtin_amdgcn_s_memrealtime();
        STAMP(2);
        u32 touch = 0;
        if (wave == 6) {
            if (lane < 4) touch = __builtin_amdgcn_raw_buffer_load_b32(rs, WS_H + (2 + lane) * WS_HS + id * 8192, 0, 16);
            else if (lane == 4) touch = ((const u32*)out)[id * 2048];
        }
        if (r == 0) {
            tap_prev = (u32x4){0, 0, 0, 0};
            tap_own[0] = (tap_own[0] & 0xffff0000u) | (pk_bf(bf_lo(tap_own[0]) + bf_hi(dA), 0.f) & 0xffffu);
        }
        stage_taps(smem, 1, r, tap_prev, tap_own);
        cnt_arrive(smem, 0, lane);
        if (wave == 7) {
            flags_finish(fl, rs, flag_off, epoch);
            if (blockIdx.x == 0 && lane == 0) __hip_atomic_store((GAS u32*)(ws + WS_EPOCH), epoch, RLX_AGENT);
            cnt_arrive(smem, 4, lane);
        }
        cnt_wait(smem, 4, 1);
        STAMP(3);

        wait_until(t_s + POLL_DELAY);
        Piece2 pc = poll2_issue(rs, WS_H + 0 * WS_HS + col_a, WS_H + 0 * WS_HS + col_b);
        layer_norm(x, u0, red, lane, w);
        poll2_finish(pc, desc, WS_H + 0 * WS_HS + col_a, WS_H + 0 * WS_HS + col_b);
        STAMP(21);
        *(u32x4*)u_dst = reverse8(pc.a);
        *(u32x4*)(u_dst - 2048) = reverse8(pc.b);
        asm volatile("" :: "v"(touch));
        asm volatile("" :: "v"(code_pf));
        BAR();
        STAMP(5);
        if (wave == 7 && lane < 32) __hip_atomic_store((GAS u32*)(ws + WS_FLAGS + 32768) + lane * 256 + id, epoch, RLX_AGENT);
        conv_row(smem, wave, lane, rs, id, 2, 27, [&] {
            __builtin_amdgcn_s_setprio(0);
            KState ks;
            kgen_init(ks);
            kgen_steps<R_STEPS, false>(kgen_params(smem, wave, 4 - R_STEPS), lane, ks);
            kgen_save(smem, wave, lane, ks);
            cnt_arrive(smem, 1, lane);
            __builtin_amdgcn_s_setprio(2);
            if (wave == 7) fl = poll_issue(rs, flag_off + 32768);
        });
        publish(rs, WS_H + 1 * WS_HS + own_a, *(const u32x4*)y_src);
        publish(rs, WS_H + 1 * WS_HS + own_b, *(const u32x4*)(y_src + 2048));
        t_s = __builtin_amdgcn_s_memrealtime();
        STAMP(6);
        if (wave == 7) {
            flags_finish(fl, rs, flag_off + 32768, epoch);
            cnt_arrive(smem, 5, lane);
        }

        wait_until(t_s + POLL_DELAY);
        cnt_wait(smem, 5, 1);
        Piece2 pr = poll2_issue(rs, WS_H + 1 * WS_HS + row_a, WS_H + 1 * WS_HS + row_b);
        STAMP(7);

        poll2_finish(pr, desc, WS_H + 1 * WS_HS + row_a, WS_H + 1 * WS_HS + row_b);
        STAMP(8);
        Rows yA, u;
        unpack_rows(pr.a, yA.a);
        unpack_rows(pr.b, yA.b);
#pragma unroll
        for (int i = 0; i < 4; ++i) { x.a[i] += u0.a[i] * yA.a[i]; x.b[i] += u0.b[i] * yA.b[i]; }
        round_rows(x.a);
        round_rows(x.b);
        layer_norm(x, u, red + 64, lane, w);
        publish(rs, WS_H + 2 * WS_HS + own_a, pack_rows(u.a));
        publish(rs, WS_H + 2 * WS_HS + own_b, pack_rows(u.b));
        t_s = __builtin_amdgcn_s_memrealtime();
        STAMP(9);

        wait_until(t_s + POLL_DELAY);
        pc = poll2_issue(rs, WS_H + 2 * WS_HS + col_a, WS_H + 2 * WS_HS + col_b);
        poll2_finish(pc, desc, WS_H + 2 * WS_HS + col_a, WS_H + 2 * WS_HS + col_b);
        STAMP(26);
        *(u32x4*)u_dst = reverse8(pc.a);
        *(u32x4*)(u_dst - 2048) = reverse8(pc.b);
        BAR();
        STAMP(10);
        conv_row(smem, wave, lane, rs, id, 4, 28, [] {});
        publish(rs, WS_H + 3 * WS_HS + own_a, *(const u32x4*)y_src);
        publish(rs, WS_H + 3 * WS_HS + own_b, *(const u32x4*)(y_src + 2048));
        t_s = __builtin_amdgcn_s_memrealtime();
        STAMP(11);

        wait_until(t_s + POLL_DELAY);
        pr = poll2_issue(rs, WS_H + 3 * WS_HS + row_a, WS_H + 3 * WS_HS + row_b);
        poll2_finish(pr, desc, WS_H + 3 * WS_HS + row_a, WS_H + 3 * WS_HS + row_b);
        STAMP(12);
        {
            Rows y;
            unpack_rows(pr.a, y.a);
            unpack_rows(pr.b, y.b);
#pragma unroll
            for (int i = 0; i < 4; ++i) { x.a[i] += y.a[i]; x.b[i] += y.b[i]; }
            round_rows(x.a);
            round_rows(x.b);
            layer_norm(x, u, red + 128, lane, w);
#pragma unroll
            for (int i = 0; i < 4; ++i) { x.a[i] += u.a[i] * yA.a[i]; x.b[i] += u.b[i] * yA.b[i]; }
            round_rows(x.a);
            round_rows(x.b);
            layer_norm(x, u, red + 192, lane, w);
        }
        publish(rs, WS_H + 4 * WS_HS + own_a, pack_rows(u.a));
        publish(rs, WS_H + 4 * WS_HS + own_b, pack_rows(u.b));
        t_s = __builtin_amdgcn_s_memrealtime();
        STAMP(13);

        wait_until(t_s + POLL_DELAY);
        pc = poll2_issue(rs, WS_H + 4 * WS_HS + col_a, WS_H + 4 * WS_HS + col_b);
        poll2_finish(pc, desc, WS_H + 4 * WS_HS + col_a, WS_H + 4 * WS_HS + col_b);
        *(u32x4*)u_dst = reverse8(pc.a);
        *(u32x4*)(u_dst - 2048) = reverse8(pc.b);
        BAR();
        STAMP(14);
        conv_row(smem, wave, lane, rs, id, -1, 29, [] {});
        publish(rs, WS_H + 5 * WS_HS + own_a, *(const u32x4*)y_src);
        publish(rs, WS_H + 5 * WS_HS + own_b, *(const u32x4*)(y_src + 2048));
        t_s = __builtin_amdgcn_s_memrealtime();
        STAMP(15);

        wait_until(t_s + POLL_DELAY);
        pr = poll2_issue(rs, WS_H + 5 * WS_HS + row_a, WS_H + 5 * WS_HS + row_b);
        poll2_finish(pr, desc, WS_H + 5 * WS_HS + row_a, WS_H + 5 * WS_HS + row_b);
        STAMP(16);
        {
            Rows y;
            unpack_rows(pr.a, y.a);
            unpack_rows(pr.b, y.b);
#pragma unroll
            for (int i = 0; i < 4; ++i) { y.a[i] += x.a[i]; y.b[i] += x.b[i]; }
            const u32x4 oa = pack_rows(y.a), ob = pack_rows(y.b);
            u16* dst = out + (8 * id) * DM + r;
#pragma unroll
            for (int j = 0; j < 4; ++j) {
                dst[(2 * j) * DM] = (u16)oa[j];
                dst[(2 * j) * DM + 256] = (u16)ob[j];
                dst[(2 * j + 1) * DM] = (u16)(oa[j] >> 16);
                dst[(2 * j + 1) * DM + 256] = (u16)(ob[j] >> 16);
            }
        }
        STAMP(17);
    }
    asm volatile("h3s4_text_b:");
    asm volatile("s_waitcnt vmcnt(0)" ::: "memory");
    STAMP(18);
    BAR();
    if (tid == 0) {
        const u64* st = (const u64*)(smem + OFF_ST);
        const u64 dur = st[DIAG_B] - st[DIAG_A];
        const u64 until = __builtin_amdgcn_s_memrealtime() + DIAG * dur;
        while (__builtin_amdgcn_s_memrealtime() < until) __builtin_amdgcn_s_sleep(2);
    }
}

extern "C" void kernel_launch(void* const* d_in, const int* in_sizes, int n_in,
                              void* d_out, int out_size, void* d_ws, size_t ws_size,
                              hipStream_t stream) {
    h3s4_kernel<<<256, 512, 0, stream>>>(
        (const char*)d_in[0], (const char*)d_in[1], (const char*)d_in[2], (const u32x2*)d_in[3], (const u32x4*)d_in[4],
        (const u32*)d_in[5], (const u16*)d_in[7], (const u16*)d_in[8], (const u16*)d_in[9], (const u16*)d_in[10],
        (const u16*)d_in[11], (const u16*)d_in[12], (const u16*)d_in[13], (const u32*)d_in[14], (u16*)d_out, (char*)d_ws
        );
}
```

```cpp
#include <hip/hip_runtime.h>
#include <hip/hip_bf16.h>
#include <stdint.h>

typedef unsigned int u32;
typedef unsigned short u16;
typedef unsigned long long u64;
typedef __attribute__((ext_vector_type(4))) float f32x4;
typedef __attribute__((ext_vector_type(2))) float f32x2;
typedef __attribute__((ext_vector_type(4))) u32 u32x4;
typedef __attribute__((ext_vector_type(2))) u32 u32x2;
typedef __attribute__((ext_vector_type(4))) short s16x4;
typedef __attribute__((ext_vector_type(8))) __bf16 bf16x8;
typedef __attribute__((ext_vector_type(2))) __bf16 bf16x2;
#define LAS __attribute__((address_space(3)))
#define GAS __attribute__((address_space(1)))
#define RLX_AGENT __ATOMIC_RELAXED, __HIP_MEMORY_SCOPE_AGENT
#define SB() __builtin_amdgcn_sched_barrier(0)
#define BAR() asm volatile("s_waitcnt lgkmcnt(0)\n\ts_barrier" ::: "memory")
#define MEMFENCE() asm volatile("" ::: "memory")
#define WAIT_VM(n) do { MEMFENCE(); __builtin_amdgcn_s_waitcnt(0x0F70 | ((n) & 15) | (((n) >> 4) << 14)); MEMFENCE(); } while (0)

constexpr int DM = 512;
constexpr int NS = 128;
constexpr u32 SENT = 0xffffffffu;
constexpr u32 SPIN_CAP = 20000;
#define FILL_GAP 70
#define R_STEPS 1
#define POLL_DELAY 40

constexpr u32 WS_EPOCH = 0;
constexpr u32 WS_FLAGS = 4096;
constexpr u32 WS_H = 1u << 20;
constexpr u32 WS_HS = 2u << 20;
constexpr u32 WS_USED = WS_H + 6 * WS_HS;

constexpr int T_COPY = 4160, T_CH = 8 * T_COPY;
constexpr int U_STRIDE = 4672;
constexpr int R_ROW = 1088;
constexpr int KP_WAVE = 2816;
constexpr int OFF_T = 0;
constexpr int OFF_U = OFF_T + 2 * T_CH;
constexpr int OFF_X = OFF_U + 2 * U_STRIDE;
constexpr int OFF_Y = OFF_X + 16384;
constexpr int OFF_R = OFF_Y + 8192;
constexpr int OFF_P = OFF_R + 28 * R_ROW;
constexpr int OFF_KP = OFF_P + 4096;
constexpr int OFF_KC = OFF_KP + 8 * KP_WAVE;
constexpr int OFF_RED = OFF_KC + 2 * 648 * 2;
constexpr int OFF_CNT = OFF_RED + 1024;
constexpr int OFF_ST = OFF_CNT + 64;
constexpr int LDS_BYTES = OFF_ST + 512;
static_assert(LDS_BYTES <= 163840, "LDS");

#define STAMP(i)

__device__ __forceinline__ float bf_lo(u32 p) { return __builtin_bit_cast(float, p << 16); }
__device__ __forceinline__ float bf_hi(u32 p) { return __builtin_bit_cast(float, p & 0xffff0000u); }
__device__ __forceinline__ float bf_u16(u16 p) { return __builtin_bit_cast(float, (u32)p << 16); }
__device__ __forceinline__ u32 pk_bf(float lo, float hi) {
    bf16x2 v = {(__bf16)lo, (__bf16)hi};
    return __builtin_bit_cast(u32, v);
}
__device__ __forceinline__ float as_f(u32 x) { return __builtin_bit_cast(float, x); }
__device__ __forceinline__ u32 as_u(float x) { return __builtin_bit_cast(u32, x); }
__device__ __forceinline__ float rbf(float x) { return bf_lo(pk_bf(x, x)); }
__device__ __forceinline__ void unpack8(u32x4 p, float (&f)[8]) {
#pragma unroll
    for (int i = 0; i < 4; ++i) { f[2 * i] = bf_lo(p[i]); f[2 * i + 1] = bf_hi(p[i]); }
}
__device__ __forceinline__ u32x4 pack8(const float (&f)[8]) {
    return (u32x4){pk_bf(f[0], f[1]), pk_bf(f[2], f[3]), pk_bf(f[4], f[5]), pk_bf(f[6], f[7])};
}
__device__ __forceinline__ u32x4 reverse8(u32x4 p) {
    return (u32x4){__builtin_amdgcn_alignbit(p[3], p[3], 16), __builtin_amdgcn_alignbit(p[2], p[2], 16),
                   __builtin_amdgcn_alignbit(p[1], p[1], 16), __builtin_amdgcn_alignbit(p[0], p[0], 16)};
}
template <int CTRL> __device__ __forceinline__ float dppf(float v) {
    return __builtin_bit_cast(float, __builtin_amdgcn_update_dpp(0, __builtin_bit_cast(int, v), CTRL, 0xf, 0xf, true));
}

__device__ __forceinline__ void lds_dma16(const char* src, u32 lds) {
    u32 keep;
    asm volatile("s_mov_b32 %0, m0\n\ts_mov_b32 m0, %2\n\ts_nop 0\n\tglobal_load_lds_dwordx4 %1, off\n\ts_mov_b32 m0, %0"
                 : "=&s"(keep) : "v"(src), "s"(lds) : "memory");
}

__device__ __forceinline__ u32 ld_u16(const void* base, u32 off) {
    u32 r;
    asm volatile("global_load_ushort %0, %1, %2" : "=v"(r) : "v"(off), "s"(base) : "memory");
    return r;
}
__device__ __forceinline__ u32x2 ld_b64(const void* base, u32 off) {
    u32x2 r;
    asm volatile("global_load_dwordx2 %0, %1, %2" : "=v"(r) : "v"(off), "s"(base) : "memory");
    return r;
}
__device__ __forceinline__ u32x4 ld_b128(const void* base, u32 off) {
    u32x4 r;
    asm volatile("global_load_dwordx4 %0, %1, %2" : "=v"(r) : "v"(off), "s"(base) : "memory");
    return r;
}

typedef __amdgpu_buffer_rsrc_t rsrc_t;
__device__ __forceinline__ u32x4 ld_dev(rsrc_t rs, u32 off) { return __builtin_amdgcn_raw_buffer_load_b128(rs, off, 0, 16); }
__device__ __forceinline__ void st_dev(rsrc_t rs, u32 off, u32x4 v) { __builtin_amdgcn_raw_buffer_store_b128(v, rs, off, 0, 16); }
__device__ __forceinline__ void publish(rsrc_t rs, u32 off, u32x4 v) {
#pragma unroll
    for (int i = 0; i < 4; ++i) v[i] = v[i] < SENT - 1u ? v[i] : SENT - 1u;
    st_dev(rs, off, v);
}
__device__ __forceinline__ u32 launch_tag(const char* ws) {
    u32 e;
    asm volatile("s_load_dword %0, %1, 0x0\n\ts_waitcnt lgkmcnt(0)" : "=&s"(e) : "s"(ws + WS_EPOCH) : "memory");
    return e + 1u;
}
__device__ __forceinline__ void wait_until(u64 t) {
    while (__builtin_amdgcn_s_memrealtime() < t) __builtin_amdgcn_s_sleep(1);
}
__device__ __forceinline__ u32x4 poll_issue(rsrc_t rs, u32 off) {
    MEMFENCE();
    u32x4 x = ld_dev(rs, off);
    MEMFENCE();
    return x;
}
struct Piece2 { u32x4 a, b; };
__device__ __forceinline__ Piece2 poll2_issue(rsrc_t rs, u32 off_a, u32 off_b) {
    MEMFENCE();
    Piece2 p = {ld_dev(rs, off_a), ld_dev(rs, off_b)};
    MEMFENCE();
    return p;
}
__device__ __forceinline__ bool has_sentinel(const u32x4& x) {
    u32 mx = x[0] > x[1] ? x[0] : x[1];
    mx = mx > x[2] ? mx : x[2];
    mx = mx > x[3] ? mx : x[3];
    return mx == SENT;
}
__device__ __forceinline__ void poll2_finish(Piece2& p, u32x4 desc, u32 off_a, u32 off_b) {
#pragma nounroll
    for (u32 spins = 0; spins < SPIN_CAP; ++spins) {
        const bool bad = has_sentinel(p.a) || has_sentinel(p.b);
        if (__builtin_amdgcn_ballot_w64(bad) == 0ull) break;
        __builtin_amdgcn_s_sleep(1);
        if (bad) asm volatile("buffer_load_dwordx4 %0, %2, %4, 0 offen sc1\n\tbuffer_load_dwordx4 %1, %3, %4, 0 offen sc1\n\ts_waitcnt vmcnt(0)"
                              : "+v"(p.a), "+v"(p.b) : "v"(off_a), "v"(off_b), "s"(desc) : "memory");
    }
}
__device__ __forceinline__ void flags_finish(u32x4& fl, rsrc_t rs, u32 off, u32 epoch) {
    u32 spins = 0;
    while (__builtin_amdgcn_ballot_w64(fl[0] != epoch || fl[1] != epoch || fl[2] != epoch || fl[3] != epoch) != 0ull) {
        if (++spins > SPIN_CAP) break;
        __builtin_amdgcn_s_sleep(2);
        fl = poll_issue(rs, off);
    }
}

__device__ __forceinline__ void cnt_arrive(char* smem, int k, int lane) {
    asm volatile("s_waitcnt lgkmcnt(0)" ::: "memory");
    if (lane == 0) __hip_atomic_fetch_add((LAS u32*)(smem + OFF_CNT) + k, 1u, __ATOMIC_RELAXED, __HIP_MEMORY_SCOPE_WORKGROUP);
}
__device__ __forceinline__ void cnt_wait(char* smem, int k, u32 target) {
    u32 spins = 0;
    while (__hip_atomic_load((LAS u32*)(smem + OFF_CNT) + k, __ATOMIC_RELAXED, __HIP_MEMORY_SCOPE_WORKGROUP) < target) {
        if (++spins > 50 * SPIN_CAP) break;
        __builtin_amdgcn_s_sleep(1);
    }
    MEMFENCE();
}

struct Rows { f32x2 a[4], b[4]; };
__device__ __forceinline__ void unpack_rows(u32x4 p, f32x2 (&f)[4]) {
#pragma unroll
    for (int i = 0; i < 4; ++i) f[i] = (f32x2){bf_lo(p[i]), bf_hi(p[i])};
}
__device__ __forceinline__ u32x4 pack_rows(const f32x2 (&f)[4]) {
    return (u32x4){pk_bf(f[0][0], f[0][1]), pk_bf(f[1][0], f[1][1]), pk_bf(f[2][0], f[2][1]), pk_bf(f[3][0], f[3][1])};
}
__device__ __forceinline__ u32x4 round_rows(f32x2 (&f)[4]) {
    u32x4 p = pack_rows(f);
    asm("" : "+v"(p));
    unpack_rows(p, f);
    return p;
}
__device__ __forceinline__ void layer_norm(const Rows& x, Rows& u, float* red, int lane, int w) {
#pragma clang fp contract(fast)
    u32 a[8], b[8];
#pragma unroll
    for (int i = 0; i < 4; ++i) {
        const f32x2 s1 = x.a[i] + x.b[i], s2 = x.a[i] * x.a[i] + x.b[i] * x.b[i];
        a[2 * i] = as_u(s1[0]); a[2 * i + 1] = as_u(s1[1]);
        b[2 * i] = as_u(s2[0]); b[2 * i + 1] = as_u(s2[1]);
    }
    SB();
#pragma unroll
    for (int j = 0; j < 8; ++j) {
        const auto s = __builtin_amdgcn_permlane32_swap(a[j], b[j], false, false);
        a[j] = s[0]; b[j] = s[1];
    }
    SB();
    float r[8];
#pragma unroll
    for (int j = 0; j < 8; ++j) r[j] = as_f(a[j]) + as_f(b[j]);
    SB();
#pragma unroll
    for (int j = 0; j < 4; ++j) {
        const auto s = __builtin_amdgcn_permlane16_swap(as_u(r[j]), as_u(r[j + 4]), false, false);
        a[j] = s[0]; b[j] = s[1];
    }
    SB();
    float s4[4];
#pragma unroll
    for (int j = 0; j < 4; ++j) s4[j] = as_f(a[j]) + as_f(b[j]);
    SB();
#pragma unroll
    for (int j = 0; j < 4; ++j) s4[j] += dppf<0x128>(s4[j]);
    const float t0 = (lane & 8) ? s4[2] : s4[0], t1 = (lane & 8) ? s4[3] : s4[1];
    const float c0 = t0 + dppf<0x141>(t0), c1 = t1 + dppf<0x141>(t1);
    float s = (lane & 4) ? c1 : c0;
    s += dppf<0xB1>(s);
    s += dppf<0x4E>(s);
    const u32 sb = as_u(s) < SENT - 1u ? as_u(s) : SENT - 1u;
    if ((lane & 3) == 0) ((volatile LAS u32*)(LAS char*)(char*)red)[(lane >> 2) * 4 + w] = sb;
    u32x4 p;
    u32 spins = 0;
    while (true) {
        p = *(volatile LAS u32x4*)((LAS char*)(char*)red + (lane & 15) * 16);
        u32 mx = p[0] > p[1] ? p[0] : p[1];
        mx = mx > p[2] ? mx : p[2];
        mx = mx > p[3] ? mx : p[3];
        if (__builtin_amdgcn_ballot_w64(mx == SENT) == 0ull) break;
        if (++spins > 50 * SPIN_CAP) break;
    }
    const float m = ((as_f(p[0]) + as_f(p[1])) + (as_f(p[2]) + as_f(p[3]))) * (1.0f / DM);
    const float ex2 = dppf<0x128>(m);
    const float rstd = __builtin_amdgcn_rsqf(fmaxf(fmaf(-m, m, ex2), 0.f) + 1e-5f);
    const float off = -m * rstd;
    const f32x2 R[4] = {{dppf<0x150>(rstd), dppf<0x151>(rstd)}, {dppf<0x152>(rstd), dppf<0x153>(rstd)},
                        {dppf<0x154>(rstd), dppf<0x155>(rstd)}, {dppf<0x156>(rstd), dppf<0x157>(rstd)}};
    const f32x2 O[4] = {{dppf<0x150>(off), dppf<0x151>(off)}, {dppf<0x152>(off), dppf<0x153>(off)},
                        {dppf<0x154>(off), dppf<0x155>(off)}, {dppf<0x156>(off), dppf<0x157>(off)}};
#pragma unroll
    for (int i = 0; i < 4; ++i) { u.a[i] = x.a[i] * R[i] + O[i]; u.b[i] = x.b[i] * R[i] + O[i]; }
}

__device__ __forceinline__ void stage_taps(char* smem, int ch, int i, u32x4 prev, u32x4 own) {
    const u32 D[8] = {prev[0], prev[1], prev[2], prev[3], own[0], own[1], own[2], own[3]};
    u32 O[7];
#pragma unroll
    for (int y = 0; y < 7; ++y) O[y] = __builtin_amdgcn_alignbit(D[y + 1], D[y], 16);
    char* base = smem + OFF_T + ch * T_CH + (i + 1) * 16;
#pragma unroll
    for (int c = 0; c < 8; ++c) {
        const int y = (c + 1) >> 1;
        const u32x4 v = (c & 1) ? (u32x4){D[y], D[y + 1], D[y + 2], D[y + 3]} : (u32x4){O[y], O[y + 1], O[y + 2], O[y + 3]};
        *(u32x4*)(base + c * T_COPY) = v;
    }
}

__device__ __forceinline__ f32x4 mfma16(u32x4 a, u32x4 b, f32x4 c) {
    return __builtin_amdgcn_mfma_f32_16x16x32_bf16(__builtin_bit_cast(bf16x8, a), __builtin_bit_cast(bf16x8, b), c, 0, 0, 0);
}
struct KState { f32x4 acc[3]; };
__device__ __forceinline__ void kgen_init(KState& ks) {
#pragma unroll
    for (int e = 0; e < 3; ++e) ks.acc[e] = (f32x4){0, 0, 0, 0};
}
#define OPQ(x) asm volatile("" : "+v"(x))
template <int NS_, bool PK>
__device__ __forceinline__ void kgen_steps(const f32x4* P, int lane, KState& ks) {
#pragma clang fp contract(fast)
    const int g = lane >> 4, m = lane & 15;
    const float lo = (float)(8 * m);
    const float hi[3] = {(float)(128 * (m >> 3) + (m & 7)),
                         (float)(m < 8 ? 256 + 128 * (m >> 2) + 2 * (m & 3) : 512 + 128 * ((m - 8) >> 1) + 4 * (m & 1)),
                         (float)(m < 8 ? 1024 + 128 * m : 2048)};
    auto eval = [&](f32x2 ex, f32x2 an, bool conj, u32& o0, u32& o1) {
        if (!PK) { OPQ(ex[0]); OPQ(ex[1]); OPQ(an[0]); OPQ(an[1]); }
        const float m0 = __builtin_amdgcn_exp2f(ex[0]), m1 = __builtin_amdgcn_exp2f(ex[1]);
        const float f0 = __builtin_amdgcn_fractf(an[0]), f1 = __builtin_amdgcn_fractf(an[1]);
        const float c0 = __builtin_amdgcn_cosf(f0), c1 = __builtin_amdgcn_cosf(f1);
        const float s0 = __builtin_amdgcn_sinf(f0), s1 = __builtin_amdgcn_sinf(f1);
        float r0, r1, i0, i1;
        if (PK) {
            const f32x2 r = (f32x2){c0, c1} * (f32x2){m0, m1}, i = (f32x2){s0, s1} * (f32x2){m0, m1};
            r0 = r[0]; r1 = r[1]; i0 = i[0]; i1 = i[1];
        } else {
            r0 = c0 * m0; OPQ(r0); i0 = s0 * m0; OPQ(i0); r1 = c1 * m1; OPQ(r1); i1 = s1 * m1; OPQ(i1);
        }
        o0 = pk_bf(r0, conj ? -i0 : i0);
        o1 = pk_bf(r1, conj ? -i1 : i1);
    };
    auto affine = [&](f32x2 a, float p, f32x2 b) -> f32x2 {
        if (PK) return a * p + b;
        float x = fmaf(a[0], p, b[0]), y = fmaf(a[1], p, b[1]);
        OPQ(x); OPQ(y);
        return (f32x2){x, y};
    };
#pragma unroll
    for (int s = 0; s < NS_; ++s) {
        u32x4 B, A[3];
#pragma unroll
        for (int h = 0; h < 2; ++h) {
            const f32x4 p0 = P[16 * s + 4 * g + 2 * h], p1 = P[16 * s + 4 * g + 2 * h + 1];
            u32 o0, o1;
            eval(affine(p0.xy, lo, (f32x2){0.f, 0.f}), affine(p0.zw, lo, (f32x2){0.f, 0.f}), true, o0, o1);
            B[2 * h] = o0; B[2 * h + 1] = o1;
#pragma unroll
            for (int e = 0; e < 3; ++e) {
                eval(affine(p0.xy, hi[e], p1.xy), affine(p0.zw, hi[e], p1.zw), false, o0, o1);
                A[e][2 * h] = o0; A[e][2 * h + 1] = o1;
            }
        }
#pragma unroll
        for (int e = 0; e < 3; ++e) ks.acc[e] = mfma16(A[e], B, ks.acc[e]);
    }
}
__device__ __forceinline__ const f32x4* kgen_params(char* smem, int wave, int step) {
    return (const f32x4*)(smem + OFF_P) + ((wave >> 1) & 1) * 128 + ((wave & 1) * 4 + step) * 16;
}
__device__ __forceinline__ void kgen_save(char* smem, int wave, int lane, const KState& ks) {
    char* p = smem + OFF_KP + wave * KP_WAVE + lane * 16;
    *(f32x4*)p = ks.acc[0];
    *(f32x4*)(p + 1024) = ks.acc[1];
    if (lane < 48) *(f32x4*)(p + 2048) = ks.acc[2];
}
__device__ __forceinline__ f32x4 kgen_sum(const char* p) {
    return (*(const f32x4*)p + *(const f32x4*)(p + KP_WAVE)) + (*(const f32x4*)(p + 4 * KP_WAVE) + *(const f32x4*)(p + 5 * KP_WAVE));
}
__device__ __forceinline__ void kgen_reduce(char* smem, int wave, int lane, u32 dskip) {
    const int c = wave >> 1, j = lane & 15, g = lane >> 4;
    char* KC = smem + OFF_KC + c * 1296;
    const char* p = smem + OFF_KP + 2 * c * KP_WAVE + lane * 16;
    if ((wave & 1) == 0) {
        f32x4 s = kgen_sum(p);
        const f32x4 t = kgen_sum(smem + OFF_KP + 2 * c * KP_WAVE + 2048 + (lane < 48 ? lane : 0) * 16);
        if (lane == 0) s[0] += c ? bf_hi(dskip) : bf_lo(dskip);
        *(u32x2*)(KC + 2 * (128 * (g >> 1) + 4 * (g & 1) + 8 * j)) = (u32x2){pk_bf(s[0], s[1]), pk_bf(s[2], s[3])};
        const u32 lo = pk_bf(t[0], t[1]), hi = pk_bf(t[2], t[3]);
        u16* o = (u16*)KC + 512 + 64 * g + j;
        if (g < 2) { o[0] = (u16)lo; o[16] = (u16)(lo >> 16); o[32] = (u16)hi; o[48] = (u16)(hi >> 16); }
        else if (lane == 32) { ((u16*)KC)[640] = (u16)lo; ((u16*)KC)[641] = 0; }
    } else {
        const f32x4 s = kgen_sum(p + 1024);
        const u32 lo = pk_bf(s[0], s[1]), hi = pk_bf(s[2], s[3]);
        if (g < 2) *(u32x2*)(KC + 2 * (256 + 64 * g + 4 * j)) = (u32x2){lo, hi};
        else {
            u32* o = (u32*)(KC + 2 * (384 + 64 * (g - 2) + 2 * j));
            o[0] = lo; o[16] = hi;
        }
    }
}

struct TapIndex {
    u16 off[2048 + 8];
    constexpr TapIndex() : off() {
        for (int i = 0; i < 8; ++i) off[i] = 641 * 2;
        for (int l = 0; l < 2048; ++l) {
            int e = 0;
            while ((l >> e) >= 256) ++e;
            int v = l >> e;
            if (e > 0) {
                const int rem = l & ((1 << e) - 1), half = 1 << (e - 1);
                if (rem > half || (rem == half && (v & 1))) ++v;
            }
            off[8 + l] = (u16)(2 * (128 * e + v));
        }
    }
};
__device__ const TapIndex tap_index = TapIndex();

__device__ __forceinline__ void s4_taps(char* smem, int ch, int i, u32x4 tix0, u32x4 tix1) {
    const char* kc = smem + OFF_KC + ch * 1296;
    u32 d[8];
#pragma unroll
    for (int j = 0; j < 8; ++j) {
        const u32 o = j < 4 ? tix0[j] : tix1[j - 4];
        d[j] = (u32)*(const u16*)(kc + (o & 0xffffu)) | ((u32)*(const u16*)(kc + (o >> 16)) << 16);
    }
    stage_taps(smem, ch, i, (u32x4){d[0], d[1], d[2], d[3]}, (u32x4){d[4], d[5], d[6], d[7]});
}

struct Frags { u32x4 A[8], B[8]; };
template <bool INIT> __device__ __forceinline__ void mma(f32x4& acc, const u32x4& a, const u32x4& b) {
    if constexpr (INIT) asm volatile("v_mfma_f32_16x16x32_bf16 %0, %1, %2, 0" : "=&v"(acc) : "v"(a), "v"(b));
    else asm volatile("v_mfma_f32_16x16x32_bf16 %0, %1, %2, %0" : "+v"(acc) : "v"(a), "v"(b));
}
template <int I> __device__ __forceinline__ void load_one(Frags& f, const char* ap, const char* bp) {
    if constexpr (I == 0) f.A[0] = *(const u32x4*)ap;
    else if constexpr (I <= 8) f.B[I - 1] = *(const u32x4*)(bp + 512 * (8 - I));
    else f.A[I - 8] = *(const u32x4*)(ap + 512 * (I - 8));
}
template <int I> __device__ __forceinline__ void load_all(Frags& f, const char* ap, const char* bp) {
    if constexpr (I < 16) { load_one<I>(f, ap, bp); SB(); load_all<I + 1>(f, ap, bp); }
}
template <int T0, int T1, int T2, int T3> struct TileSet {
    static constexpr int tile(int i) { return i == 0 ? T0 : i == 1 ? T1 : i == 2 ? T2 : T3; }
    static constexpr int walk(int I, bool want_tile) {
        int n = 0;
        for (int a = 0; a < 8; ++a)
            for (int j = 0; j < 4; ++j)
                if (tile(j) >= a) { if (n == I) return want_tile ? tile(j) : a; ++n; }
        return -1;
    }
};
constexpr int tri_a(int I) { int a = 0; while (I >= 8 - a) { I -= 8 - a; ++a; } return a; }
constexpr int tri_b(int I) { int a = 0; while (I >= 8 - a) { I -= 8 - a; ++a; } return I; }
template <int I, bool FIRST> __device__ __forceinline__ void class_step(const Frags& cur, Frags& nxt, f32x4 (&acc)[8], const char* ap, const char* bp) {
    if constexpr (I < 36) {
        constexpr int a = tri_a(I), b = tri_b(I);
        mma<FIRST && a == 0>(acc[a + b], cur.A[a], cur.B[b]);
        if constexpr ((I & 1) == 1 && (I >> 1) < 16) load_one<(I >> 1)>(nxt, ap, bp);
        SB();
        class_step<I + 1, FIRST>(cur, nxt, acc, ap, bp);
    }
}
template <class TS, int I, int TO, int PRE> __device__ __forceinline__ void set_step(const Frags& cur, Frags& nxt, f32x4 (&acc)[8], const char* ap, const char* bp) {
    if constexpr (I < TO) {
        constexpr int t = TS::walk(I, true), a = TS::walk(I, false);
        mma<false>(acc[t], cur.A[a], cur.B[t - a]);
        if constexpr (PRE == 1 && (I * 11) / 18 != ((I + 1) * 11) / 18) load_one<(I * 11) / 18>(nxt, ap, bp);
        if constexpr (PRE == 2 && I < 5) load_one<11 + I>(nxt, ap, bp);
        SB();
        set_step<TS, I + 1, TO, PRE>(cur, nxt, acc, ap, bp);
    }
}
template <class TS, int J> __device__ __forceinline__ void tile_step(const Frags& cur, f32x4 (&acc)[8]) {
    constexpr int t = TS::tile(J);
#pragma unroll
    for (int a = 0; a <= t; ++a) { mma<false>(acc[t], cur.A[a], cur.B[t - a]); SB(); }
}
template <class TS, int J> __device__ __forceinline__ void tile_out(char* Y, f32x4 (&acc)[8], const f32x4 (&o)[4]) {
    constexpr int t = TS::tile(J);
    const f32x4 s = acc[t] + o[J];
    const u32x2 pk = {pk_bf(s[0], s[1]), pk_bf(s[2], s[3])};
    *(u32x2*)(Y + 512 * t) = pk;
    SB();
}
template <class SEND, class KEEP>
__device__ __forceinline__ void conv_half(char* smem, const u32x4 (&A0)[8], const char* ap, const char* bp, int wave, int lane, int ch, u32 seq) {
    f32x4 acc[8];
    Frags f0, f1;
#pragma unroll
    for (int a = 0; a < 8; ++a) f0.A[a] = A0[a];
#pragma unroll
    for (int b = 0; b < 8; ++b) { f0.B[b] = *(const u32x4*)(bp + 512 * (7 - b)); SB(); }
    class_step<0, true>(f0, f1, acc, ap + 64, bp + 64);
    class_step<0, false>(f1, f0, acc, ap + 128, bp + 128);
    set_step<SEND, 0, 18, 1>(f0, f1, acc, ap + 192, bp + 192);
    set_step<SEND, 0, 18, 2>(f1, f1, acc, ap + 192, bp + 192);
    set_step<KEEP, 0, 4, 0>(f0, f1, acc, ap, bp);
    asm volatile("s_nop 3" : "+v"(acc[SEND::tile(0)]), "+v"(acc[SEND::tile(1)]), "+v"(acc[SEND::tile(2)]), "+v"(acc[SEND::tile(3)]));
    f32x4* X = (f32x4*)(smem + OFF_X);
#pragma unroll
    for (int i = 0; i < 4; ++i) X[(wave * 4 + i) * 64 + lane] = acc[SEND::tile(i)];
    volatile LAS u32* xf = (volatile LAS u32*)(LAS char*)(smem + OFF_CNT + 32);
    xf[wave] = seq;
    SB();
    set_step<KEEP, 4, 14, 0>(f0, f1, acc, ap, bp);
    u32 seen = xf[wave ^ 1];
    SB();
    set_step<KEEP, 14, 18, 0>(f0, f1, acc, ap, bp);
    for (u32 spins = 0; seen < seq && spins < 50 * SPIN_CAP; ++spins) seen = xf[wave ^ 1];
    SB();
    f32x4 o[4];
#pragma unroll
    for (int i = 0; i < 4; ++i) o[i] = X[((wave ^ 1) * 4 + i) * 64 + lane];
    SB();
    char* Y = smem + OFF_Y + ch * 4096 + (16 * (lane & 15) + 4 * (lane >> 4)) * 2;
    tile_step<KEEP, 0>(f1, acc);
    tile_step<KEEP, 1>(f1, acc);
    tile_step<KEEP, 2>(f1, acc);
    asm volatile("s_nop 3" : "+v"(acc[KEEP::tile(0)]), "+v"(acc[KEEP::tile(1)]));
    tile_out<KEEP, 0>(Y, acc, o);
    tile_out<KEEP, 1>(Y, acc, o);
    tile_step<KEEP, 3>(f1, acc);
    asm volatile("" : "+v"(acc[KEEP::tile(2)]));
    tile_out<KEEP, 2>(Y, acc, o);
    asm volatile("s_nop 7" : "+v"(acc[KEEP::tile(3)]));
    tile_out<KEEP, 3>(Y, acc, o);
}
struct ConvAddr { const char* ap; const char* bp; };
__device__ __forceinline__ ConvAddr conv_addr(char* smem, int wave, int lane) {
    const int ch = (wave >> 1) & 1, hf = wave & 1;
    const int m = lane & 15, g = lane >> 4;
    ConvAddr c;
    c.ap = smem + OFF_T + ch * T_CH + (m & 7) * T_COPY + 16 * (m >> 3) + 16 * g + 256 * hf;
    c.bp = smem + OFF_U + ch * U_STRIDE + 4064 - 32 * m + 16 * g + 256 * hf - 512 * 7;
    return c;
}
__device__ __forceinline__ void conv_preload(u32x4 (&A0)[8], const ConvAddr& c) {
#pragma unroll
    for (int a = 0; a < 8; ++a) A0[a] = *(const u32x4*)(c.ap + 512 * a);
}
template <bool EARLY>
__device__ __forceinline__ void conv_col(char* smem, const ConvAddr& c, int wave, int lane, u32 seq) {
    u32x4 A0[8];
    if (EARLY) conv_preload(A0, c);
    BAR();
    if (!EARLY) conv_preload(A0, c);
    if ((wave & 1) == 0) conv_half<TileSet<1, 3, 4, 6>, TileSet<0, 2, 5, 7>>(smem, A0, c.ap, c.bp, wave, lane, wave >> 1, seq);
    else conv_half<TileSet<0, 2, 5, 7>, TileSet<1, 3, 4, 6>>(smem, A0, c.ap, c.bp, wave, lane, wave >> 1, seq);
    STAMP(26 + seq);
    BAR();
}
template <class PRE>
__device__ __forceinline__ void conv_row(char* smem, int wave, int lane, rsrc_t rs, int id, int fill0, int st, PRE pre) {
    const u32x4 sent = {SENT, SENT, SENT, SENT};
    const u32 off = WS_H + (fill0 < 0 ? 0 : fill0) * WS_HS + id * 8192 + ((wave - 4) * 64 + lane) * 16;
    const u64 t0 = __builtin_amdgcn_s_memrealtime();
    if (fill0 >= 0) { st_dev(rs, off, sent); st_dev(rs, off + 4096, sent); }
    pre();
    if (fill0 >= 0) {
        wait_until(t0 + FILL_GAP);
        st_dev(rs, off + WS_HS, sent); st_dev(rs, off + WS_HS + 4096, sent);
        WAIT_VM(2);
    }
    STAMP(st);
    BAR();
}

__global__ void __launch_bounds__(512, 2)
h3s4_kernel(const char* __restrict__ q, const char* __restrict__ k, const char* __restrict__ v,
            const u32x2* __restrict__ shiftk, const u32x4* __restrict__ ssmk, const u32* __restrict__ ssmD,
            const u16* __restrict__ Are, const u16* __restrict__ Aim, const u16* __restrict__ Bre, const u16* __restrict__ Bim,
            const u16* __restrict__ Cre, const u16* __restrict__ Cim, const u16* __restrict__ logdt, const u32* __restrict__ Dskip,
            u16* __restrict__ out, char* ws
            ) {
    __shared__ __attribute__((aligned(16))) char smem[LDS_BYTES];
    const int tid = threadIdx.x, lane = tid & 63, wave = __builtin_amdgcn_readfirstlane(tid >> 6);
    const int id = (blockIdx.x & 7) * 32 + (blockIdx.x >> 3);
    const rsrc_t rs = __builtin_amdgcn_make_buffer_rsrc(ws, 0, WS_USED, 0x00020000);
    const u32x4 desc = {(u32)(size_t)ws, (u32)((size_t)ws >> 32) & 0xffffu, WS_USED, 0x00020000u};
    STAMP(0);

    u32 code_pf = 0;
    {
        u64 pc; u32 len;
        asm volatile("s_getpc_b64 %0\nh3s4_text_a:\n\ts_mov_b32 %1, h3s4_text_b - h3s4_text_a" : "=s"(pc), "=s"(len));
        const u32 off = ((blockIdx.x >> 3) * 8 + wave) * 256 + lane * 4;
        if (off + 4 <= len) asm volatile("global_load_dword %0, %1, %2" : "=v"(code_pf) : "v"(off), "s"(pc) : "memory");
    }
    const u32x4 sent = {SENT, SENT, SENT, SENT};
    const u32 dA = ssmD[id], dB = Dskip[id];
    MEMFENCE(); SB();
    st_dev(rs, WS_H + 0 * WS_HS + id * 8192 + tid * 16, sent);
    MEMFENCE(); SB();
#pragma unroll
    for (int s = 0; s < 4; ++s) {
        const int slot = wave + 8 * s;
        if (slot < 28) {
            int row = 8 * id + (slot < 12 ? slot - 4 : slot < 20 ? slot - 12 : slot - 20);
            row = row < 0 ? 0 : row;
            const char* src = slot < 12 ? k : slot < 20 ? v : q;
            lds_dma16(src + row * 1024 + lane * 16, (u32)(size_t)(LAS char*)(smem + OFF_R) + slot * R_ROW);
        }
    }

    if (wave < 4) {
        u32x4 tap_own = ld_b128(ssmk, (2 * id) * 4096 + tid * 16), tap_prev = ld_b128(ssmk, (2 * id) * 4096 + (tid ? tid - 1 : 0) * 16);
        const int pd = 2 * id + (tid >> 7), po = (pd * NS + (tid & 127)) * 2;
        u32 r_dt = ld_u16(logdt, pd * 2), r_are = ld_u16(Are, po), r_aim = ld_u16(Aim, po), r_bre = ld_u16(Bre, po);
        u32 r_bim = ld_u16(Bim, po), r_cre = ld_u16(Cre, po), r_cim = ld_u16(Cim, po);
        MEMFENCE(); SB();
        st_dev(rs, WS_H + 1 * WS_HS + id * 8192 + tid * 16, sent);
        MEMFENCE(); SB();
        const u32 epoch = launch_tag(ws);
        asm volatile("s_waitcnt vmcnt(13)\n\ts_barrier" ::: "memory");
        if (tid < 32) __hip_atomic_store((GAS u32*)(ws + WS_FLAGS) + tid * 256 + id, epoch, RLX_AGENT);
        STAMP(19);
        if (tid < 60) *(u32x4*)(smem + OFF_U + (tid / 30) * U_STRIDE + 4096 + (tid % 30) * 16) = (u32x4){0, 0, 0, 0};
        else if (tid >= 64 && tid < 80) *(u32x4*)(smem + OFF_T + (tid - 64) * T_COPY) = (u32x4){0, 0, 0, 0};
        asm volatile("s_waitcnt vmcnt(1)" : "+v"(tap_own), "+v"(tap_prev), "+v"(r_dt), "+v"(r_are), "+v"(r_aim), "+v"(r_bre), "+v"(r_bim), "+v"(r_cre), "+v"(r_cim) :: "memory");
        BAR();
        STAMP(1);

        {
            const float dt = rbf(__builtin_amdgcn_exp2f(bf_lo(r_dt) * 1.4426950408889634f));
            const float are = dt * bf_lo(r_are), aim = dt * bf_lo(r_aim);
            const float br = dt * bf_lo(r_bre), bi = dt * bf_lo(r_bim);
            const float cr = bf_lo(r_cre), ci = bf_lo(r_cim);
            const float wr = cr * br - ci * bi, wi = cr * bi + ci * br;
            const float mx = fmaxf(fabsf(wr), fabsf(wi)), mn = fminf(fabsf(wr), fabsf(wi));
            const float ratio = mx > 0.f ? mn / mx : 0.f;
            float* p = (float*)(smem + OFF_P) + (tid >> 1) * 8 + (tid & 1);
            p[0] = are * 1.4426950408889634f;
            p[2] = aim * 0.15915494309189535f;
            p[4] = __builtin_amdgcn_logf(mx) + 0.5f * __builtin_amdgcn_logf(fmaf(ratio, ratio, 1.0f));
            p[6] = atan2f(wi, wr) * 0.15915494309189535f;
        }
        if (tid == 0) {
            tap_prev = (u32x4){0, 0, 0, 0};
            tap_own[0] = (tap_own[0] & 0xffff0000u) | (pk_bf(bf_lo(tap_own[0]) + bf_lo(dA), 0.f) & 0xffffu);
        }
        stage_taps(smem, 0, tid, tap_prev, tap_own);
        cnt_arrive(smem, 0, lane);
        STAMP(2);

        const ConvAddr ca = conv_addr(smem, wave, lane);
        WAIT_VM(0);
        asm volatile("" :: "v"(code_pf));
        cnt_wait(smem, 0, 8);
        STAMP(4);
#pragma nounroll
        for (u32 seq = 1; seq <= 3; ++seq) {
            if (seq == 2) {
                const u32x4 tix0 = ((const u32x4*)tap_index.off)[tid], tix1 = ((const u32x4*)tap_index.off)[tid + 1];
                {
                    KState ks;
                    kgen_init(ks);
                    kgen_steps<4 - R_STEPS, true>(kgen_params(smem, wave, 0), lane, ks);
                    kgen_save(smem, wave, lane, ks);
                }
                STAMP(7);
                cnt_arrive(smem, 1, lane);
                cnt_wait(smem, 1, 8);
                kgen_reduce(smem, wave, lane, dB);
                cnt_arrive(smem, 2, lane);
                cnt_wait(smem, 2, 4);
                STAMP(23);
                s4_taps(smem, 0, tid, tix0, tix1);
                cnt_arrive(smem, 3, lane);
                cnt_wait(smem, 3, 8);
                STAMP(25);
            }
            conv_col<true>(smem, ca, wave, lane, seq);
            STAMP(seq == 1 ? 6 : seq == 2 ? 11 : 15);
        }
    } else {
        const int r = tid - 256, w = wave - 4;
        u32x2 skA = ld_b64(shiftk, r * 8), skB = ld_b64(shiftk, (r + 256) * 8);
        u32x4 tap_own = ld_b128(ssmk, (2 * id + 1) * 4096 + r * 16), tap_prev = ld_b128(ssmk, (2 * id + 1) * 4096 + (r ? r - 1 : 0) * 16);
        MEMFENCE(); SB();
        st_dev(rs, WS_H + 1 * WS_HS + id * 8192 + tid * 16, sent);
        MEMFENCE(); SB();
        const u32 epoch = launch_tag(ws);
        asm volatile("s_waitcnt vmcnt(7)\n\ts_barrier" ::: "memory");
        __builtin_amdgcn_s_setprio(2);
        ((u32*)(smem + OFF_RED))[r] = SENT;
        if (r < 16) ((u32*)(smem + OFF_CNT))[r] = 0u;
        asm volatile("s_waitcnt vmcnt(1)" : "+v"(skA), "+v"(skB), "+v"(tap_own), "+v"(tap_prev) :: "memory");
        BAR();
        STAMP(1);

        const u32 own_a = id * 8192 + r * 16, own_b = own_a + 4096;
        const u32 col_a = (r >> 1) * 8192 + (2 * id + (r & 1)) * 16, col_b = col_a + 128 * 8192;
        const u32 row_a = (r >> 1) * 8192 + id * 32 + (r & 1) * 16, row_b = row_a + 128 * 8192;
        char* const u_dst = smem + OFF_U + (r & 1) * U_STRIDE + 4080 - (r >> 1) * 16;
        const char* const y_src = smem + OFF_Y + (r & 1) * 4096 + (r >> 1) * 16;
        float* red = (float*)(smem + OFF_RED);

        const u32 flag_off = WS_FLAGS + (blockIdx.x & 31) * 1024 + lane * 16;
        u32x4 fl = {epoch, epoch, epoch, epoch};
        if (wave == 7) fl = poll_issue(rs, flag_off);

        Rows x, u0;
        {
            const u32 rp = (u32)(size_t)(LAS char*)(smem + OFF_R + ((lane & 15) >> 2) * R_ROW + (64 * w + 16 * (lane >> 4) + 4 * (lane & 3)) * 2);
            u32x2 kk[2][3], vv[2][2], qq[2][2];
            static_assert(4 * R_ROW == 4352, "offsets below");
            asm volatile("ds_read_b64_tr_b16 %0, %10\n\tds_read_b64_tr_b16 %1, %10 offset:4352\n\tds_read_b64_tr_b16 %2, %10 offset:8704\n\t"
                         "ds_read_b64_tr_b16 %3, %10 offset:13056\n\tds_read_b64_tr_b16 %4, %10 offset:17408\n\t"
                         "ds_read_b64_tr_b16 %5, %10 offset:512\n\tds_read_b64_tr_b16 %6, %10 offset:4864\n\tds_read_b64_tr_b16 %7, %10 offset:9216\n\t"
                         "ds_read_b64_tr_b16 %8, %10 offset:13568\n\tds_read_b64_tr_b16 %9, %10 offset:17920\n\ts_waitcnt lgkmcnt(0)"
                         : "=&v"(kk[0][0]), "=&v"(kk[0][1]), "=&v"(kk[0][2]), "=&v"(vv[0][0]), "=&v"(vv[0][1]),
                           "=&v"(kk[1][0]), "=&v"(kk[1][1]), "=&v"(kk[1][2]), "=&v"(vv[1][0]), "=&v"(vv[1][1]) : "v"(rp) : "memory");
            const u32 first = id ? 0xffffffffu : 0u;
            u32x4 o[2];
#pragma unroll
            for (int c = 0; c < 2; ++c) {
                const u32x2 sk = c ? skB : skA;
                const u32 kp = kk[c][0][0] & first, kq = kk[c][0][1] & first;
                const float kf[12] = {0.f, bf_hi(kp), bf_lo(kq), bf_hi(kq), bf_lo(kk[c][1][0]), bf_hi(kk[c][1][0]),
                                      bf_lo(kk[c][1][1]), bf_hi(kk[c][1][1]), bf_lo(kk[c][2][0]), bf_hi(kk[c][2][0]),
                                      bf_lo(kk[c][2][1]), bf_hi(kk[c][2][1])};
                float vf[8];
                unpack8((u32x4){vv[c][0][0], vv[c][0][1], vv[c][1][0], vv[c][1][1]}, vf);
                const float w0 = bf_lo(sk[0]), w1 = bf_hi(sk[0]), w2 = bf_lo(sk[1]), w3 = bf_hi(sk[1]);
                float kv[8];
#pragma unroll
                for (int j = 0; j < 8; ++j) kv[j] = fmaf(kf[j + 4], w0, fmaf(kf[j + 3], w1, fmaf(kf[j + 2], w2, kf[j + 1] * w3))) * vf[j];
                o[c] = pack8(kv);
            }
            publish(rs, WS_H + 0 * WS_HS + own_a, o[0]);
            publish(rs, WS_H + 0 * WS_HS + own_b, o[1]);
            asm volatile("ds_read_b64_tr_b16 %0, %4 offset:21760\n\tds_read_b64_tr_b16 %1, %4 offset:26112\n\t"
                         "ds_read_b64_tr_b16 %2, %4 offset:22272\n\tds_read_b64_tr_b16 %3, %4 offset:26624\n\ts_waitcnt lgkmcnt(0)"
                         : "=&v"(qq[0][0]), "=&v"(qq[0][1]), "=&v"(qq[1][0]), "=&v"(qq[1][1]) : "v"(rp) : "memory");
            unpack_rows((u32x4){qq[0][0][0], qq[0][0][1], qq[0][1][0], qq[0][1][1]}, x.a);
            unpack_rows((u32x4){qq[1][0][0], qq[1][0][1], qq[1][1][0], qq[1][1][1]}, x.b);
        }
        u64 t_s = __builtin_amdgcn_s_memrealtime();
        STAMP(2);
        u32 touch = 0;
        if (wave == 6) {
            if (lane < 4) touch = __builtin_amdgcn_raw_buffer_load_b32(rs, WS_H + (2 + lane) * WS_HS + id * 8192, 0, 16);
            else if (lane == 4) touch = ((const u32*)out)[id * 2048];
        }
        if (r == 0) {
            tap_prev = (u32x4){0, 0, 0, 0};
            tap_own[0] = (tap_own[0] & 0xffff0000u) | (pk_bf(bf_lo(tap_own[0]) + bf_hi(dA), 0.f) & 0xffffu);
        }
        stage_taps(smem, 1, r, tap_prev, tap_own);
        cnt_arrive(smem, 0, lane);
        if (wave == 7) {
            flags_finish(fl, rs, flag_off, epoch);
            if (blockIdx.x == 0 && lane == 0) __hip_atomic_store((GAS u32*)(ws + WS_EPOCH), epoch, RLX_AGENT);
            cnt_arrive(smem, 4, lane);
        }
        cnt_wait(smem, 4, 1);
        STAMP(3);

        wait_until(t_s + POLL_DELAY);
        Piece2 pc = poll2_issue(rs, WS_H + col_a, WS_H + col_b), pr;
        Rows yA, u;
        auto stage_input = [&](u32 h) {
            poll2_finish(pc, desc, WS_H + h * WS_HS + col_a, WS_H + h * WS_HS + col_b);
            *(u32x4*)u_dst = reverse8(pc.a);
            *(u32x4*)(u_dst - 2048) = reverse8(pc.b);
        };
        auto send_result = [&](u32 h) {
            publish(rs, WS_H + h * WS_HS + own_a, *(const u32x4*)y_src);
            publish(rs, WS_H + h * WS_HS + own_b, *(const u32x4*)(y_src + 2048));
            t_s = __builtin_amdgcn_s_memrealtime();
        };
        auto fetch_rows = [&](u32 h) {
            pr = poll2_issue(rs, WS_H + h * WS_HS + row_a, WS_H + h * WS_HS + row_b);
            poll2_finish(pr, desc, WS_H + h * WS_HS + row_a, WS_H + h * WS_HS + row_b);
        };
#pragma nounroll
        for (u32 it = 0; it < 2; ++it) {
            layer_norm(x, u0, red + 128 * it, lane, w);
            u32x4 tix0, tix1;
            if (it == 0) {
                stage_input(0);
                STAMP(21);
                asm volatile("" :: "v"(touch));
                asm volatile("" :: "v"(code_pf));
                BAR();
                STAMP(5);
                if (wave == 7 && lane < 32) __hip_atomic_store((GAS u32*)(ws + WS_FLAGS + 32768) + lane * 256 + id, epoch, RLX_AGENT);
                conv_row(smem, wave, lane, rs, id, 2, 27, [&] {
                    __builtin_amdgcn_s_setprio(0);
                    KState ks;
                    kgen_init(ks);
                    kgen_steps<R_STEPS, false>(kgen_params(smem, wave, 4 - R_STEPS), lane, ks);
                    kgen_save(smem, wave, lane, ks);
                    cnt_arrive(smem, 1, lane);
                    __builtin_amdgcn_s_setprio(2);
                    if (wave == 7) fl = poll_issue(rs, flag_off + 32768);
                });
                send_result(1);
                STAMP(6);
                tix0 = ((const u32x4*)tap_index.off)[r]; tix1 = ((const u32x4*)tap_index.off)[r + 1];
                if (wave == 7) {
                    flags_finish(fl, rs, flag_off + 32768, epoch);
                    cnt_arrive(smem, 5, lane);
                }
                wait_until(t_s + POLL_DELAY);
                cnt_wait(smem, 5, 1);
                fetch_rows(1);
                STAMP(8);
                unpack_rows(pr.a, yA.a);
                unpack_rows(pr.b, yA.b);
            }
#pragma unroll
            for (int i = 0; i < 4; ++i) { x.a[i] += u0.a[i] * yA.a[i]; x.b[i] += u0.b[i] * yA.b[i]; }
            round_rows(x.a);
            round_rows(x.b);
            layer_norm(x, u, red + 128 * it + 64, lane, w);
            publish(rs, WS_H + (2 + 2 * it) * WS_HS + own_a, pack_rows(u.a));
            publish(rs, WS_H + (2 + 2 * it) * WS_HS + own_b, pack_rows(u.b));
            t_s = __builtin_amdgcn_s_memrealtime();
            STAMP(it ? 13 : 9);

            wait_until(t_s + POLL_DELAY);
            pc = poll2_issue(rs, WS_H + (2 + 2 * it) * WS_HS + col_a, WS_H + (2 + 2 * it) * WS_HS + col_b);
            if (it == 0) {
                cnt_wait(smem, 2, 4);
                s4_taps(smem, 1, r, tix0, tix1);
                cnt_arrive(smem, 3, lane);
            }
            stage_input(2 + 2 * it);
            BAR();
            STAMP(it ? 14 : 10);
            conv_row(smem, wave, lane, rs, id, it ? -1 : 4, 28, [] {});
            send_result(3 + 2 * it);
            STAMP(it ? 15 : 11);

            wait_until(t_s + POLL_DELAY);
            fetch_rows(3 + 2 * it);
            STAMP(it ? 16 : 12);
            Rows y;
            unpack_rows(pr.a, y.a);
            unpack_rows(pr.b, y.b);
#pragma unroll
            for (int i = 0; i < 4; ++i) { x.a[i] += y.a[i]; x.b[i] += y.b[i]; }
            pr.a = round_rows(x.a);
            pr.b = round_rows(x.b);
        }
        {
            u16* dst = out + (8 * id) * DM + r;
#pragma unroll
            for (int j = 0; j < 4; ++j) {
                dst[(2 * j) * DM] = (u16)pr.a[j];
                dst[(2 * j) * DM + 256] = (u16)pr.b[j];
                dst[(2 * j + 1) * DM] = (u16)(pr.a[j] >> 16);
                dst[(2 * j + 1) * DM + 256] = (u16)(pr.b[j] >> 16);
            }
        }
        STAMP(17);
    }
    asm volatile("h3s4_text_b:");
}

extern "C" void kernel_launch(void* const* d_in, const int* in_sizes, int n_in,
                              void* d_out, int out_size, void* d_ws, size_t ws_size,
                              hipStream_t stream) {
    h3s4_kernel<<<256, 512, 0, stream>>>(
        (const char*)d_in[0], (const char*)d_in[1], (const char*)d_in[2], (const u32x2*)d_in[3], (const u32x4*)d_in[4],
        (const u32*)d_in[5], (const u16*)d_in[7], (const u16*)d_in[8], (const u16*)d_in[9], (const u16*)d_in[10],
        (const u16*)d_in[11], (const u16*)d_in[12], (const u16*)d_in[13], (const u32*)d_in[14], (u16*)d_out, (char*)d_ws
        );
}
```

```cpp
#include <hip/hip_runtime.h>
#include <hip/hip_bf16.h>
#include <stdint.h>

typedef unsigned int u32;
typedef unsigned short u16;
typedef unsigned long long u64;
typedef __attribute__((ext_vector_type(4))) float f32x4;
typedef __attribute__((ext_vector_type(2))) float f32x2;
typedef __attribute__((ext_vector_type(4))) u32 u32x4;
typedef __attribute__((ext_vector_type(2))) u32 u32x2;
typedef __attribute__((ext_vector_type(4))) short s16x4;
typedef __attribute__((ext_vector_type(8))) __bf16 bf16x8;
typedef __attribute__((ext_vector_type(2))) __bf16 bf16x2;
#define LAS __attribute__((address_space(3)))
#define GAS __attribute__((address_space(1)))
#define RLX_AGENT __ATOMIC_RELAXED, __HIP_MEMORY_SCOPE_AGENT
#define SB() __builtin_amdgcn_sched_barrier(0)
#define BAR() asm volatile("s_waitcnt lgkmcnt(0)\n\ts_barrier" ::: "memory")
#define MEMFENCE() asm volatile("" ::: "memory")
#define WAIT_VM(n) do { MEMFENCE(); __builtin_amdgcn_s_waitcnt(0x0F70 | ((n) & 15) | (((n) >> 4) << 14)); MEMFENCE(); } while (0)

constexpr int DM = 512;
constexpr int NS = 128;
constexpr u32 SENT = 0xffffffffu;
constexpr u32 SPIN_CAP = 20000;
#define FILL_GAP 70
#define R_STEPS 1
#define POLL_DELAY 45

constexpr u32 WS_EPOCH = 0;
constexpr u32 WS_FLAGS = 4096;
constexpr u32 WS_H = 1u << 20;
constexpr u32 WS_HS = 2u << 20;
constexpr u32 WS_USED = WS_H + 6 * WS_HS;
__device__ __forceinline__ u32 hbuf(u32 h) { return WS_H + (h >= 4 ? h - 4 : h) * WS_HS; }

constexpr int T_COPY = 4160, T_CH = 8 * T_COPY;
constexpr int U_STRIDE = 4672;
constexpr int R_ROW = 1088;
constexpr int KP_WAVE = 2816;
constexpr int OFF_T = 0;
constexpr int OFF_U = OFF_T + 2 * T_CH;
constexpr int OFF_X = OFF_U + 2 * U_STRIDE;
constexpr int OFF_Y = OFF_X + 16384;
constexpr int OFF_R = OFF_Y + 8192;
constexpr int OFF_P = OFF_R + 28 * R_ROW;
constexpr int OFF_KP = OFF_P + 4096;
constexpr int OFF_KC = OFF_KP + 8 * KP_WAVE;
constexpr int OFF_RED = OFF_KC + 2 * 648 * 2;
constexpr int OFF_CNT = OFF_RED + 1024;
constexpr int OFF_ST = OFF_CNT + 64;
constexpr int LDS_BYTES = OFF_ST + 512;
static_assert(LDS_BYTES <= 163840, "LDS");

#define STAMP(i)

__device__ __forceinline__ float bf_lo(u32 p) { return __builtin_bit_cast(float, p << 16); }
__device__ __forceinline__ float bf_hi(u32 p) { return __builtin_bit_cast(float, p & 0xffff0000u); }
__device__ __forceinline__ float bf_u16(u16 p) { return __builtin_bit_cast(float, (u32)p << 16); }
__device__ __forceinline__ u32 pk_bf(float lo, float hi) {
    bf16x2 v = {(__bf16)lo, (__bf16)hi};
    return __builtin_bit_cast(u32, v);
}
__device__ __forceinline__ float as_f(u32 x) { return __builtin_bit_cast(float, x); }
__device__ __forceinline__ u32 as_u(float x) { return __builtin_bit_cast(u32, x); }
__device__ __forceinline__ float rbf(float x) { return bf_lo(pk_bf(x, x)); }
__device__ __forceinline__ void unpack8(u32x4 p, float (&f)[8]) {
#pragma unroll
    for (int i = 0; i < 4; ++i) { f[2 * i] = bf_lo(p[i]); f[2 * i + 1] = bf_hi(p[i]); }
}
__device__ __forceinline__ u32x4 pack8(const float (&f)[8]) {
    return (u32x4){pk_bf(f[0], f[1]), pk_bf(f[2], f[3]), pk_bf(f[4], f[5]), pk_bf(f[6], f[7])};
}
__device__ __forceinline__ u32x4 reverse8(u32x4 p) {
    return (u32x4){__builtin_amdgcn_alignbit(p[3], p[3], 16), __builtin_amdgcn_alignbit(p[2], p[2], 16),
                   __builtin_amdgcn_alignbit(p[1], p[1], 16), __builtin_amdgcn_alignbit(p[0], p[0], 16)};
}
template <int CTRL> __device__ __forceinline__ float dppf(float v) {
    return __builtin_bit_cast(float, __builtin_amdgcn_update_dpp(0, __builtin_bit_cast(int, v), CTRL, 0xf, 0xf, true));
}

__device__ __forceinline__ void lds_dma16(const char* src, u32 lds) {
    u32 keep;
    asm volatile("s_mov_b32 %0, m0\n\ts_mov_b32 m0, %2\n\ts_nop 0\n\tglobal_load_lds_dwordx4 %1, off\n\ts_mov_b32 m0, %0"
                 : "=&s"(keep) : "v"(src), "s"(lds) : "memory");
}

__device__ __forceinline__ u32 ld_u16(const void* base, u32 off) {
    u32 r;
    asm volatile("global_load_ushort %0, %1, %2" : "=v"(r) : "v"(off), "s"(base) : "memory");
    return r;
}
__device__ __forceinline__ u32x2 ld_b64(const void* base, u32 off) {
    u32x2 r;
    asm volatile("global_load_dwordx2 %0, %1, %2" : "=v"(r) : "v"(off), "s"(base) : "memory");
    return r;
}
__device__ __forceinline__ u32x4 ld_b128(const void* base, u32 off) {
    u32x4 r;
    asm volatile("global_load_dwordx4 %0, %1, %2" : "=v"(r) : "v"(off), "s"(base) : "memory");
    return r;
}

typedef __amdgpu_buffer_rsrc_t rsrc_t;
__device__ __forceinline__ u32x4 ld_dev(rsrc_t rs, u32 off) { return __builtin_amdgcn_raw_buffer_load_b128(rs, off, 0, 16); }
__device__ __forceinline__ void st_dev(rsrc_t rs, u32 off, u32x4 v) { __builtin_amdgcn_raw_buffer_store_b128(v, rs, off, 0, 16); }
__device__ __forceinline__ void publish(rsrc_t rs, u32 off, u32x4 v) {
#pragma unroll
    for (int i = 0; i < 4; ++i) v[i] = v[i] < SENT - 1u ? v[i] : SENT - 1u;
    st_dev(rs, off, v);
}
__device__ __forceinline__ u32 launch_tag(const char* ws) {
    u32 e;
    asm volatile("s_load_dword %0, %1, 0x0\n\ts_waitcnt lgkmcnt(0)" : "=&s"(e) : "s"(ws + WS_EPOCH) : "memory");
    return e + 1u;
}
__device__ __forceinline__ void wait_until(u64 t) {
    while (__builtin_amdgcn_s_memrealtime() < t) __builtin_amdgcn_s_sleep(1);
}
__device__ __forceinline__ u32x4 poll_issue(rsrc_t rs, u32 off) {
    MEMFENCE();
    u32x4 x = ld_dev(rs, off);
    MEMFENCE();
    return x;
}
struct Piece2 { u32x4 a, b; };
__device__ __forceinline__ Piece2 poll2_issue(rsrc_t rs, u32 off_a, u32 off_b) {
    MEMFENCE();
    Piece2 p = {ld_dev(rs, off_a), ld_dev(rs, off_b)};
    MEMFENCE();
    return p;
}
__device__ __forceinline__ bool has_sentinel(const u32x4& x) {
    u32 mx = x[0] > x[1] ? x[0] : x[1];
    mx = mx > x[2] ? mx : x[2];
    mx = mx > x[3] ? mx : x[3];
    return mx == SENT;
}
__device__ __forceinline__ void poll2_finish(Piece2& p, u32x4 desc, u32 off_a, u32 off_b) {
#pragma nounroll
    for (u32 spins = 0; spins < SPIN_CAP; ++spins) {
        const bool bad = has_sentinel(p.a) || has_sentinel(p.b);
        if (__builtin_amdgcn_ballot_w64(bad) == 0ull) break;
        __builtin_amdgcn_s_sleep(1);
        if (bad) asm volatile("buffer_load_dwordx4 %0, %2, %4, 0 offen sc1\n\tbuffer_load_dwordx4 %1, %3, %4, 0 offen sc1\n\ts_waitcnt vmcnt(0)"
                              : "+v"(p.a), "+v"(p.b) : "v"(off_a), "v"(off_b), "s"(desc) : "memory");
    }
}
__device__ __forceinline__ void flags_finish(u32x4& fl, rsrc_t rs, u32 off, u32 epoch) {
    u32 spins = 0;
    while (__builtin_amdgcn_ballot_w64(fl[0] != epoch || fl[1] != epoch || fl[2] != epoch || fl[3] != epoch) != 0ull) {
        if (++spins > SPIN_CAP) break;
        __builtin_amdgcn_s_sleep(2);
        fl = poll_issue(rs, off);
    }
}

__device__ __forceinline__ void cnt_arrive(char* smem, int k, int lane) {
    asm volatile("s_waitcnt lgkmcnt(0)" ::: "memory");
    if (lane == 0) __hip_atomic_fetch_add((LAS u32*)(smem + OFF_CNT) + k, 1u, __ATOMIC_RELAXED, __HIP_MEMORY_SCOPE_WORKGROUP);
}
__device__ __forceinline__ void cnt_wait(char* smem, int k, u32 target) {
    u32 spins = 0;
    while (__hip_atomic_load((LAS u32*)(smem + OFF_CNT) + k, __ATOMIC_RELAXED, __HIP_MEMORY_SCOPE_WORKGROUP) < target) {
        if (++spins > 50 * SPIN_CAP) break;
        __builtin_amdgcn_s_sleep(1);
    }
    MEMFENCE();
}

struct Rows { f32x2 a[4], b[4]; };
__device__ __forceinline__ void unpack_rows(u32x4 p, f32x2 (&f)[4]) {
#pragma unroll
    for (int i = 0; i < 4; ++i) f[i] = (f32x2){bf_lo(p[i]), bf_hi(p[i])};
}
__device__ __forceinline__ u32x4 pack_rows(const f32x2 (&f)[4]) {
    return (u32x4){pk_bf(f[0][0], f[0][1]), pk_bf(f[1][0], f[1][1]), pk_bf(f[2][0], f[2][1]), pk_bf(f[3][0], f[3][1])};
}
__device__ __forceinline__ u32x4 round_rows(f32x2 (&f)[4]) {
    u32x4 p = pack_rows(f);
    asm("" : "+v"(p));
    unpack_rows(p, f);
    return p;
}
__device__ __forceinline__ void layer_norm(const Rows& x, Rows& u, float* red, int lane, int w) {
#pragma clang fp contract(fast)
    u32 a[8], b[8];
#pragma unroll
    for (int i = 0; i < 4; ++i) {
        const f32x2 s1 = x.a[i] + x.b[i], s2 = x.a[i] * x.a[i] + x.b[i] * x.b[i];
        a[2 * i] = as_u(s1[0]); a[2 * i + 1] = as_u(s1[1]);
        b[2 * i] = as_u(s2[0]); b[2 * i + 1] = as_u(s2[1]);
    }
    SB();
#pragma unroll
    for (int j = 0; j < 8; ++j) {
        const auto s = __builtin_amdgcn_permlane32_swap(a[j], b[j], false, false);
        a[j] = s[0]; b[j] = s[1];
    }
    SB();
    float r[8];
#pragma unroll
    for (int j = 0; j < 8; ++j) r[j] = as_f(a[j]) + as_f(b[j]);
    SB();
#pragma unroll
    for (int j = 0; j < 4; ++j) {
        const auto s = __builtin_amdgcn_permlane16_swap(as_u(r[j]), as_u(r[j + 4]), false, false);
        a[j] = s[0]; b[j] = s[1];
    }
    SB();
    float s4[4];
#pragma unroll
    for (int j = 0; j < 4; ++j) s4[j] = as_f(a[j]) + as_f(b[j]);
    SB();
#pragma unroll
    for (int j = 0; j < 4; ++j) s4[j] += dppf<0x128>(s4[j]);
    const float t0 = (lane & 8) ? s4[2] : s4[0], t1 = (lane & 8) ? s4[3] : s4[1];
    const float c0 = t0 + dppf<0x141>(t0), c1 = t1 + dppf<0x141>(t1);
    float s = (lane & 4) ? c1 : c0;
    s += dppf<0xB1>(s);
    s += dppf<0x4E>(s);
    const u32 sb = as_u(s) < SENT - 1u ? as_u(s) : SENT - 1u;
    if ((lane & 3) == 0) ((volatile LAS u32*)(LAS char*)(char*)red)[(lane >> 2) * 4 + w] = sb;
    u32x4 p;
    u32 spins = 0;
    while (true) {
        p = *(volatile LAS u32x4*)((LAS char*)(char*)red + (lane & 15) * 16);
        u32 mx = p[0] > p[1] ? p[0] : p[1];
        mx = mx > p[2] ? mx : p[2];
        mx = mx > p[3] ? mx : p[3];
        if (__builtin_amdgcn_ballot_w64(mx == SENT) == 0ull) break;
        if (++spins > 50 * SPIN_CAP) break;
    }
    const float m = ((as_f(p[0]) + as_f(p[1])) + (as_f(p[2]) + as_f(p[3]))) * (1.0f / DM);
    const float ex2 = dppf<0x128>(m);
    const float rstd = __builtin_amdgcn_rsqf(fmaxf(fmaf(-m, m, ex2), 0.f) + 1e-5f);
    const float off = -m * rstd;
    const f32x2 R[4] = {{dppf<0x150>(rstd), dppf<0x151>(rstd)}, {dppf<0x152>(rstd), dppf<0x153>(rstd)},
                        {dppf<0x154>(rstd), dppf<0x155>(rstd)}, {dppf<0x156>(rstd), dppf<0x157>(rstd)}};
    const f32x2 O[4] = {{dppf<0x150>(off), dppf<0x151>(off)}, {dppf<0x152>(off), dppf<0x153>(off)},
                        {dppf<0x154>(off), dppf<0x155>(off)}, {dppf<0x156>(off), dppf<0x157>(off)}};
#pragma unroll
    for (int i = 0; i < 4; ++i) { u.a[i] = x.a[i] * R[i] + O[i]; u.b[i] = x.b[i] * R[i] + O[i]; }
}

__device__ __forceinline__ void stage_taps(char* smem, int ch, int i, u32x4 prev, u32x4 own) {
    const u32 D[8] = {prev[0], prev[1], prev[2], prev[3], own[0], own[1], own[2], own[3]};
    u32 O[7];
#pragma unroll
    for (int y = 0; y < 7; ++y) O[y] = __builtin_amdgcn_alignbit(D[y + 1], D[y], 16);
    char* base = smem + OFF_T + ch * T_CH + (i + 1) * 16;
#pragma unroll
    for (int c = 0; c < 8; ++c) {
        const int y = (c + 1) >> 1;
        const u32x4 v = (c & 1) ? (u32x4){D[y], D[y + 1], D[y + 2], D[y + 3]} : (u32x4){O[y], O[y + 1], O[y + 2], O[y + 3]};
        *(u32x4*)(base + c * T_COPY) = v;
    }
}

__device__ __forceinline__ f32x4 mfma16(u32x4 a, u32x4 b, f32x4 c) {
    return __builtin_amdgcn_mfma_f32_16x16x32_bf16(__builtin_bit_cast(bf16x8, a), __builtin_bit_cast(bf16x8, b), c, 0, 0, 0);
}
struct KState { f32x4 acc[3]; };
__device__ __forceinline__ void kgen_init(KState& ks) {
#pragma unroll
    for (int e = 0; e < 3; ++e) ks.acc[e] = (f32x4){0, 0, 0, 0};
}
#define OPQ(x) asm volatile("" : "+v"(x))
template <int NS_, bool PK>
__device__ __forceinline__ void kgen_steps(const f32x4* P, int lane, KState& ks) {
#pragma clang fp contract(fast)
    const int g = lane >> 4, m = lane & 15;
    const float lo = (float)(8 * m);
    const float hi[3] = {(float)(128 * (m >> 3) + (m & 7)),
                         (float)(m < 8 ? 256 + 128 * (m >> 2) + 2 * (m & 3) : 512 + 128 * ((m - 8) >> 1) + 4 * (m & 1)),
                         (float)(m < 8 ? 1024 + 128 * m : 2048)};
    auto eval = [&](f32x2 ex, f32x2 an, bool conj, u32& o0, u32& o1) {
        if (!PK) { OPQ(ex[0]); OPQ(ex[1]); OPQ(an[0]); OPQ(an[1]); }
        const float m0 = __builtin_amdgcn_exp2f(ex[0]), m1 = __builtin_amdgcn_exp2f(ex[1]);
        const float f0 = __builtin_amdgcn_fractf(an[0]), f1 = __builtin_amdgcn_fractf(an[1]);
        const float c0 = __builtin_amdgcn_cosf(f0), c1 = __builtin_amdgcn_cosf(f1);
        const float s0 = __builtin_amdgcn_sinf(f0), s1 = __builtin_amdgcn_sinf(f1);
        float r0, r1, i0, i1;
        if (PK) {
            const f32x2 r = (f32x2){c0, c1} * (f32x2){m0, m1}, i = (f32x2){s0, s1} * (f32x2){m0, m1};
            r0 = r[0]; r1 = r[1]; i0 = i[0]; i1 = i[1];
        } else {
            r0 = c0 * m0; OPQ(r0); i0 = s0 * m0; OPQ(i0); r1 = c1 * m1; OPQ(r1); i1 = s1 * m1; OPQ(i1);
        }
        o0 = pk_bf(r0, conj ? -i0 : i0);
        o1 = pk_bf(r1, conj ? -i1 : i1);
    };
    auto affine = [&](f32x2 a, float p, f32x2 b) -> f32x2 {
        if (PK) return a * p + b;
        float x = fmaf(a[0], p, b[0]), y = fmaf(a[1], p, b[1]);
        OPQ(x); OPQ(y);
        return (f32x2){x, y};
    };
#pragma unroll
    for (int s = 0; s < NS_; ++s) {
        u32x4 B, A[3];
#pragma unroll
        for (int h = 0; h < 2; ++h) {
            const f32x4 p0 = P[16 * s + 4 * g + 2 * h], p1 = P[16 * s + 4 * g + 2 * h + 1];
            u32 o0, o1;
            eval(affine(p0.xy, lo, (f32x2){0.f, 0.f}), affine(p0.zw, lo, (f32x2){0.f, 0.f}), true, o0, o1);
            B[2 * h] = o0; B[2 * h + 1] = o1;
#pragma unroll
            for (int e = 0; e < 3; ++e) {
                eval(affine(p0.xy, hi[e], p1.xy), affine(p0.zw, hi[e], p1.zw), false, o0, o1);
                A[e][2 * h] = o0; A[e][2 * h + 1] = o1;
            }
        }
#pragma unroll
        for (int e = 0; e < 3; ++e) ks.acc[e] = mfma16(A[e], B, ks.acc[e]);
    }
}
__device__ __forceinline__ const f32x4* kgen_params(char* smem, int wave, int step) {
    return (const f32x4*)(smem + OFF_P) + ((wave >> 1) & 1) * 128 + ((wave & 1) * 4 + step) * 16;
}
__device__ __forceinline__ void kgen_save(char* smem, int wave, int lane, const KState& ks) {
    char* p = smem + OFF_KP + wave * KP_WAVE + lane * 16;
    *(f32x4*)p = ks.acc[0];
    *(f32x4*)(p + 1024) = ks.acc[1];
    if (lane < 48) *(f32x4*)(p + 2048) = ks.acc[2];
}
__device__ __forceinline__ f32x4 kgen_sum(const char* p) {
    return (*(const f32x4*)p + *(const f32x4*)(p + KP_WAVE)) + (*(const f32x4*)(p + 4 * KP_WAVE) + *(const f32x4*)(p + 5 * KP_WAVE));
}
__device__ __forceinline__ void kgen_reduce(char* smem, int wave, int lane, u32 dskip) {
    const int c = wave >> 1, j = lane & 15, g = lane >> 4;
    char* KC = smem + OFF_KC + c * 1296;
    const char* p = smem + OFF_KP + 2 * c * KP_WAVE + lane * 16;
    if ((wave & 1) == 0) {
        f32x4 s = kgen_sum(p);
        const f32x4 t = kgen_sum(smem + OFF_KP + 2 * c * KP_WAVE + 2048 + (lane < 48 ? lane : 0) * 16);
        if (lane == 0) s[0] += c ? bf_hi(dskip) : bf_lo(dskip);
        *(u32x2*)(KC + 2 * (128 * (g >> 1) + 4 * (g & 1) + 8 * j)) = (u32x2){pk_bf(s[0], s[1]), pk_bf(s[2], s[3])};
        const u32 lo = pk_bf(t[0], t[1]), hi = pk_bf(t[2], t[3]);
        u16* o = (u16*)KC + 512 + 64 * g + j;
        if (g < 2) { o[0] = (u16)lo; o[16] = (u16)(lo >> 16); o[32] = (u16)hi; o[48] = (u16)(hi >> 16); }
        else if (lane == 32) { ((u16*)KC)[640] = (u16)lo; ((u16*)KC)[641] = 0; }
    } else {
        const f32x4 s = kgen_sum(p + 1024);
        const u32 lo = pk_bf(s[0], s[1]), hi = pk_bf(s[2], s[3]);
        if (g < 2) *(u32x2*)(KC + 2 * (256 + 64 * g + 4 * j)) = (u32x2){lo, hi};
        else {
            u32* o = (u32*)(KC + 2 * (384 + 64 * (g - 2) + 2 * j));
            o[0] = lo; o[16] = hi;
        }
    }
}

struct TapIndex {
    u16 off[2048 + 8];
    constexpr TapIndex() : off() {
        for (int i = 0; i < 8; ++i) off[i] = 641 * 2;
        for (int l = 0; l < 2048; ++l) {
            int e = 0;
            while ((l >> e) >= 256) ++e;
            int v = l >> e;
            if (e > 0) {
                const int rem = l & ((1 << e) - 1), half = 1 << (e - 1);
                if (rem > half || (rem == half && (v & 1))) ++v;
            }
            off[8 + l] = (u16)(2 * (128 * e + v));
        }
    }
};
__device__ const TapIndex tap_index = TapIndex();

__device__ __forceinline__ void s4_taps(char* smem, int ch, int i, u32x4 tix0, u32x4 tix1) {
    const char* kc = smem + OFF_KC + ch * 1296;
    u32 d[8];
#pragma unroll
    for (int j = 0; j < 8; ++j) {
        const u32 o = j < 4 ? tix0[j] : tix1[j - 4];
        d[j] = (u32)*(const u16*)(kc + (o & 0xffffu)) | ((u32)*(const u16*)(kc + (o >> 16)) << 16);
    }
    stage_taps(smem, ch, i, (u32x4){d[0], d[1], d[2], d[3]}, (u32x4){d[4], d[5], d[6], d[7]});
}

struct Frags { u32x4 A[8], B[8]; };
template <bool INIT> __device__ __forceinline__ void mma(f32x4& acc, const u32x4& a, const u32x4& b) {
    if constexpr (INIT) asm volatile("v_mfma_f32_16x16x32_bf16 %0, %1, %2, 0" : "=&v"(acc) : "v"(a), "v"(b));
    else asm volatile("v_mfma_f32_16x16x32_bf16 %0, %1, %2, %0" : "+v"(acc) : "v"(a), "v"(b));
}
template <int I> __device__ __forceinline__ void load_one(Frags& f, const char* ap, const char* bp) {
    if constexpr (I == 0) f.A[0] = *(const u32x4*)ap;
    else if constexpr (I <= 8) f.B[I - 1] = *(const u32x4*)(bp + 512 * (8 - I));
    else f.A[I - 8] = *(const u32x4*)(ap + 512 * (I - 8));
}
template <int I> __device__ __forceinline__ void load_all(Frags& f, const char* ap, const char* bp) {
    if constexpr (I < 16) { load_one<I>(f, ap, bp); SB(); load_all<I + 1>(f, ap, bp); }
}
template <int T0, int T1, int T2, int T3> struct TileSet {
    static constexpr int tile(int i) { return i == 0 ? T0 : i == 1 ? T1 : i == 2 ? T2 : T3; }
    static constexpr int walk(int I, bool want_tile) {
        int n = 0;
        for (int a = 0; a < 8; ++a)
            for (int j = 0; j < 4; ++j)
                if (tile(j) >= a) { if (n == I) return want_tile ? tile(j) : a; ++n; }
        return -1;
    }
};
constexpr int tri_a(int I) { int a = 0; while (I >= 8 - a) { I -= 8 - a; ++a; } return a; }
constexpr int tri_b(int I) { int a = 0; while (I >= 8 - a) { I -= 8 - a; ++a; } return I; }
template <int I, bool FIRST> __device__ __forceinline__ void class_step(const Frags& cur, Frags& nxt, f32x4 (&acc)[8], const char* ap, const char* bp) {
    if constexpr (I < 36) {
        constexpr int a = tri_a(I), b = tri_b(I);
        mma<FIRST && a == 0>(acc[a + b], cur.A[a], cur.B[b]);
        if constexpr ((I & 1) == 1 && (I >> 1) < 16) load_one<(I >> 1)>(nxt, ap, bp);
        SB();
        class_step<I + 1, FIRST>(cur, nxt, acc, ap, bp);
    }
}
template <class TS, int I, int TO, int PRE> __device__ __forceinline__ void set_step(const Frags& cur, Frags& nxt, f32x4 (&acc)[8], const char* ap, const char* bp) {
    if constexpr (I < TO) {
        constexpr int t = TS::walk(I, true), a = TS::walk(I, false);
        mma<false>(acc[t], cur.A[a], cur.B[t - a]);
        if constexpr (PRE == 1 && (I * 11) / 18 != ((I + 1) * 11) / 18) load_one<(I * 11) / 18>(nxt, ap, bp);
        if constexpr (PRE == 2 && I < 5) load_one<11 + I>(nxt, ap, bp);
        SB();
        set_step<TS, I + 1, TO, PRE>(cur, nxt, acc, ap, bp);
    }
}
template <class TS, int J> __device__ __forceinline__ void tile_step(const Frags& cur, f32x4 (&acc)[8]) {
    constexpr int t = TS::tile(J);
#pragma unroll
    for (int a = 0; a <= t; ++a) { mma<false>(acc[t], cur.A[a], cur.B[t - a]); SB(); }
}
template <class TS, int J> __device__ __forceinline__ void tile_out(char* Y, f32x4 (&acc)[8], const f32x4 (&o)[4]) {
    constexpr int t = TS::tile(J);
    const f32x4 s = acc[t] + o[J];
    const u32x2 pk = {pk_bf(s[0], s[1]), pk_bf(s[2], s[3])};
    *(u32x2*)(Y + 512 * t) = pk;
    SB();
}
template <class SEND, class KEEP>
__device__ __forceinline__ void conv_half(char* smem, const u32x4 (&A0)[8], const char* ap, const char* bp, int wave, int lane, int ch, u32 seq) {
    f32x4 acc[8];
    Frags f0, f1;
#pragma unroll
    for (int a = 0; a < 8; ++a) f0.A[a] = A0[a];
#pragma unroll
    for (int b = 0; b < 8; ++b) { f0.B[b] = *(const u32x4*)(bp + 512 * (7 - b)); SB(); }
    class_step<0, true>(f0, f1, acc, ap + 64, bp + 64);
    class_step<0, false>(f1, f0, acc, ap + 128, bp + 128);
    set_step<SEND, 0, 18, 1>(f0, f1, acc, ap + 192, bp + 192);
    set_step<SEND, 0, 18, 2>(f1, f1, acc, ap + 192, bp + 192);
    set_step<KEEP, 0, 4, 0>(f0, f1, acc, ap, bp);
    asm volatile("s_nop 3" : "+v"(acc[SEND::tile(0)]), "+v"(acc[SEND::tile(1)]), "+v"(acc[SEND::tile(2)]), "+v"(acc[SEND::tile(3)]));
    f32x4* X = (f32x4*)(smem + OFF_X);
#pragma unroll
    for (int i = 0; i < 4; ++i) X[(wave * 4 + i) * 64 + lane] = acc[SEND::tile(i)];
    volatile LAS u32* xf = (volatile LAS u32*)(LAS char*)(smem + OFF_CNT + 32);
    xf[wave] = seq;
    SB();
    set_step<KEEP, 4, 14, 0>(f0, f1, acc, ap, bp);
    u32 seen = xf[wave ^ 1];
    SB();
    set_step<KEEP, 14, 18, 0>(f0, f1, acc, ap, bp);
    for (u32 spins = 0; seen < seq && spins < 50 * SPIN_CAP; ++spins) seen = xf[wave ^ 1];
    SB();
    f32x4 o[4];
#pragma unroll
    for (int i = 0; i < 4; ++i) o[i] = X[((wave ^ 1) * 4 + i) * 64 + lane];
    SB();
    char* Y = smem + OFF_Y + ch * 4096 + (16 * (lane & 15) + 4 * (lane >> 4)) * 2;
    tile_step<KEEP, 0>(f1, acc);
    tile_step<KEEP, 1>(f1, acc);
    tile_step<KEEP, 2>(f1, acc);
    asm volatile("s_nop 3" : "+v"(acc[KEEP::tile(0)]), "+v"(acc[KEEP::tile(1)]));
    tile_out<KEEP, 0>(Y, acc, o);
    tile_out<KEEP, 1>(Y, acc, o);
    tile_step<KEEP, 3>(f1, acc);
    asm volatile("" : "+v"(acc[KEEP::tile(2)]));
    tile_out<KEEP, 2>(Y, acc, o);
    asm volatile("s_nop 7" : "+v"(acc[KEEP::tile(3)]));
    tile_out<KEEP, 3>(Y, acc, o);
}
struct ConvAddr { const char* ap; const char* bp; };
__device__ __forceinline__ ConvAddr conv_addr(char* smem, int wave, int lane) {
    const int ch = (wave >> 1) & 1, hf = wave & 1;
    const int m = lane & 15, g = lane >> 4;
    ConvAddr c;
    c.ap = smem + OFF_T + ch * T_CH + (m & 7) * T_COPY + 16 * (m >> 3) + 16 * g + 256 * hf;
    c.bp = smem + OFF_U + ch * U_STRIDE + 4064 - 32 * m + 16 * g + 256 * hf - 512 * 7;
    return c;
}
__device__ __forceinline__ void conv_preload(u32x4 (&A0)[8], const ConvAddr& c) {
#pragma unroll
    for (int a = 0; a < 8; ++a) A0[a] = *(const u32x4*)(c.ap + 512 * a);
}
template <bool EARLY>
__device__ __forceinline__ void conv_col(char* smem, const ConvAddr& c, int wave, int lane, u32 seq) {
    u32x4 A0[8];
    if (EARLY) conv_preload(A0, c);
    BAR();
    if (!EARLY) conv_preload(A0, c);
    if ((wave & 1) == 0) conv_half<TileSet<1, 3, 4, 6>, TileSet<0, 2, 5, 7>>(smem, A0, c.ap, c.bp, wave, lane, wave >> 1, seq);
    else conv_half<TileSet<0, 2, 5, 7>, TileSet<1, 3, 4, 6>>(smem, A0, c.ap, c.bp, wave, lane, wave >> 1, seq);
    STAMP(26 + seq);
    BAR();
}
template <class PRE>
__device__ __forceinline__ void conv_row(char* smem, int wave, int lane, rsrc_t rs, int id, int fill0, int st, PRE pre) {
    const u32x4 sent = {SENT, SENT, SENT, SENT};
    const u32 off = hbuf(fill0 < 0 ? 0 : fill0) + id * 8192 + ((wave - 4) * 64 + lane) * 16;
    const u64 t0 = __builtin_amdgcn_s_memrealtime();
    if (fill0 >= 0) { st_dev(rs, off, sent); st_dev(rs, off + 4096, sent); }
    pre();
    if (fill0 >= 0) {
        wait_until(t0 + FILL_GAP);
        st_dev(rs, off + WS_HS, sent); st_dev(rs, off + WS_HS + 4096, sent);
        WAIT_VM(2);
    }
    STAMP(st);
    BAR();
}

__global__ void __launch_bounds__(512, 2)
h3s4_kernel(const char* __restrict__ q, const char* __restrict__ k, const char* __restrict__ v,
            const u32x2* __restrict__ shiftk, const u32x4* __restrict__ ssmk, const u32* __restrict__ ssmD,
            const u16* __restrict__ Are, const u16* __restrict__ Aim, const u16* __restrict__ Bre, const u16* __restrict__ Bim,
            const u16* __restrict__ Cre, const u16* __restrict__ Cim, const u16* __restrict__ logdt, const u32* __restrict__ Dskip,
            u16* __restrict__ out, char* ws
            ) {
    __shared__ __attribute__((aligned(16))) char smem[LDS_BYTES];
    const int tid = threadIdx.x, lane = tid & 63, wave = __builtin_amdgcn_readfirstlane(tid >> 6);
    const int id = (blockIdx.x & 7) * 32 + (blockIdx.x >> 3);
    const rsrc_t rs = __builtin_amdgcn_make_buffer_rsrc(ws, 0, WS_USED, 0x00020000);
    const u32x4 desc = {(u32)(size_t)ws, (u32)((size_t)ws >> 32) & 0xffffu, WS_USED, 0x00020000u};
    STAMP(0);

    u32 code_pf = 0;
    {
        u64 pc; u32 len;
        asm volatile("s_getpc_b64 %0\nh3s4_text_a:\n\ts_mov_b32 %1, h3s4_text_b - h3s4_text_a" : "=s"(pc), "=s"(len));
        const u32 off = ((blockIdx.x >> 3) * 8 + wave) * 256 + lane * 4;
        if (off + 4 <= len) asm volatile("global_load_dword %0, %1, %2" : "=v"(code_pf) : "v"(off), "s"(pc) : "memory");
    }
    const u32x4 sent = {SENT, SENT, SENT, SENT};
    const u32 dA = ssmD[id], dB = Dskip[id];
    MEMFENCE(); SB();
    st_dev(rs, WS_H + 0 * WS_HS + id * 8192 + tid * 16, sent);
    MEMFENCE(); SB();
#pragma unroll
    for (int s = 0; s < 4; ++s) {
        const int slot = wave + 8 * s;
        if (slot < 28) {
            int row = 8 * id + (slot < 12 ? slot - 4 : slot < 20 ? slot - 12 : slot - 20);
            row = row < 0 ? 0 : row;
            const char* src = slot < 12 ? k : slot < 20 ? v : q;
            lds_dma16(src + row * 1024 + lane * 16, (u32)(size_t)(LAS char*)(smem + OFF_R) + slot * R_ROW);
        }
    }

    if (wave < 4) {
        u32x4 tap_own = ld_b128(ssmk, (2 * id) * 4096 + tid * 16), tap_prev = ld_b128(ssmk, (2 * id) * 4096 + (tid ? tid - 1 : 0) * 16);
        const int pd = 2 * id + (tid >> 7), po = (pd * NS + (tid & 127)) * 2;
        u32 r_dt = ld_u16(logdt, pd * 2), r_are = ld_u16(Are, po), r_aim = ld_u16(Aim, po), r_bre = ld_u16(Bre, po);
        u32 r_bim = ld_u16(Bim, po), r_cre = ld_u16(Cre, po), r_cim = ld_u16(Cim, po);
        MEMFENCE(); SB();
        st_dev(rs, WS_H + 1 * WS_HS + id * 8192 + tid * 16, sent);
        MEMFENCE(); SB();
        const u32 epoch = launch_tag(ws);
        asm volatile("s_waitcnt vmcnt(13)\n\ts_barrier" ::: "memory");
        if (tid < 32) __hip_atomic_store((GAS u32*)(ws + WS_FLAGS) + tid * 256 + id, epoch, RLX_AGENT);
        STAMP(19);
        if (tid < 60) *(u32x4*)(smem + OFF_U + (tid / 30) * U_STRIDE + 4096 + (tid % 30) * 16) = (u32x4){0, 0, 0, 0};
        else if (tid >= 64 && tid < 80) *(u32x4*)(smem + OFF_T + (tid - 64) * T_COPY) = (u32x4){0, 0, 0, 0};
        asm volatile("s_waitcnt vmcnt(1)" : "+v"(tap_own), "+v"(tap_prev), "+v"(r_dt), "+v"(r_are), "+v"(r_aim), "+v"(r_bre), "+v"(r_bim), "+v"(r_cre), "+v"(r_cim) :: "memory");
        BAR();
        STAMP(1);

        {
            const float dt = rbf(__builtin_amdgcn_exp2f(bf_lo(r_dt) * 1.4426950408889634f));
            const float are = dt * bf_lo(r_are), aim = dt * bf_lo(r_aim);
            const float br = dt * bf_lo(r_bre), bi = dt * bf_lo(r_bim);
            const float cr = bf_lo(r_cre), ci = bf_lo(r_cim);
            const float wr = cr * br - ci * bi, wi = cr * bi + ci * br;
            const float mx = fmaxf(fabsf(wr), fabsf(wi)), mn = fminf(fabsf(wr), fabsf(wi));
            const float ratio = mx > 0.f ? mn / mx : 0.f;
            float* p = (float*)(smem + OFF_P) + (tid >> 1) * 8 + (tid & 1);
            p[0] = are * 1.4426950408889634f;
            p[2] = aim * 0.15915494309189535f;
            p[4] = __builtin_amdgcn_logf(mx) + 0.5f * __builtin_amdgcn_logf(fmaf(ratio, ratio, 1.0f));
            p[6] = atan2f(wi, wr) * 0.15915494309189535f;
        }
        if (tid == 0) {
            tap_prev = (u32x4){0, 0, 0, 0};
            tap_own[0] = (tap_own[0] & 0xffff0000u) | (pk_bf(bf_lo(tap_own[0]) + bf_lo(dA), 0.f) & 0xffffu);
        }
        stage_taps(smem, 0, tid, tap_prev, tap_own);
        cnt_arrive(smem, 0, lane);
        STAMP(2);

        const ConvAddr ca = conv_addr(smem, wave, lane);
        WAIT_VM(0);
        asm volatile("" :: "v"(code_pf));
        cnt_wait(smem, 0, 8);
        STAMP(4);
#pragma nounroll
        for (u32 seq = 1; seq <= 3; ++seq) {
            if (seq == 2) {
                const u32x4 tix0 = ((const u32x4*)tap_index.off)[tid], tix1 = ((const u32x4*)tap_index.off)[tid + 1];
                {
                    KState ks;
                    kgen_init(ks);
                    kgen_steps<4 - R_STEPS, true>(kgen_params(smem, wave, 0), lane, ks);
                    kgen_save(smem, wave, lane, ks);
                }
                STAMP(7);
                cnt_arrive(smem, 1, lane);
                cnt_wait(smem, 1, 8);
                kgen_reduce(smem, wave, lane, dB);
                cnt_arrive(smem, 2, lane);
                cnt_wait(smem, 2, 4);
                STAMP(23);
                s4_taps(smem, 0, tid, tix0, tix1);
                cnt_arrive(smem, 3, lane);
                cnt_wait(smem, 3, 8);
                STAMP(25);
            }
            conv_col<true>(smem, ca, wave, lane, seq);
            STAMP(seq == 1 ? 6 : seq == 2 ? 11 : 15);
        }
    } else {
        const int r = tid - 256, w = wave - 4;
        u32x2 skA = ld_b64(shiftk, r * 8), skB = ld_b64(shiftk, (r + 256) * 8);
        u32x4 tap_own = ld_b128(ssmk, (2 * id + 1) * 4096 + r * 16), tap_prev = ld_b128(ssmk, (2 * id + 1) * 4096 + (r ? r - 1 : 0) * 16);
        MEMFENCE(); SB();
        st_dev(rs, WS_H + 1 * WS_HS + id * 8192 + tid * 16, sent);
        MEMFENCE(); SB();
        const u32 epoch = launch_tag(ws);
        asm volatile("s_waitcnt vmcnt(7)\n\ts_barrier" ::: "memory");
        __builtin_amdgcn_s_setprio(2);
        ((u32*)(smem + OFF_RED))[r] = SENT;
        if (r < 16) ((u32*)(smem + OFF_CNT))[r] = 0u;
        asm volatile("s_waitcnt vmcnt(1)" : "+v"(skA), "+v"(skB), "+v"(tap_own), "+v"(tap_prev) :: "memory");
        BAR();
        STAMP(1);

        const u32 own_a = id * 8192 + r * 16, own_b = own_a + 4096;
        const u32 col_a = (r >> 1) * 8192 + (2 * id + (r & 1)) * 16, col_b = col_a + 128 * 8192;
        const u32 row_a = (r >> 1) * 8192 + id * 32 + (r & 1) * 16, row_b = row_a + 128 * 8192;
        char* const u_dst = smem + OFF_U + (r & 1) * U_STRIDE + 4080 - (r >> 1) * 16;
        const char* const y_src = smem + OFF_Y + (r & 1) * 4096 + (r >> 1) * 16;
        float* red = (float*)(smem + OFF_RED);

        const u32 flag_off = WS_FLAGS + (blockIdx.x & 31) * 1024 + lane * 16;
        u32x4 fl = {epoch, epoch, epoch, epoch};
        if (wave == 7) fl = poll_issue(rs, flag_off);

        Rows x, u0;
        {
            const u32 rp = (u32)(size_t)(LAS char*)(smem + OFF_R + ((lane & 15) >> 2) * R_ROW + (64 * w + 16 * (lane >> 4) + 4 * (lane & 3)) * 2);
            u32x2 kk[2][3], vv[2][2], qq[2][2];
            static_assert(4 * R_ROW == 4352, "offsets below");
            asm volatile("ds_read_b64_tr_b16 %0, %10\n\tds_read_b64_tr_b16 %1, %10 offset:4352\n\tds_read_b64_tr_b16 %2, %10 offset:8704\n\t"
                         "ds_read_b64_tr_b16 %3, %10 offset:13056\n\tds_read_b64_tr_b16 %4, %10 offset:17408\n\t"
                         "ds_read_b64_tr_b16 %5, %10 offset:512\n\tds_read_b64_tr_b16 %6, %10 offset:4864\n\tds_read_b64_tr_b16 %7, %10 offset:9216\n\t"
                         "ds_read_b64_tr_b16 %8, %10 offset:13568\n\tds_read_b64_tr_b16 %9, %10 offset:17920\n\ts_waitcnt lgkmcnt(0)"
                         : "=&v"(kk[0][0]), "=&v"(kk[0][1]), "=&v"(kk[0][2]), "=&v"(vv[0][0]), "=&v"(vv[0][1]),
                           "=&v"(kk[1][0]), "=&v"(kk[1][1]), "=&v"(kk[1][2]), "=&v"(vv[1][0]), "=&v"(vv[1][1]) : "v"(rp) : "memory");
            const u32 first = id ? 0xffffffffu : 0u;
            u32x4 o[2];
#pragma unroll
            for (int c = 0; c < 2; ++c) {
                const u32x2 sk = c ? skB : skA;
                const u32 kp = kk[c][0][0] & first, kq = kk[c][0][1] & first;
                const float kf[12] = {0.f, bf_hi(kp), bf_lo(kq), bf_hi(kq), bf_lo(kk[c][1][0]), bf_hi(kk[c][1][0]),
                                      bf_lo(kk[c][1][1]), bf_hi(kk[c][1][1]), bf_lo(kk[c][2][0]), bf_hi(kk[c][2][0]),
                                      bf_lo(kk[c][2][1]), bf_hi(kk[c][2][1])};
                float vf[8];
                unpack8((u32x4){vv[c][0][0], vv[c][0][1], vv[c][1][0], vv[c][1][1]}, vf);
                const float w0 = bf_lo(sk[0]), w1 = bf_hi(sk[0]), w2 = bf_lo(sk[1]), w3 = bf_hi(sk[1]);
                float kv[8];
#pragma unroll
                for (int j = 0; j < 8; ++j) kv[j] = fmaf(kf[j + 4], w0, fmaf(kf[j + 3], w1, fmaf(kf[j + 2], w2, kf[j + 1] * w3))) * vf[j];
                o[c] = pack8(kv);
            }
            publish(rs, WS_H + 0 * WS_HS + own_a, o[0]);
            publish(rs, WS_H + 0 * WS_HS + own_b, o[1]);
            asm volatile("ds_read_b64_tr_b16 %0, %4 offset:21760\n\tds_read_b64_tr_b16 %1, %4 offset:26112\n\t"
                         "ds_read_b64_tr_b16 %2, %4 offset:22272\n\tds_read_b64_tr_b16 %3, %4 offset:26624\n\ts_waitcnt lgkmcnt(0)"
                         : "=&v"(qq[0][0]), "=&v"(qq[0][1]), "=&v"(qq[1][0]), "=&v"(qq[1][1]) : "v"(rp) : "memory");
            unpack_rows((u32x4){qq[0][0][0], qq[0][0][1], qq[0][1][0], qq[0][1][1]}, x.a);
            unpack_rows((u32x4){qq[1][0][0], qq[1][0][1], qq[1][1][0], qq[1][1][1]}, x.b);
        }
        u64 t_s = __builtin_amdgcn_s_memrealtime();
        STAMP(2);
        u32 touch = 0;
        if (wave == 6) {
            if (lane < 4) touch = __builtin_amdgcn_raw_buffer_load_b32(rs, WS_H + (2 + lane) * WS_HS + id * 8192, 0, 16);
            else if (lane == 4) touch = ((const u32*)out)[id * 2048];
        }
        if (r == 0) {
            tap_prev = (u32x4){0, 0, 0, 0};
            tap_own[0] = (tap_own[0] & 0xffff0000u) | (pk_bf(bf_lo(tap_own[0]) + bf_hi(dA), 0.f) & 0xffffu);
        }
        stage_taps(smem, 1, r, tap_prev, tap_own);
        cnt_arrive(smem, 0, lane);
        if (wave == 7) {
            flags_finish(fl, rs, flag_off, epoch);
            if (blockIdx.x == 0 && lane == 0) __hip_atomic_store((GAS u32*)(ws + WS_EPOCH), epoch, RLX_AGENT);
            cnt_arrive(smem, 4, lane);
        }
        cnt_wait(smem, 4, 1);
        STAMP(3);

        wait_until(t_s + POLL_DELAY);
        Piece2 pc = poll2_issue(rs, WS_H + col_a, WS_H + col_b), pr;
        Rows yA, u;
        auto stage_input = [&](u32 h) {
            poll2_finish(pc, desc, hbuf(h) + col_a, hbuf(h) + col_b);
            *(u32x4*)u_dst = reverse8(pc.a);
            *(u32x4*)(u_dst - 2048) = reverse8(pc.b);
        };
        auto send_result = [&](u32 h) {
            publish(rs, hbuf(h) + own_a, *(const u32x4*)y_src);
            publish(rs, hbuf(h) + own_b, *(const u32x4*)(y_src + 2048));
            t_s = __builtin_amdgcn_s_memrealtime();
        };
        auto fetch_rows = [&](u32 h) {
            pr = poll2_issue(rs, hbuf(h) + row_a, hbuf(h) + row_b);
            poll2_finish(pr, desc, hbuf(h) + row_a, hbuf(h) + row_b);
        };
#pragma nounroll
        for (u32 it = 0; it < 2; ++it) {
            layer_norm(x, u0, red + 128 * it, lane, w);
            u32x4 tix0, tix1;
            if (it == 0) {
                stage_input(0);
                STAMP(21);
                asm volatile("" :: "v"(touch));
                asm volatile("" :: "v"(code_pf));
                BAR();
                STAMP(5);
                if (wave == 7 && lane < 32) __hip_atomic_store((GAS u32*)(ws + WS_FLAGS + 32768) + lane * 256 + id, epoch, RLX_AGENT);
                conv_row(smem, wave, lane, rs, id, 2, 27, [&] {
                    __builtin_amdgcn_s_setprio(0);
                    KState ks;
                    kgen_init(ks);
                    kgen_steps<R_STEPS, false>(kgen_params(smem, wave, 4 - R_STEPS), lane, ks);
                    kgen_save(smem, wave, lane, ks);
                    cnt_arrive(smem, 1, lane);
                    __builtin_amdgcn_s_setprio(2);
                    if (wave == 7) fl = poll_issue(rs, flag_off + 32768);
                });
                send_result(1);
                STAMP(6);
                tix0 = ((const u32x4*)tap_index.off)[r]; tix1 = ((const u32x4*)tap_index.off)[r + 1];
                if (wave == 7) {
                    flags_finish(fl, rs, flag_off + 32768, epoch);
                    cnt_arrive(smem, 5, lane);
                }
                wait_until(t_s + POLL_DELAY);
                cnt_wait(smem, 5, 1);
                fetch_rows(1);
                STAMP(8);
                unpack_rows(pr.a, yA.a);
                unpack_rows(pr.b, yA.b);
            }
#pragma unroll
            for (int i = 0; i < 4; ++i) { x.a[i] += u0.a[i] * yA.a[i]; x.b[i] += u0.b[i] * yA.b[i]; }
            round_rows(x.a);
            round_rows(x.b);
            layer_norm(x, u, red + 128 * it + 64, lane, w);
            publish(rs, hbuf(2 + 2 * it) + own_a, pack_rows(u.a));
            publish(rs, hbuf(2 + 2 * it) + own_b, pack_rows(u.b));
            t_s = __builtin_amdgcn_s_memrealtime();
            STAMP(it ? 13 : 9);

            wait_until(t_s + POLL_DELAY);
            pc = poll2_issue(rs, hbuf(2 + 2 * it) + col_a, hbuf(2 + 2 * it) + col_b);
            if (it == 0) {
                cnt_wait(smem, 2, 4);
                s4_taps(smem, 1, r, tix0, tix1);
                cnt_arrive(smem, 3, lane);
            }
            stage_input(2 + 2 * it);
            BAR();
            STAMP(it ? 14 : 10);
            conv_row(smem, wave, lane, rs, id, it ? -1 : 4, 28, [] {});
            send_result(3 + 2 * it);
            STAMP(it ? 15 : 11);

            wait_until(t_s + POLL_DELAY);
            fetch_rows(3 + 2 * it);
            STAMP(it ? 16 : 12);
            Rows y;
            unpack_rows(pr.a, y.a);
            unpack_rows(pr.b, y.b);
#pragma unroll
            for (int i = 0; i < 4; ++i) { x.a[i] += y.a[i]; x.b[i] += y.b[i]; }
            pr.a = round_rows(x.a);
            pr.b = round_rows(x.b);
        }
        {
            u16* dst = out + (8 * id) * DM + r;
#pragma unroll
            for (int j = 0; j < 4; ++j) {
                dst[(2 * j) * DM] = (u16)pr.a[j];
                dst[(2 * j) * DM + 256] = (u16)pr.b[j];
                dst[(2 * j + 1) * DM] = (u16)(pr.a[j] >> 16);
                dst[(2 * j + 1) * DM + 256] = (u16)(pr.b[j] >> 16);
            }
        }
        STAMP(17);
    }
    asm volatile("h3s4_text_b:");
}

extern "C" void kernel_launch(void* const* d_in, const int* in_sizes, int n_in,
                              void* d_out, int out_size, void* d_ws, size_t ws_size,
                              hipStream_t stream) {
    h3s4_kernel<<<256, 512, 0, stream>>>(
        (const char*)d_in[0], (const char*)d_in[1], (const char*)d_in[2], (const u32x2*)d_in[3], (const u32x4*)d_in[4],
        (const u32*)d_in[5], (const u16*)d_in[7], (const u16*)d_in[8], (const u16*)d_in[9], (const u16*)d_in[10],
        (const u16*)d_in[11], (const u16*)d_in[12], (const u16*)d_in[13], (const u32*)d_in[14], (u16*)d_out, (char*)d_ws
        );
}
```

```cpp
#define DIAG 0
#define DIAG_A 0
#define DIAG_B 0
#include <hip/hip_runtime.h>
#include <hip/hip_bf16.h>
#include <stdint.h>

typedef unsigned int u32;
typedef unsigned short u16;
typedef unsigned long long u64;
typedef __attribute__((ext_vector_type(4))) float f32x4;
typedef __attribute__((ext_vector_type(2))) float f32x2;
typedef __attribute__((ext_vector_type(4))) u32 u32x4;
typedef __attribute__((ext_vector_type(2))) u32 u32x2;
typedef __attribute__((ext_vector_type(4))) short s16x4;
typedef __attribute__((ext_vector_type(8))) __bf16 bf16x8;
typedef __attribute__((ext_vector_type(2))) __bf16 bf16x2;
#define LAS __attribute__((address_space(3)))
#define GAS __attribute__((address_space(1)))
#define RLX_AGENT __ATOMIC_RELAXED, __HIP_MEMORY_SCOPE_AGENT
#define SB() __builtin_amdgcn_sched_barrier(0)
#define BAR() asm volatile("s_waitcnt lgkmcnt(0)\n\ts_barrier" ::: "memory")
#define MEMFENCE() asm volatile("" ::: "memory")
#define WAIT_VM(n) do { MEMFENCE(); __builtin_amdgcn_s_waitcnt(0x0F70 | ((n) & 15) | (((n) >> 4) << 14)); MEMFENCE(); } while (0)

constexpr int DM = 512;
constexpr int NS = 128;
constexpr u32 SENT = 0xffffffffu;
constexpr u32 SPIN_CAP = 20000;
#define FILL_GAP 70
#define R_STEPS 1
#define POLL_DELAY 45

constexpr u32 WS_EPOCH = 0;
constexpr u32 WS_FLAGS = 4096;
constexpr u32 WS_H = 1u << 20;
constexpr u32 WS_HS = 2u << 20;
constexpr u32 WS_USED = WS_H + 6 * WS_HS;

constexpr int T_COPY = 4160, T_CH = 8 * T_COPY;
constexpr int U_STRIDE = 4672;
constexpr int R_ROW = 1088;
constexpr int KP_WAVE = 2816;
constexpr int OFF_T = 0;
constexpr int OFF_U = OFF_T + 2 * T_CH;
constexpr int OFF_X = OFF_U + 2 * U_STRIDE;
constexpr int OFF_Y = OFF_X + 16384;
constexpr int OFF_R = OFF_Y + 8192;
constexpr int OFF_P = OFF_R + 28 * R_ROW;
constexpr int OFF_KP = OFF_P + 4096;
constexpr int OFF_KC = OFF_KP + 8 * KP_WAVE;
constexpr int OFF_RED = OFF_KC + 2 * 648 * 2;
constexpr int OFF_CNT = OFF_RED + 1024;
constexpr int OFF_ST = OFF_CNT + 64;
constexpr int LDS_BYTES = OFF_ST + 512;
static_assert(LDS_BYTES <= 163840, "LDS");

#define STAMP(i) do { if ((threadIdx.x & 255) == 0) ((u64*)(smem + OFF_ST))[(i) + (threadIdx.x ? 32 : 0)] = __builtin_amdgcn_s_memrealtime(); } while (0)

__device__ __forceinline__ float bf_lo(u32 p) { return __builtin_bit_cast(float, p << 16); }
__device__ __forceinline__ float bf_hi(u32 p) { return __builtin_bit_cast(float, p & 0xffff0000u); }
__device__ __forceinline__ float bf_u16(u16 p) { return __builtin_bit_cast(float, (u32)p << 16); }
__device__ __forceinline__ u32 pk_bf(float lo, float hi) {
    bf16x2 v = {(__bf16)lo, (__bf16)hi};
    return __builtin_bit_cast(u32, v);
}
__device__ __forceinline__ float as_f(u32 x) { return __builtin_bit_cast(float, x); }
__device__ __forceinline__ u32 as_u(float x) { return __builtin_bit_cast(u32, x); }
__device__ __forceinline__ float rbf(float x) { return bf_lo(pk_bf(x, x)); }
__device__ __forceinline__ void unpack8(u32x4 p, float (&f)[8]) {
#pragma unroll
    for (int i = 0; i < 4; ++i) { f[2 * i] = bf_lo(p[i]); f[2 * i + 1] = bf_hi(p[i]); }
}
__device__ __forceinline__ u32x4 pack8(const float (&f)[8]) {
    return (u32x4){pk_bf(f[0], f[1]), pk_bf(f[2], f[3]), pk_bf(f[4], f[5]), pk_bf(f[6], f[7])};
}
__device__ __forceinline__ u32x4 reverse8(u32x4 p) {
    return (u32x4){__builtin_amdgcn_alignbit(p[3], p[3], 16), __builtin_amdgcn_alignbit(p[2], p[2], 16),
                   __builtin_amdgcn_alignbit(p[1], p[1], 16), __builtin_amdgcn_alignbit(p[0], p[0], 16)};
}
template <int CTRL> __device__ __forceinline__ float dppf(float v) {
    return __builtin_bit_cast(float, __builtin_amdgcn_update_dpp(0, __builtin_bit_cast(int, v), CTRL, 0xf, 0xf, true));
}

__device__ __forceinline__ void lds_dma16(const char* src, u32 lds) {
    u32 keep;
    asm volatile("s_mov_b32 %0, m0\n\ts_mov_b32 m0, %2\n\ts_nop 0\n\tglobal_load_lds_dwordx4 %1, off\n\ts_mov_b32 m0, %0"
                 : "=&s"(keep) : "v"(src), "s"(lds) : "memory");
}

__device__ __forceinline__ u32 ld_u16(const void* base, u32 off) {
    u32 r;
    asm volatile("global_load_ushort %0, %1, %2" : "=v"(r) : "v"(off), "s"(base) : "memory");
    return r;
}
__device__ __forceinline__ u32x2 ld_b64(const void* base, u32 off) {
    u32x2 r;
    asm volatile("global_load_dwordx2 %0, %1, %2" : "=v"(r) : "v"(off), "s"(base) : "memory");
    return r;
}
__device__ __forceinline__ u32x4 ld_b128(const void* base, u32 off) {
    u32x4 r;
    asm volatile("global_load_dwordx4 %0, %1, %2" : "=v"(r) : "v"(off), "s"(base) : "memory");
    return r;
}

typedef __amdgpu_buffer_rsrc_t rsrc_t;
__device__ __forceinline__ u32x4 ld_dev(rsrc_t rs, u32 off) { return __builtin_amdgcn_raw_buffer_load_b128(rs, off, 0, 16); }
__device__ __forceinline__ void st_dev(rsrc_t rs, u32 off, u32x4 v) { __builtin_amdgcn_raw_buffer_store_b128(v, rs, off, 0, 16); }
__device__ __forceinline__ void publish(rsrc_t rs, u32 off, u32x4 v) {
#pragma unroll
    for (int i = 0; i < 4; ++i) v[i] = v[i] < SENT - 1u ? v[i] : SENT - 1u;
    st_dev(rs, off, v);
}
__device__ __forceinline__ u32 launch_tag(const char* ws) {
    u32 e;
    asm volatile("s_load_dword %0, %1, 0x0\n\ts_waitcnt lgkmcnt(0)" : "=&s"(e) : "s"(ws + WS_EPOCH) : "memory");
    return e + 1u;
}
__device__ __forceinline__ void wait_until(u64 t) {
    while (__builtin_amdgcn_s_memrealtime() < t) __builtin_amdgcn_s_sleep(1);
}
__device__ __forceinline__ u32x4 poll_issue(rsrc_t rs, u32 off) {
    MEMFENCE();
    u32x4 x = ld_dev(rs, off);
    MEMFENCE();
    return x;
}
struct Piece2 { u32x4 a, b; };
__device__ __forceinline__ Piece2 poll2_issue(rsrc_t rs, u32 off_a, u32 off_b) {
    MEMFENCE();
    Piece2 p = {ld_dev(rs, off_a), ld_dev(rs, off_b)};
    MEMFENCE();
    return p;
}
__device__ __forceinline__ bool has_sentinel(const u32x4& x) {
    u32 mx = x[0] > x[1] ? x[0] : x[1];
    mx = mx > x[2] ? mx : x[2];
    mx = mx > x[3] ? mx : x[3];
    return mx == SENT;
}
__device__ __forceinline__ void poll2_finish(Piece2& p, u32x4 desc, u32 off_a, u32 off_b) {
#pragma nounroll
    for (u32 spins = 0; spins < SPIN_CAP; ++spins) {
        const bool bad = has_sentinel(p.a) || has_sentinel(p.b);
        if (__builtin_amdgcn_ballot_w64(bad) == 0ull) break;
        __builtin_amdgcn_s_sleep(1);
        if (bad) asm volatile("buffer_load_dwordx4 %0, %2, %4, 0 offen sc1\n\tbuffer_load_dwordx4 %1, %3, %4, 0 offen sc1\n\ts_waitcnt vmcnt(0)"
                              : "+v"(p.a), "+v"(p.b) : "v"(off_a), "v"(off_b), "s"(desc) : "memory");
    }
}
__device__ __forceinline__ void flags_finish(u32x4& fl, rsrc_t rs, u32 off, u32 epoch) {
    u32 spins = 0;
    while (__builtin_amdgcn_ballot_w64(fl[0] != epoch || fl[1] != epoch || fl[2] != epoch || fl[3] != epoch) != 0ull) {
        if (++spins > SPIN_CAP) break;
        __builtin_amdgcn_s_sleep(2);
        fl = poll_issue(rs, off);
    }
}

__device__ __forceinline__ void cnt_arrive(char* smem, int k, int lane) {
    asm volatile("s_waitcnt lgkmcnt(0)" ::: "memory");
    if (lane == 0) __hip_atomic_fetch_add((LAS u32*)(smem + OFF_CNT) + k, 1u, __ATOMIC_RELAXED, __HIP_MEMORY_SCOPE_WORKGROUP);
}
__device__ __forceinline__ void cnt_wait(char* smem, int k, u32 target) {
    u32 spins = 0;
    while (__hip_atomic_load((LAS u32*)(smem + OFF_CNT) + k, __ATOMIC_RELAXED, __HIP_MEMORY_SCOPE_WORKGROUP) < target) {
        if (++spins > 50 * SPIN_CAP) break;
        __builtin_amdgcn_s_sleep(1);
    }
    MEMFENCE();
}

struct Rows { f32x2 a[4], b[4]; };
__device__ __forceinline__ void unpack_rows(u32x4 p, f32x2 (&f)[4]) {
#pragma unroll
    for (int i = 0; i < 4; ++i) f[i] = (f32x2){bf_lo(p[i]), bf_hi(p[i])};
}
__device__ __forceinline__ u32x4 pack_rows(const f32x2 (&f)[4]) {
    return (u32x4){pk_bf(f[0][0], f[0][1]), pk_bf(f[1][0], f[1][1]), pk_bf(f[2][0], f[2][1]), pk_bf(f[3][0], f[3][1])};
}
__device__ __forceinline__ u32x4 round_rows(f32x2 (&f)[4]) {
    u32x4 p = pack_rows(f);
    asm("" : "+v"(p));
    unpack_rows(p, f);
    return p;
}
__device__ __forceinline__ void layer_norm(const Rows& x, Rows& u, float* red, int lane, int w) {
#pragma clang fp contract(fast)
    u32 a[8], b[8];
#pragma unroll
    for (int i = 0; i < 4; ++i) {
        const f32x2 s1 = x.a[i] + x.b[i], s2 = x.a[i] * x.a[i] + x.b[i] * x.b[i];
        a[2 * i] = as_u(s1[0]); a[2 * i + 1] = as_u(s1[1]);
        b[2 * i] = as_u(s2[0]); b[2 * i + 1] = as_u(s2[1]);
    }
    SB();
#pragma unroll
    for (int j = 0; j < 8; ++j) {
        const auto s = __builtin_amdgcn_permlane32_swap(a[j], b[j], false, false);
        a[j] = s[0]; b[j] = s[1];
    }
    SB();
    float r[8];
#pragma unroll
    for (int j = 0; j < 8; ++j) r[j] = as_f(a[j]) + as_f(b[j]);
    SB();
#pragma unroll
    for (int j = 0; j < 4; ++j) {
        const auto s = __builtin_amdgcn_permlane16_swap(as_u(r[j]), as_u(r[j + 4]), false, false);
        a[j] = s[0]; b[j] = s[1];
    }
    SB();
    float s4[4];
#pragma unroll
    for (int j = 0; j < 4; ++j) s4[j] = as_f(a[j]) + as_f(b[j]);
    SB();
#pragma unroll
    for (int j = 0; j < 4; ++j) s4[j] += dppf<0x128>(s4[j]);
    const float t0 = (lane & 8) ? s4[2] : s4[0], t1 = (lane & 8) ? s4[3] : s4[1];
    const float c0 = t0 + dppf<0x141>(t0), c1 = t1 + dppf<0x141>(t1);
    float s = (lane & 4) ? c1 : c0;
    s += dppf<0xB1>(s);
    s += dppf<0x4E>(s);
    const u32 sb = as_u(s) < SENT - 1u ? as_u(s) : SENT - 1u;
    if ((lane & 3) == 0) ((volatile LAS u32*)(LAS char*)(char*)red)[(lane >> 2) * 4 + w] = sb;
    u32x4 p;
    u32 spins = 0;
    while (true) {
        p = *(volatile LAS u32x4*)((LAS char*)(char*)red + (lane & 15) * 16);
        u32 mx = p[0] > p[1] ? p[0] : p[1];
        mx = mx > p[2] ? mx : p[2];
        mx = mx > p[3] ? mx : p[3];
        if (__builtin_amdgcn_ballot_w64(mx == SENT) == 0ull) break;
        if (++spins > 50 * SPIN_CAP) break;
    }
    const float m = ((as_f(p[0]) + as_f(p[1])) + (as_f(p[2]) + as_f(p[3]))) * (1.0f / DM);
    const float ex2 = dppf<0x128>(m);
    const float rstd = __builtin_amdgcn_rsqf(fmaxf(fmaf(-m, m, ex2), 0.f) + 1e-5f);
    const float off = -m * rstd;
    const f32x2 R[4] = {{dppf<0x150>(rstd), dppf<0x151>(rstd)}, {dppf<0x152>(rstd), dppf<0x153>(rstd)},
                        {dppf<0x154>(rstd), dppf<0x155>(rstd)}, {dppf<0x156>(rstd), dppf<0x157>(rstd)}};
    const f32x2 O[4] = {{dppf<0x150>(off), dppf<0x151>(off)}, {dppf<0x152>(off), dppf<0x153>(off)},
                        {dppf<0x154>(off), dppf<0x155>(off)}, {dppf<0x156>(off), dppf<0x157>(off)}};
#pragma unroll
    for (int i = 0; i < 4; ++i) { u.a[i] = x.a[i] * R[i] + O[i]; u.b[i] = x.b[i] * R[i] + O[i]; }
}

__device__ __forceinline__ void stage_taps(char* smem, int ch, int i, u32x4 prev, u32x4 own) {
    const u32 D[8] = {prev[0], prev[1], prev[2], prev[3], own[0], own[1], own[2], own[3]};
    u32 O[7];
#pragma unroll
    for (int y = 0; y < 7; ++y) O[y] = __builtin_amdgcn_alignbit(D[y + 1], D[y], 16);
    char* base = smem + OFF_T + ch * T_CH + (i + 1) * 16;
#pragma unroll
    for (int c = 0; c < 8; ++c) {
        const int y = (c + 1) >> 1;
        const u32x4 v = (c & 1) ? (u32x4){D[y], D[y + 1], D[y + 2], D[y + 3]} : (u32x4){O[y], O[y + 1], O[y + 2], O[y + 3]};
        *(u32x4*)(base + c * T_COPY) = v;
    }
}

__device__ __forceinline__ f32x4 mfma16(u32x4 a, u32x4 b, f32x4 c) {
    return __builtin_amdgcn_mfma_f32_16x16x32_bf16(__builtin_bit_cast(bf16x8, a), __builtin_bit_cast(bf16x8, b), c, 0, 0, 0);
}
struct KState { f32x4 acc[3]; };
__device__ __forceinline__ void kgen_init(KState& ks) {
#pragma unroll
    for (int e = 0; e < 3; ++e) ks.acc[e] = (f32x4){0, 0, 0, 0};
}
#define OPQ(x) asm volatile("" : "+v"(x))
template <int NS_, bool PK>
__device__ __forceinline__ void kgen_steps(const f32x4* P, int lane, KState& ks) {
#pragma clang fp contract(fast)
    const int g = lane >> 4, m = lane & 15;
    const float lo = (float)(8 * m);
    const float hi[3] = {(float)(128 * (m >> 3) + (m & 7)),
                         (float)(m < 8 ? 256 + 128 * (m >> 2) + 2 * (m & 3) : 512 + 128 * ((m - 8) >> 1) + 4 * (m & 1)),
                         (float)(m < 8 ? 1024 + 128 * m : 2048)};
    auto eval = [&](f32x2 ex, f32x2 an, bool conj, u32& o0, u32& o1) {
        if (!PK) { OPQ(ex[0]); OPQ(ex[1]); OPQ(an[0]); OPQ(an[1]); }
        const float m0 = __builtin_amdgcn_exp2f(ex[0]), m1 = __builtin_amdgcn_exp2f(ex[1]);
        const float f0 = __builtin_amdgcn_fractf(an[0]), f1 = __builtin_amdgcn_fractf(an[1]);
        const float c0 = __builtin_amdgcn_cosf(f0), c1 = __builtin_amdgcn_cosf(f1);
        const float s0 = __builtin_amdgcn_sinf(f0), s1 = __builtin_amdgcn_sinf(f1);
        float r0, r1, i0, i1;
        if (PK) {
            const f32x2 r = (f32x2){c0, c1} * (f32x2){m0, m1}, i = (f32x2){s0, s1} * (f32x2){m0, m1};
            r0 = r[0]; r1 = r[1]; i0 = i[0]; i1 = i[1];
        } else {
            r0 = c0 * m0; OPQ(r0); i0 = s0 * m0; OPQ(i0); r1 = c1 * m1; OPQ(r1); i1 = s1 * m1; OPQ(i1);
        }
        o0 = pk_bf(r0, conj ? -i0 : i0);
        o1 = pk_bf(r1, conj ? -i1 : i1);
    };
    auto affine = [&](f32x2 a, float p, f32x2 b) -> f32x2 {
        if (PK) return a * p + b;
        float x = fmaf(a[0], p, b[0]), y = fmaf(a[1], p, b[1]);
        OPQ(x); OPQ(y);
        return (f32x2){x, y};
    };
#pragma unroll
    for (int s = 0; s < NS_; ++s) {
        u32x4 B, A[3];
#pragma unroll
        for (int h = 0; h < 2; ++h) {
            const f32x4 p0 = P[16 * s + 4 * g + 2 * h], p1 = P[16 * s + 4 * g + 2 * h + 1];
            u32 o0, o1;
            eval(affine(p0.xy, lo, (f32x2){0.f, 0.f}), affine(p0.zw, lo, (f32x2){0.f, 0.f}), true, o0, o1);
            B[2 * h] = o0; B[2 * h + 1] = o1;
#pragma unroll
            for (int e = 0; e < 3; ++e) {
                eval(affine(p0.xy, hi[e], p1.xy), affine(p0.zw, hi[e], p1.zw), false, o0, o1);
                A[e][2 * h] = o0; A[e][2 * h + 1] = o1;
            }
        }
#pragma unroll
        for (int e = 0; e < 3; ++e) ks.acc[e] = mfma16(A[e], B, ks.acc[e]);
    }
}
__device__ __forceinline__ const f32x4* kgen_params(char* smem, int wave, int step) {
    return (const f32x4*)(smem + OFF_P) + ((wave >> 1) & 1) * 128 + ((wave & 1) * 4 + step) * 16;
}
__device__ __forceinline__ void kgen_save(char* smem, int wave, int lane, const KState& ks) {
    char* p = smem + OFF_KP + wave * KP_WAVE + lane * 16;
    *(f32x4*)p = ks.acc[0];
    *(f32x4*)(p + 1024) = ks.acc[1];
    if (lane < 48) *(f32x4*)(p + 2048) = ks.acc[2];
}
__device__ __forceinline__ f32x4 kgen_sum(const char* p) {
    return (*(const f32x4*)p + *(const f32x4*)(p + KP_WAVE)) + (*(const f32x4*)(p + 4 * KP_WAVE) + *(const f32x4*)(p + 5 * KP_WAVE));
}
__device__ __forceinline__ void kgen_reduce(char* smem, int wave, int lane, u32 dskip) {
    const int c = wave >> 1, j = lane & 15, g = lane >> 4;
    char* KC = smem + OFF_KC + c * 1296;
    const char* p = smem + OFF_KP + 2 * c * KP_WAVE + lane * 16;
    if ((wave & 1) == 0) {
        f32x4 s = kgen_sum(p);
        const f32x4 t = kgen_sum(smem + OFF_KP + 2 * c * KP_WAVE + 2048 + (lane < 48 ? lane : 0) * 16);
        if (lane == 0) s[0] += c ? bf_hi(dskip) : bf_lo(dskip);
        *(u32x2*)(KC + 2 * (128 * (g >> 1) + 4 * (g & 1) + 8 * j)) = (u32x2){pk_bf(s[0], s[1]), pk_bf(s[2], s[3])};
        const u32 lo = pk_bf(t[0], t[1]), hi = pk_bf(t[2], t[3]);
        u16* o = (u16*)KC + 512 + 64 * g + j;
        if (g < 2) { o[0] = (u16)lo; o[16] = (u16)(lo >> 16); o[32] = (u16)hi; o[48] = (u16)(hi >> 16); }
        else if (lane == 32) { ((u16*)KC)[640] = (u16)lo; ((u16*)KC)[641] = 0; }
    } else {
        const f32x4 s = kgen_sum(p + 1024);
        const u32 lo = pk_bf(s[0], s[1]), hi = pk_bf(s[2], s[3]);
        if (g < 2) *(u32x2*)(KC + 2 * (256 + 64 * g + 4 * j)) = (u32x2){lo, hi};
        else {
            u32* o = (u32*)(KC + 2 * (384 + 64 * (g - 2) + 2 * j));
            o[0] = lo; o[16] = hi;
        }
    }
}

struct TapIndex {
    u16 off[2048 + 8];
    constexpr TapIndex() : off() {
        for (int i = 0; i < 8; ++i) off[i] = 641 * 2;
        for (int l = 0; l < 2048; ++l) {
            int e = 0;
            while ((l >> e) >= 256) ++e;
            int v = l >> e;
            if (e > 0) {
                const int rem = l & ((1 << e) - 1), half = 1 << (e - 1);
                if (rem > half || (rem == half && (v & 1))) ++v;
            }
            off[8 + l] = (u16)(2 * (128 * e + v));
        }
    }
};
__device__ const TapIndex tap_index = TapIndex();

__device__ __forceinline__ void s4_taps(char* smem, int ch, int i, u32x4 tix0, u32x4 tix1) {
    const char* kc = smem + OFF_KC + ch * 1296;
    u32 d[8];
#pragma unroll
    for (int j = 0; j < 8; ++j) {
        const u32 o = j < 4 ? tix0[j] : tix1[j - 4];
        d[j] = (u32)*(const u16*)(kc + (o & 0xffffu)) | ((u32)*(const u16*)(kc + (o >> 16)) << 16);
    }
    stage_taps(smem, ch, i, (u32x4){d[0], d[1], d[2], d[3]}, (u32x4){d[4], d[5], d[6], d[7]});
}

struct Frags { u32x4 A[8], B[8]; };
template <bool INIT> __device__ __forceinline__ void mma(f32x4& acc, const u32x4& a, const u32x4& b) {
    if constexpr (INIT) asm volatile("v_mfma_f32_16x16x32_bf16 %0, %1, %2, 0" : "=&v"(acc) : "v"(a), "v"(b));
    else asm volatile("v_mfma_f32_16x16x32_bf16 %0, %1, %2, %0" : "+v"(acc) : "v"(a), "v"(b));
}
template <int I> __device__ __forceinline__ void load_one(Frags& f, const char* ap, const char* bp) {
    if constexpr (I == 0) f.A[0] = *(const u32x4*)ap;
    else if constexpr (I <= 8) f.B[I - 1] = *(const u32x4*)(bp + 512 * (8 - I));
    else f.A[I - 8] = *(const u32x4*)(ap + 512 * (I - 8));
}
template <int I> __device__ __forceinline__ void load_all(Frags& f, const char* ap, const char* bp) {
    if constexpr (I < 16) { load_one<I>(f, ap, bp); SB(); load_all<I + 1>(f, ap, bp); }
}
template <int T0, int T1, int T2, int T3> struct TileSet {
    static constexpr int tile(int i) { return i == 0 ? T0 : i == 1 ? T1 : i == 2 ? T2 : T3; }
    static constexpr int walk(int I, bool want_tile) {
        int n = 0;
        for (int a = 0; a < 8; ++a)
            for (int j = 0; j < 4; ++j)
                if (tile(j) >= a) { if (n == I) return want_tile ? tile(j) : a; ++n; }
        return -1;
    }
};
constexpr int tri_a(int I) { int a = 0; while (I >= 8 - a) { I -= 8 - a; ++a; } return a; }
constexpr int tri_b(int I) { int a = 0; while (I >= 8 - a) { I -= 8 - a; ++a; } return I; }
template <int I, bool FIRST> __device__ __forceinline__ void class_step(const Frags& cur, Frags& nxt, f32x4 (&acc)[8], const char* ap, const char* bp) {
    if constexpr (I < 36) {
        constexpr int a = tri_a(I), b = tri_b(I);
        mma<FIRST && a == 0>(acc[a + b], cur.A[a], cur.B[b]);
        if constexpr ((I & 1) == 1 && (I >> 1) < 16) load_one<(I >> 1)>(nxt, ap, bp);
        SB();
        class_step<I + 1, FIRST>(cur, nxt, acc, ap, bp);
    }
}
template <class TS, int I, int TO, int PRE> __device__ __forceinline__ void set_step(const Frags& cur, Frags& nxt, f32x4 (&acc)[8], const char* ap, const char* bp) {
    if constexpr (I < TO) {
        constexpr int t = TS::walk(I, true), a = TS::walk(I, false);
        mma<false>(acc[t], cur.A[a], cur.B[t - a]);
        if constexpr (PRE == 1 && (I * 11) / 18 != ((I + 1) * 11) / 18) load_one<(I * 11) / 18>(nxt, ap, bp);
        if constexpr (PRE == 2 && I < 5) load_one<11 + I>(nxt, ap, bp);
        SB();
        set_step<TS, I + 1, TO, PRE>(cur, nxt, acc, ap, bp);
    }
}
template <class TS, int J> __device__ __forceinline__ void tile_step(const Frags& cur, f32x4 (&acc)[8]) {
    constexpr int t = TS::tile(J);
#pragma unroll
    for (int a = 0; a <= t; ++a) { mma<false>(acc[t], cur.A[a], cur.B[t - a]); SB(); }
}
template <class TS, int J> __device__ __forceinline__ void tile_out(char* Y, f32x4 (&acc)[8], const f32x4 (&o)[4]) {
    constexpr int t = TS::tile(J);
    const f32x4 s = acc[t] + o[J];
    const u32x2 pk = {pk_bf(s[0], s[1]), pk_bf(s[2], s[3])};
    *(u32x2*)(Y + 512 * t) = pk;
    SB();
}
template <class SEND, class KEEP>
__device__ __forceinline__ void conv_half(char* smem, const u32x4 (&A0)[8], const char* ap, const char* bp, int wave, int lane, int ch, u32 seq) {
    f32x4 acc[8];
    Frags f0, f1;
#pragma unroll
    for (int a = 0; a < 8; ++a) f0.A[a] = A0[a];
#pragma unroll
    for (int b = 0; b < 8; ++b) { f0.B[b] = *(const u32x4*)(bp + 512 * (7 - b)); SB(); }
    class_step<0, true>(f0, f1, acc, ap + 64, bp + 64);
    class_step<0, false>(f1, f0, acc, ap + 128, bp + 128);
    set_step<SEND, 0, 18, 1>(f0, f1, acc, ap + 192, bp + 192);
    set_step<SEND, 0, 18, 2>(f1, f1, acc, ap + 192, bp + 192);
    set_step<KEEP, 0, 4, 0>(f0, f1, acc, ap, bp);
    asm volatile("s_nop 3" : "+v"(acc[SEND::tile(0)]), "+v"(acc[SEND::tile(1)]), "+v"(acc[SEND::tile(2)]), "+v"(acc[SEND::tile(3)]));
    f32x4* X = (f32x4*)(smem + OFF_X);
#pragma unroll
    for (int i = 0; i < 4; ++i) X[(wave * 4 + i) * 64 + lane] = acc[SEND::tile(i)];
    volatile LAS u32* xf = (volatile LAS u32*)(LAS char*)(smem + OFF_CNT + 32);
    xf[wave] = seq;
    SB();
    set_step<KEEP, 4, 14, 0>(f0, f1, acc, ap, bp);
    u32 seen = xf[wave ^ 1];
    SB();
    set_step<KEEP, 14, 18, 0>(f0, f1, acc, ap, bp);
    for (u32 spins = 0; seen < seq && spins < 50 * SPIN_CAP; ++spins) seen = xf[wave ^ 1];
    SB();
    f32x4 o[4];
#pragma unroll
    for (int i = 0; i < 4; ++i) o[i] = X[((wave ^ 1) * 4 + i) * 64 + lane];
    SB();
    char* Y = smem + OFF_Y + ch * 4096 + (16 * (lane & 15) + 4 * (lane >> 4)) * 2;
    tile_step<KEEP, 0>(f1, acc);
    tile_step<KEEP, 1>(f1, acc);
    tile_step<KEEP, 2>(f1, acc);
    asm volatile("s_nop 3" : "+v"(acc[KEEP::tile(0)]), "+v"(acc[KEEP::tile(1)]));
    tile_out<KEEP, 0>(Y, acc, o);
    tile_out<KEEP, 1>(Y, acc, o);
    tile_step<KEEP, 3>(f1, acc);
    asm volatile("" : "+v"(acc[KEEP::tile(2)]));
    tile_out<KEEP, 2>(Y, acc, o);
    asm volatile("s_nop 7" : "+v"(acc[KEEP::tile(3)]));
    tile_out<KEEP, 3>(Y, acc, o);
}
struct ConvAddr { const char* ap; const char* bp; };
__device__ __forceinline__ ConvAddr conv_addr(char* smem, int wave, int lane) {
    const int ch = (wave >> 1) & 1, hf = wave & 1;
    const int m = lane & 15, g = lane >> 4;
    ConvAddr c;
    c.ap = smem + OFF_T + ch * T_CH + (m & 7) * T_COPY + 16 * (m >> 3) + 16 * g + 256 * hf;
    c.bp = smem + OFF_U + ch * U_STRIDE + 4064 - 32 * m + 16 * g + 256 * hf - 512 * 7;
    return c;
}
__device__ __forceinline__ void conv_preload(u32x4 (&A0)[8], const ConvAddr& c) {
#pragma unroll
    for (int a = 0; a < 8; ++a) A0[a] = *(const u32x4*)(c.ap + 512 * a);
}
template <bool EARLY>
__device__ __forceinline__ void conv_col(char* smem, const ConvAddr& c, int wave, int lane, u32 seq) {
    u32x4 A0[8];
    if (EARLY) conv_preload(A0, c);
    BAR();
    if (!EARLY) conv_preload(A0, c);
    if ((wave & 1) == 0) conv_half<TileSet<1, 3, 4, 6>, TileSet<0, 2, 5, 7>>(smem, A0, c.ap, c.bp, wave, lane, wave >> 1, seq);
    else conv_half<TileSet<0, 2, 5, 7>, TileSet<1, 3, 4, 6>>(smem, A0, c.ap, c.bp, wave, lane, wave >> 1, seq);
    STAMP(26 + seq);
    BAR();
}
template <class PRE>
__device__ __forceinline__ void conv_row(char* smem, int wave, int lane, rsrc_t rs, int id, int fill0, int st, PRE pre) {
    const u32x4 sent = {SENT, SENT, SENT, SENT};
    const u32 off = WS_H + (fill0 < 0 ? 0 : fill0) * WS_HS + id * 8192 + ((wave - 4) * 64 + lane) * 16;
    const u64 t0 = __builtin_amdgcn_s_memrealtime();
    if (fill0 >= 0) { st_dev(rs, off, sent); st_dev(rs, off + 4096, sent); }
    pre();
    if (fill0 >= 0) {
        wait_until(t0 + FILL_GAP);
        st_dev(rs, off + WS_HS, sent); st_dev(rs, off + WS_HS + 4096, sent);
        WAIT_VM(2);
    }
    STAMP(st);
    BAR();
}

__global__ void __launch_bounds__(512, 2)
h3s4_kernel(const char* __restrict__ q, const char* __restrict__ k, const char* __restrict__ v,
            const u32x2* __restrict__ shiftk, const u32x4* __restrict__ ssmk, const u32* __restrict__ ssmD,
            const u16* __restrict__ Are, const u16* __restrict__ Aim, const u16* __restrict__ Bre, const u16* __restrict__ Bim,
            const u16* __restrict__ Cre, const u16* __restrict__ Cim, const u16* __restrict__ logdt, const u32* __restrict__ Dskip,
            u16* __restrict__ out, char* ws
            ) {
    __shared__ __attribute__((aligned(16))) char smem[LDS_BYTES];
    const int tid = threadIdx.x, lane = tid & 63, wave = __builtin_amdgcn_readfirstlane(tid >> 6);
    const int id = (blockIdx.x & 7) * 32 + (blockIdx.x >> 3);
    const rsrc_t rs = __builtin_amdgcn_make_buffer_rsrc(ws, 0, WS_USED, 0x00020000);
    const u32x4 desc = {(u32)(size_t)ws, (u32)((size_t)ws >> 32) & 0xffffu, WS_USED, 0x00020000u};
    STAMP(0);

    u32 code_pf = 0;
    {
        u64 pc; u32 len;
        asm volatile("s_getpc_b64 %0\nh3s4_text_a:\n\ts_mov_b32 %1, h3s4_text_b - h3s4_text_a" : "=s"(pc), "=s"(len));
        const u32 off = ((blockIdx.x >> 3) * 8 + wave) * 256 + lane * 4;
        if (off + 4 <= len) asm volatile("global_load_dword %0, %1, %2" : "=v"(code_pf) : "v"(off), "s"(pc) : "memory");
    }
    const u32x4 sent = {SENT, SENT, SENT, SENT};
    const u32 dA = ssmD[id], dB = Dskip[id];
    MEMFENCE(); SB();
    st_dev(rs, WS_H + 0 * WS_HS + id * 8192 + tid * 16, sent);
    MEMFENCE(); SB();
#pragma unroll
    for (int s = 0; s < 4; ++s) {
        const int slot = wave + 8 * s;
        if (slot < 28) {
            int row = 8 * id + (slot < 12 ? slot - 4 : slot < 20 ? slot - 12 : slot - 20);
            row = row < 0 ? 0 : row;
            const char* src = slot < 12 ? k : slot < 20 ? v : q;
            lds_dma16(src + row * 1024 + lane * 16, (u32)(size_t)(LAS char*)(smem + OFF_R) + slot * R_ROW);
        }
    }

    if (wave < 4) {
        u32x4 tap_own = ld_b128(ssmk, (2 * id) * 4096 + tid * 16), tap_prev = ld_b128(ssmk, (2 * id) * 4096 + (tid ? tid - 1 : 0) * 16);
        const int pd = 2 * id + (tid >> 7), po = (pd * NS + (tid & 127)) * 2;
        u32 r_dt = ld_u16(logdt, pd * 2), r_are = ld_u16(Are, po), r_aim = ld_u16(Aim, po), r_bre = ld_u16(Bre, po);
        u32 r_bim = ld_u16(Bim, po), r_cre = ld_u16(Cre, po), r_cim = ld_u16(Cim, po);
        MEMFENCE(); SB();
        st_dev(rs, WS_H + 1 * WS_HS + id * 8192 + tid * 16, sent);
        MEMFENCE(); SB();
        const u32 epoch = launch_tag(ws);
        asm volatile("s_waitcnt vmcnt(13)\n\ts_barrier" ::: "memory");
        if (tid < 32) __hip_atomic_store((GAS u32*)(ws + WS_FLAGS) + tid * 256 + id, epoch, RLX_AGENT);
        STAMP(19);
        if (tid < 60) *(u32x4*)(smem + OFF_U + (tid / 30) * U_STRIDE + 4096 + (tid % 30) * 16) = (u32x4){0, 0, 0, 0};
        else if (tid >= 64 && tid < 80) *(u32x4*)(smem + OFF_T + (tid - 64) * T_COPY) = (u32x4){0, 0, 0, 0};
        asm volatile("s_waitcnt vmcnt(1)" : "+v"(tap_own), "+v"(tap_prev), "+v"(r_dt), "+v"(r_are), "+v"(r_aim), "+v"(r_bre), "+v"(r_bim), "+v"(r_cre), "+v"(r_cim) :: "memory");
        BAR();
        STAMP(1);

        {
            const float dt = rbf(__builtin_amdgcn_exp2f(bf_lo(r_dt) * 1.4426950408889634f));
            const float are = dt * bf_lo(r_are), aim = dt * bf_lo(r_aim);
            const float br = dt * bf_lo(r_bre), bi = dt * bf_lo(r_bim);
            const float cr = bf_lo(r_cre), ci = bf_lo(r_cim);
            const float wr = cr * br - ci * bi, wi = cr * bi + ci * br;
            const float mx = fmaxf(fabsf(wr), fabsf(wi)), mn = fminf(fabsf(wr), fabsf(wi));
            const float ratio = mx > 0.f ? mn / mx : 0.f;
            float* p = (float*)(smem + OFF_P) + (tid >> 1) * 8 + (tid & 1);
            p[0] = are * 1.4426950408889634f;
            p[2] = aim * 0.15915494309189535f;
            p[4] = __builtin_amdgcn_logf(mx) + 0.5f * __builtin_amdgcn_logf(fmaf(ratio, ratio, 1.0f));
            p[6] = atan2f(wi, wr) * 0.15915494309189535f;
        }
        if (tid == 0) {
            tap_prev = (u32x4){0, 0, 0, 0};
            tap_own[0] = (tap_own[0] & 0xffff0000u) | (pk_bf(bf_lo(tap_own[0]) + bf_lo(dA), 0.f) & 0xffffu);
        }
        stage_taps(smem, 0, tid, tap_prev, tap_own);
        cnt_arrive(smem, 0, lane);
        STAMP(2);

        const ConvAddr ca = conv_addr(smem, wave, lane);
        WAIT_VM(0);
        asm volatile("" :: "v"(code_pf));
        cnt_wait(smem, 0, 8);
        STAMP(4);
#pragma nounroll
        for (u32 seq = 1; seq <= 3; ++seq) {
            if (seq == 2) {
                const u32x4 tix0 = ((const u32x4*)tap_index.off)[tid], tix1 = ((const u32x4*)tap_index.off)[tid + 1];
                {
                    KState ks;
                    kgen_init(ks);
                    kgen_steps<4 - R_STEPS, true>(kgen_params(smem, wave, 0), lane, ks);
                    kgen_save(smem, wave, lane, ks);
                }
                STAMP(7);
                cnt_arrive(smem, 1, lane);
                cnt_wait(smem, 1, 8);
                kgen_reduce(smem, wave, lane, dB);
                cnt_arrive(smem, 2, lane);
                cnt_wait(smem, 2, 4);
                STAMP(23);
                s4_taps(smem, 0, tid, tix0, tix1);
                cnt_arrive(smem, 3, lane);
                cnt_wait(smem, 3, 8);
                STAMP(25);
            }
            conv_col<true>(smem, ca, wave, lane, seq);
            STAMP(seq == 1 ? 6 : seq == 2 ? 11 : 15);
        }
    } else {
        const int r = tid - 256, w = wave - 4;
        u32x2 skA = ld_b64(shiftk, r * 8), skB = ld_b64(shiftk, (r + 256) * 8);
        u32x4 tap_own = ld_b128(ssmk, (2 * id + 1) * 4096 + r * 16), tap_prev = ld_b128(ssmk, (2 * id + 1) * 4096 + (r ? r - 1 : 0) * 16);
        MEMFENCE(); SB();
        st_dev(rs, WS_H + 1 * WS_HS + id * 8192 + tid * 16, sent);
        MEMFENCE(); SB();
        const u32 epoch = launch_tag(ws);
        asm volatile("s_waitcnt vmcnt(7)\n\ts_barrier" ::: "memory");
        __builtin_amdgcn_s_setprio(2);
        ((u32*)(smem + OFF_RED))[r] = SENT;
        if (r < 16) ((u32*)(smem + OFF_CNT))[r] = 0u;
        asm volatile("s_waitcnt vmcnt(1)" : "+v"(skA), "+v"(skB), "+v"(tap_own), "+v"(tap_prev) :: "memory");
        BAR();
        STAMP(1);

        const u32 own_a = id * 8192 + r * 16, own_b = own_a + 4096;
        const u32 col_a = (r >> 1) * 8192 + (2 * id + (r & 1)) * 16, col_b = col_a + 128 * 8192;
        const u32 row_a = (r >> 1) * 8192 + id * 32 + (r & 1) * 16, row_b = row_a + 128 * 8192;
        char* const u_dst = smem + OFF_U + (r & 1) * U_STRIDE + 4080 - (r >> 1) * 16;
        const char* const y_src = smem + OFF_Y + (r & 1) * 4096 + (r >> 1) * 16;
        float* red = (float*)(smem + OFF_RED);

        const u32 flag_off = WS_FLAGS + (blockIdx.x & 31) * 1024 + lane * 16;
        u32x4 fl = {epoch, epoch, epoch, epoch};
        if (wave == 7) fl = poll_issue(rs, flag_off);

        Rows x, u0;
        {
            const u32 rp = (u32)(size_t)(LAS char*)(smem + OFF_R + ((lane & 15) >> 2) * R_ROW + (64 * w + 16 * (lane >> 4) + 4 * (lane & 3)) * 2);
            u32x2 kk[2][3], vv[2][2], qq[2][2];
            static_assert(4 * R_ROW == 4352, "offsets below");
            asm volatile("ds_read_b64_tr_b16 %0, %10\n\tds_read_b64_tr_b16 %1, %10 offset:4352\n\tds_read_b64_tr_b16 %2, %10 offset:8704\n\t"
                         "ds_read_b64_tr_b16 %3, %10 offset:13056\n\tds_read_b64_tr_b16 %4, %10 offset:17408\n\t"
                         "ds_read_b64_tr_b16 %5, %10 offset:512\n\tds_read_b64_tr_b16 %6, %10 offset:4864\n\tds_read_b64_tr_b16 %7, %10 offset:9216\n\t"
                         "ds_read_b64_tr_b16 %8, %10 offset:13568\n\tds_read_b64_tr_b16 %9, %10 offset:17920\n\ts_waitcnt lgkmcnt(0)"
                         : "=&v"(kk[0][0]), "=&v"(kk[0][1]), "=&v"(kk[0][2]), "=&v"(vv[0][0]), "=&v"(vv[0][1]),
                           "=&v"(kk[1][0]), "=&v"(kk[1][1]), "=&v"(kk[1][2]), "=&v"(vv[1][0]), "=&v"(vv[1][1]) : "v"(rp) : "memory");
            const u32 first = id ? 0xffffffffu : 0u;
            u32x4 o[2];
#pragma unroll
            for (int c = 0; c < 2; ++c) {
                const u32x2 sk = c ? skB : skA;
                const u32 kp = kk[c][0][0] & first, kq = kk[c][0][1] & first;
                const float kf[12] = {0.f, bf_hi(kp), bf_lo(kq), bf_hi(kq), bf_lo(kk[c][1][0]), bf_hi(kk[c][1][0]),
                                      bf_lo(kk[c][1][1]), bf_hi(kk[c][1][1]), bf_lo(kk[c][2][0]), bf_hi(kk[c][2][0]),
                                      bf_lo(kk[c][2][1]), bf_hi(kk[c][2][1])};
                float vf[8];
                unpack8((u32x4){vv[c][0][0], vv[c][0][1], vv[c][1][0], vv[c][1][1]}, vf);
                const float w0 = bf_lo(sk[0]), w1 = bf_hi(sk[0]), w2 = bf_lo(sk[1]), w3 = bf_hi(sk[1]);
                float kv[8];
#pragma unroll
                for (int j = 0; j < 8; ++j) kv[j] = fmaf(kf[j + 4], w0, fmaf(kf[j + 3], w1, fmaf(kf[j + 2], w2, kf[j + 1] * w3))) * vf[j];
                o[c] = pack8(kv);
            }
            publish(rs, WS_H + 0 * WS_HS + own_a, o[0]);
            publish(rs, WS_H + 0 * WS_HS + own_b, o[1]);
            asm volatile("ds_read_b64_tr_b16 %0, %4 offset:21760\n\tds_read_b64_tr_b16 %1, %4 offset:26112\n\t"
                         "ds_read_b64_tr_b16 %2, %4 offset:22272\n\tds_read_b64_tr_b16 %3, %4 offset:26624\n\ts_waitcnt lgkmcnt(0)"
                         : "=&v"(qq[0][0]), "=&v"(qq[0][1]), "=&v"(qq[1][0]), "=&v"(qq[1][1]) : "v"(rp) : "memory");
            unpack_rows((u32x4){qq[0][0][0], qq[0][0][1], qq[0][1][0], qq[0][1][1]}, x.a);
            unpack_rows((u32x4){qq[1][0][0], qq[1][0][1], qq[1][1][0], qq[1][1][1]}, x.b);
        }
        u64 t_s = __builtin_amdgcn_s_memrealtime();
        STAMP(2);
        u32 touch = 0;
        if (wave == 6) {
            if (lane < 4) touch = __builtin_amdgcn_raw_buffer_load_b32(rs, WS_H + (2 + lane) * WS_HS + id * 8192, 0, 16);
            else if (lane == 4) touch = ((const u32*)out)[id * 2048];
        }
        if (r == 0) {
            tap_prev = (u32x4){0, 0, 0, 0};
            tap_own[0] = (tap_own[0] & 0xffff0000u) | (pk_bf(bf_lo(tap_own[0]) + bf_hi(dA), 0.f) & 0xffffu);
        }
        stage_taps(smem, 1, r, tap_prev, tap_own);
        cnt_arrive(smem, 0, lane);
        if (wave == 7) {
            flags_finish(fl, rs, flag_off, epoch);
            if (blockIdx.x == 0 && lane == 0) __hip_atomic_store((GAS u32*)(ws + WS_EPOCH), epoch, RLX_AGENT);
            cnt_arrive(smem, 4, lane);
        }
        cnt_wait(smem, 4, 1);
        STAMP(3);

        wait_until(t_s + POLL_DELAY);
        Piece2 pc = poll2_issue(rs, WS_H + col_a, WS_H + col_b), pr;
        Rows yA, u;
        auto stage_input = [&](u32 h) {
            poll2_finish(pc, desc, WS_H + h * WS_HS + col_a, WS_H + h * WS_HS + col_b);
            *(u32x4*)u_dst = reverse8(pc.a);
            *(u32x4*)(u_dst - 2048) = reverse8(pc.b);
        };
        auto send_result = [&](u32 h) {
            publish(rs, WS_H + h * WS_HS + own_a, *(const u32x4*)y_src);
            publish(rs, WS_H + h * WS_HS + own_b, *(const u32x4*)(y_src + 2048));
            t_s = __builtin_amdgcn_s_memrealtime();
        };
        auto fetch_rows = [&](u32 h) {
            pr = poll2_issue(rs, WS_H + h * WS_HS + row_a, WS_H + h * WS_HS + row_b);
            poll2_finish(pr, desc, WS_H + h * WS_HS + row_a, WS_H + h * WS_HS + row_b);
        };
#pragma nounroll
        for (u32 it = 0; it < 2; ++it) {
            layer_norm(x, u0, red + 128 * it, lane, w);
            u32x4 tix0, tix1;
            if (it == 0) {
                stage_input(0);
                STAMP(21);
                asm volatile("" :: "v"(touch));
                asm volatile("" :: "v"(code_pf));
                BAR();
                STAMP(5);
                if (wave == 7 && lane < 32) __hip_atomic_store((GAS u32*)(ws + WS_FLAGS + 32768) + lane * 256 + id, epoch, RLX_AGENT);
                conv_row(smem, wave, lane, rs, id, 2, 27, [&] {
                    __builtin_amdgcn_s_setprio(0);
                    KState ks;
                    kgen_init(ks);
                    kgen_steps<R_STEPS, false>(kgen_params(smem, wave, 4 - R_STEPS), lane, ks);
                    kgen_save(smem, wave, lane, ks);
                    cnt_arrive(smem, 1, lane);
                    __builtin_amdgcn_s_setprio(2);
                    if (wave == 7) fl = poll_issue(rs, flag_off + 32768);
                });
                send_result(1);
                STAMP(6);
                tix0 = ((const u32x4*)tap_index.off)[r]; tix1 = ((const u32x4*)tap_index.off)[r + 1];
                if (wave == 7) {
                    flags_finish(fl, rs, flag_off + 32768, epoch);
                    cnt_arrive(smem, 5, lane);
                }
                wait_until(t_s + POLL_DELAY);
                cnt_wait(smem, 5, 1);
                fetch_rows(1);
                STAMP(8);
                unpack_rows(pr.a, yA.a);
                unpack_rows(pr.b, yA.b);
            }
#pragma unroll
            for (int i = 0; i < 4; ++i) { x.a[i] += u0.a[i] * yA.a[i]; x.b[i] += u0.b[i] * yA.b[i]; }
            round_rows(x.a);
            round_rows(x.b);
            layer_norm(x, u, red + 128 * it + 64, lane, w);
            publish(rs, WS_H + (2 + 2 * it) * WS_HS + own_a, pack_rows(u.a));
            publish(rs, WS_H + (2 + 2 * it) * WS_HS + own_b, pack_rows(u.b));
            t_s = __builtin_amdgcn_s_memrealtime();
            STAMP(it ? 13 : 9);

            wait_until(t_s + POLL_DELAY);
            pc = poll2_issue(rs, WS_H + (2 + 2 * it) * WS_HS + col_a, WS_H + (2 + 2 * it) * WS_HS + col_b);
            if (it == 0) {
                cnt_wait(smem, 2, 4);
                s4_taps(smem, 1, r, tix0, tix1);
                cnt_arrive(smem, 3, lane);
            }
            stage_input(2 + 2 * it);
            BAR();
            STAMP(it ? 14 : 10);
            conv_row(smem, wave, lane, rs, id, it ? -1 : 4, 28, [] {});
            send_result(3 + 2 * it);
            STAMP(it ? 15 : 11);

            wait_until(t_s + POLL_DELAY);
            fetch_rows(3 + 2 * it);
            STAMP(it ? 16 : 12);
            Rows y;
            unpack_rows(pr.a, y.a);
            unpack_rows(pr.b, y.b);
#pragma unroll
            for (int i = 0; i < 4; ++i) { x.a[i] += y.a[i]; x.b[i] += y.b[i]; }
            pr.a = round_rows(x.a);
            pr.b = round_rows(x.b);
        }
        {
            u16* dst = out + (8 * id) * DM + r;
#pragma unroll
            for (int j = 0; j < 4; ++j) {
                dst[(2 * j) * DM] = (u16)pr.a[j];
                dst[(2 * j) * DM + 256] = (u16)pr.b[j];
                dst[(2 * j + 1) * DM] = (u16)(pr.a[j] >> 16);
                dst[(2 * j + 1) * DM + 256] = (u16)(pr.b[j] >> 16);
            }
        }
        STAMP(17);
    }
    asm volatile("h3s4_text_b:");
    asm volatile("s_waitcnt vmcnt(0)" ::: "memory");
    STAMP(18);
    BAR();
    if (tid == 0) {
        const u64* st = (const u64*)(smem + OFF_ST);
        const u64 dur = st[DIAG_B] - st[DIAG_A];
        const u64 until = __builtin_amdgcn_s_memrealtime() + DIAG * dur;
        while (__builtin_amdgcn_s_memrealtime() < until) __builtin_amdgcn_s_sleep(2);
    }
}

extern "C" void kernel_launch(void* const* d_in, const int* in_sizes, int n_in,
                              void* d_out, int out_size, void* d_ws, size_t ws_size,
                              hipStream_t stream) {
    h3s4_kernel<<<256, 512, 0, stream>>>(
        (const char*)d_in[0], (const char*)d_in[1], (const char*)d_in[2], (const u32x2*)d_in[3], (const u32x4*)d_in[4],
        (const u32*)d_in[5], (const u16*)d_in[7], (const u16*)d_in[8], (const u16*)d_in[9], (const u16*)d_in[10],
        (const u16*)d_in[11], (const u16*)d_in[12], (const u16*)d_in[13], (const u32*)d_in[14], (u16*)d_out, (char*)d_ws
        );
}
```

```cpp
#include <hip/hip_runtime.h>
#include <hip/hip_bf16.h>
#include <stdint.h>

typedef unsigned int u32;
typedef unsigned short u16;
typedef unsigned long long u64;
typedef __attribute__((ext_vector_type(4))) float f32x4;
typedef __attribute__((ext_vector_type(2))) float f32x2;
typedef __attribute__((ext_vector_type(4))) u32 u32x4;
typedef __attribute__((ext_vector_type(2))) u32 u32x2;
typedef __attribute__((ext_vector_type(4))) short s16x4;
typedef __attribute__((ext_vector_type(8))) __bf16 bf16x8;
typedef __attribute__((ext_vector_type(2))) __bf16 bf16x2;
#define LAS __attribute__((address_space(3)))
#define GAS __attribute__((address_space(1)))
#define RLX_AGENT __ATOMIC_RELAXED, __HIP_MEMORY_SCOPE_AGENT
#define SB() __builtin_amdgcn_sched_barrier(0)
#define BAR() asm volatile("s_waitcnt lgkmcnt(0)\n\ts_barrier" ::: "memory")
#define MEMFENCE() asm volatile("" ::: "memory")
#define WAIT_VM(n) do { MEMFENCE(); __builtin_amdgcn_s_waitcnt(0x0F70 | ((n) & 15) | (((n) >> 4) << 14)); MEMFENCE(); } while (0)

constexpr int DM = 512;
constexpr int NS = 128;
constexpr u32 SENT = 0xffffffffu;
constexpr u32 SPIN_CAP = 20000;
#define FILL_GAP 70
#define R_STEPS 1
#define POLL_DELAY 55

constexpr u32 WS_EPOCH = 0;
constexpr u32 WS_FLAGS = 4096;
constexpr u32 WS_H = 1u << 20;
constexpr u32 WS_HS = 2u << 20;
constexpr u32 WS_USED = WS_H + 6 * WS_HS;

constexpr int T_COPY = 4160, T_CH = 8 * T_COPY;
constexpr int U_STRIDE = 4672;
constexpr int R_ROW = 1088;
constexpr int KP_WAVE = 2816;
constexpr int OFF_T = 0;
constexpr int OFF_U = OFF_T + 2 * T_CH;
constexpr int OFF_X = OFF_U + 2 * U_STRIDE;
constexpr int OFF_Y = OFF_X + 16384;
constexpr int OFF_R = OFF_Y + 8192;
constexpr int OFF_P = OFF_R + 28 * R_ROW;
constexpr int OFF_KP = OFF_P + 4096;
constexpr int OFF_KC = OFF_KP + 8 * KP_WAVE;
constexpr int OFF_RED = OFF_KC + 2 * 648 * 2;
constexpr int OFF_CNT = OFF_RED + 1024;
constexpr int OFF_ST = OFF_CNT + 64;
constexpr int LDS_BYTES = OFF_ST + 512;
static_assert(LDS_BYTES <= 163840, "LDS");

#define STAMP(i)

__device__ __forceinline__ float bf_lo(u32 p) { return __builtin_bit_cast(float, p << 16); }
__device__ __forceinline__ float bf_hi(u32 p) { return __builtin_bit_cast(float, p & 0xffff0000u); }
__device__ __forceinline__ float bf_u16(u16 p) { return __builtin_bit_cast(float, (u32)p << 16); }
__device__ __forceinline__ u32 pk_bf(float lo, float hi) {
    bf16x2 v = {(__bf16)lo, (__bf16)hi};
    return __builtin_bit_cast(u32, v);
}
__device__ __forceinline__ float as_f(u32 x) { return __builtin_bit_cast(float, x); }
__device__ __forceinline__ u32 as_u(float x) { return __builtin_bit_cast(u32, x); }
__device__ __forceinline__ float rbf(float x) { return bf_lo(pk_bf(x, x)); }
__device__ __forceinline__ void unpack8(u32x4 p, float (&f)[8]) {
#pragma unroll
    for (int i = 0; i < 4; ++i) { f[2 * i] = bf_lo(p[i]); f[2 * i + 1] = bf_hi(p[i]); }
}
__device__ __forceinline__ u32x4 pack8(const float (&f)[8]) {
    return (u32x4){pk_bf(f[0], f[1]), pk_bf(f[2], f[3]), pk_bf(f[4], f[5]), pk_bf(f[6], f[7])};
}
__device__ __forceinline__ u32x4 reverse8(u32x4 p) {
    return (u32x4){__builtin_amdgcn_alignbit(p[3], p[3], 16), __builtin_amdgcn_alignbit(p[2], p[2], 16),
                   __builtin_amdgcn_alignbit(p[1], p[1], 16), __builtin_amdgcn_alignbit(p[0], p[0], 16)};
}
template <int CTRL> __device__ __forceinline__ float dppf(float v) {
    return __builtin_bit_cast(float, __builtin_amdgcn_update_dpp(0, __builtin_bit_cast(int, v), CTRL, 0xf, 0xf, true));
}

__device__ __forceinline__ void lds_dma16(const char* src, u32 lds) {
    u32 keep;
    asm volatile("s_mov_b32 %0, m0\n\ts_mov_b32 m0, %2\n\ts_nop 0\n\tglobal_load_lds_dwordx4 %1, off\n\ts_mov_b32 m0, %0"
                 : "=&s"(keep) : "v"(src), "s"(lds) : "memory");
}

__device__ __forceinline__ u32 ld_u16(const void* base, u32 off) {
    u32 r;
    asm volatile("global_load_ushort %0, %1, %2" : "=v"(r) : "v"(off), "s"(base) : "memory");
    return r;
}
__device__ __forceinline__ u32x2 ld_b64(const void* base, u32 off) {
    u32x2 r;
    asm volatile("global_load_dwordx2 %0, %1, %2" : "=v"(r) : "v"(off), "s"(base) : "memory");
    return r;
}
__device__ __forceinline__ u32x4 ld_b128(const void* base, u32 off) {
    u32x4 r;
    asm volatile("global_load_dwordx4 %0, %1, %2" : "=v"(r) : "v"(off), "s"(base) : "memory");
    return r;
}

typedef __amdgpu_buffer_rsrc_t rsrc_t;
__device__ __forceinline__ u32x4 ld_dev(rsrc_t rs, u32 off) { return __builtin_amdgcn_raw_buffer_load_b128(rs, off, 0, 16); }
__device__ __forceinline__ void st_dev(rsrc_t rs, u32 off, u32x4 v) { __builtin_amdgcn_raw_buffer_store_b128(v, rs, off, 0, 16); }
__device__ __forceinline__ void publish(rsrc_t rs, u32 off, u32x4 v) {
#pragma unroll
    for (int i = 0; i < 4; ++i) v[i] = v[i] < SENT - 1u ? v[i] : SENT - 1u;
    st_dev(rs, off, v);
}
__device__ __forceinline__ u32 launch_tag(const char* ws) {
    u32 e;
    asm volatile("s_load_dword %0, %1, 0x0\n\ts_waitcnt lgkmcnt(0)" : "=&s"(e) : "s"(ws + WS_EPOCH) : "memory");
    return e + 1u;
}
__device__ __forceinline__ void wait_until(u64 t) {
    while (__builtin_amdgcn_s_memrealtime() < t) __builtin_amdgcn_s_sleep(1);
}
__device__ __forceinline__ u32x4 poll_issue(rsrc_t rs, u32 off) {
    MEMFENCE();
    u32x4 x = ld_dev(rs, off);
    MEMFENCE();
    return x;
}
struct Piece2 { u32x4 a, b; };
__device__ __forceinline__ Piece2 poll2_issue(rsrc_t rs, u32 off_a, u32 off_b) {
    MEMFENCE();
    Piece2 p = {ld_dev(rs, off_a), ld_dev(rs, off_b)};
    MEMFENCE();
    return p;
}
__device__ __forceinline__ bool has_sentinel(const u32x4& x) {
    u32 mx = x[0] > x[1] ? x[0] : x[1];
    mx = mx > x[2] ? mx : x[2];
    mx = mx > x[3] ? mx : x[3];
    return mx == SENT;
}
__device__ __forceinline__ void poll2_finish(Piece2& p, u32x4 desc, u32 off_a, u32 off_b) {
#pragma nounroll
    for (u32 spins = 0; spins < SPIN_CAP; ++spins) {
        const bool bad = has_sentinel(p.a) || has_sentinel(p.b);
        if (__builtin_amdgcn_ballot_w64(bad) == 0ull) break;
        __builtin_amdgcn_s_sleep(1);
        if (bad) asm volatile("buffer_load_dwordx4 %0, %2, %4, 0 offen sc1\n\tbuffer_load_dwordx4 %1, %3, %4, 0 offen sc1\n\ts_waitcnt vmcnt(0)"
                              : "+v"(p.a), "+v"(p.b) : "v"(off_a), "v"(off_b), "s"(desc) : "memory");
    }
}
__device__ __forceinline__ u32 ld_flag(const char* ws, u32 off) { return __hip_atomic_load((const GAS u32*)(ws + off), RLX_AGENT); }
__device__ __forceinline__ void poll2_finish_flagged(Piece2& p, u32x4 desc, u32 off_a, u32 off_b, u32 fo_a, u32 fo_b, bool ok, u32 epoch) {
#pragma nounroll
    for (u32 spins = 0; spins < SPIN_CAP; ++spins) {
        const bool bad = !ok || has_sentinel(p.a) || has_sentinel(p.b);
        if (__builtin_amdgcn_ballot_w64(bad) == 0ull) break;
        if (!ok) {
            u32 fa, fb;
            asm volatile("buffer_load_dword %0, %2, %4, 0 offen sc1\n\tbuffer_load_dword %1, %3, %4, 0 offen sc1\n\ts_waitcnt vmcnt(0)"
                         : "=&v"(fa), "=&v"(fb) : "v"(fo_a), "v"(fo_b), "s"(desc) : "memory");
            ok = fa == epoch && fb == epoch;
        }
        if (bad) asm volatile("buffer_load_dwordx4 %0, %2, %4, 0 offen sc1\n\tbuffer_load_dwordx4 %1, %3, %4, 0 offen sc1\n\ts_waitcnt vmcnt(0)"
                              : "+v"(p.a), "+v"(p.b) : "v"(off_a), "v"(off_b), "s"(desc) : "memory");
    }
}

__device__ __forceinline__ void cnt_arrive(char* smem, int k, int lane) {
    asm volatile("s_waitcnt lgkmcnt(0)" ::: "memory");
    if (lane == 0) __hip_atomic_fetch_add((LAS u32*)(smem + OFF_CNT) + k, 1u, __ATOMIC_RELAXED, __HIP_MEMORY_SCOPE_WORKGROUP);
}
__device__ __forceinline__ void cnt_wait(char* smem, int k, u32 target) {
    u32 spins = 0;
    while (__hip_atomic_load((LAS u32*)(smem + OFF_CNT) + k, __ATOMIC_RELAXED, __HIP_MEMORY_SCOPE_WORKGROUP) < target) {
        if (++spins > 50 * SPIN_CAP) break;
        __builtin_amdgcn_s_sleep(1);
    }
    MEMFENCE();
}

struct Rows { f32x2 a[4], b[4]; };
__device__ __forceinline__ void unpack_rows(u32x4 p, f32x2 (&f)[4]) {
#pragma unroll
    for (int i = 0; i < 4; ++i) f[i] = (f32x2){bf_lo(p[i]), bf_hi(p[i])};
}
__device__ __forceinline__ u32x4 pack_rows(const f32x2 (&f)[4]) {
    return (u32x4){pk_bf(f[0][0], f[0][1]), pk_bf(f[1][0], f[1][1]), pk_bf(f[2][0], f[2][1]), pk_bf(f[3][0], f[3][1])};
}
__device__ __forceinline__ u32x4 round_rows(f32x2 (&f)[4]) {
    u32x4 p = pack_rows(f);
    asm("" : "+v"(p));
    unpack_rows(p, f);
    return p;
}
__device__ __forceinline__ void layer_norm(const Rows& x, Rows& u, float* red, int lane, int w) {
#pragma clang fp contract(fast)
    u32 a[8], b[8];
#pragma unroll
    for (int i = 0; i < 4; ++i) {
        const f32x2 s1 = x.a[i] + x.b[i], s2 = x.a[i] * x.a[i] + x.b[i] * x.b[i];
        a[2 * i] = as_u(s1[0]); a[2 * i + 1] = as_u(s1[1]);
        b[2 * i] = as_u(s2[0]); b[2 * i + 1] = as_u(s2[1]);
    }
    SB();
#pragma unroll
    for (int j = 0; j < 8; ++j) {
        const auto s = __builtin_amdgcn_permlane32_swap(a[j], b[j], false, false);
        a[j] = s[0]; b[j] = s[1];
    }
    SB();
    float r[8];
#pragma unroll
    for (int j = 0; j < 8; ++j) r[j] = as_f(a[j]) + as_f(b[j]);
    SB();
#pragma unroll
    for (int j = 0; j < 4; ++j) {
        const auto s = __builtin_amdgcn_permlane16_swap(as_u(r[j]), as_u(r[j + 4]), false, false);
        a[j] = s[0]; b[j] = s[1];
    }
    SB();
    float s4[4];
#pragma unroll
    for (int j = 0; j < 4; ++j) s4[j] = as_f(a[j]) + as_f(b[j]);
    SB();
#pragma unroll
    for (int j = 0; j < 4; ++j) s4[j] += dppf<0x128>(s4[j]);
    const float t0 = (lane & 8) ? s4[2] : s4[0], t1 = (lane & 8) ? s4[3] : s4[1];
    const float c0 = t0 + dppf<0x141>(t0), c1 = t1 + dppf<0x141>(t1);
    float s = (lane & 4) ? c1 : c0;
    s += dppf<0xB1>(s);
    s += dppf<0x4E>(s);
    const u32 sb = as_u(s) < SENT - 1u ? as_u(s) : SENT - 1u;
    if ((lane & 3) == 0) ((volatile LAS u32*)(LAS char*)(char*)red)[(lane >> 2) * 4 + w] = sb;
    u32x4 p;
    u32 spins = 0;
    while (true) {
        p = *(volatile LAS u32x4*)((LAS char*)(char*)red + (lane & 15) * 16);
        u32 mx = p[0] > p[1] ? p[0] : p[1];
        mx = mx > p[2] ? mx : p[2];
        mx = mx > p[3] ? mx : p[3];
        if (__builtin_amdgcn_ballot_w64(mx == SENT) == 0ull) break;
        if (++spins > 50 * SPIN_CAP) break;
    }
    const float m = ((as_f(p[0]) + as_f(p[1])) + (as_f(p[2]) + as_f(p[3]))) * (1.0f / DM);
    const float ex2 = dppf<0x128>(m);
    const float rstd = __builtin_amdgcn_rsqf(fmaxf(fmaf(-m, m, ex2), 0.f) + 1e-5f);
    const float off = -m * rstd;
    const f32x2 R[4] = {{dppf<0x150>(rstd), dppf<0x151>(rstd)}, {dppf<0x152>(rstd), dppf<0x153>(rstd)},
                        {dppf<0x154>(rstd), dppf<0x155>(rstd)}, {dppf<0x156>(rstd), dppf<0x157>(rstd)}};
    const f32x2 O[4] = {{dppf<0x150>(off), dppf<0x151>(off)}, {dppf<0x152>(off), dppf<0x153>(off)},
                        {dppf<0x154>(off), dppf<0x155>(off)}, {dppf<0x156>(off), dppf<0x157>(off)}};
#pragma unroll
    for (int i = 0; i < 4; ++i) { u.a[i] = x.a[i] * R[i] + O[i]; u.b[i] = x.b[i] * R[i] + O[i]; }
}

__device__ __forceinline__ void stage_taps(char* smem, int ch, int i, u32x4 prev, u32x4 own) {
    const u32 D[8] = {prev[0], prev[1], prev[2], prev[3], own[0], own[1], own[2], own[3]};
    u32 O[7];
#pragma unroll
    for (int y = 0; y < 7; ++y) O[y] = __builtin_amdgcn_alignbit(D[y + 1], D[y], 16);
    char* base = smem + OFF_T + ch * T_CH + (i + 1) * 16;
#pragma unroll
    for (int c = 0; c < 8; ++c) {
        const int y = (c + 1) >> 1;
        const u32x4 v = (c & 1) ? (u32x4){D[y], D[y + 1], D[y + 2], D[y + 3]} : (u32x4){O[y], O[y + 1], O[y + 2], O[y + 3]};
        *(u32x4*)(base + c * T_COPY) = v;
    }
}

__device__ __forceinline__ f32x4 mfma16(u32x4 a, u32x4 b, f32x4 c) {
    return __builtin_amdgcn_mfma_f32_16x16x32_bf16(__builtin_bit_cast(bf16x8, a), __builtin_bit_cast(bf16x8, b), c, 0, 0, 0);
}
struct KState { f32x4 acc[3]; };
__device__ __forceinline__ void kgen_init(KState& ks) {
#pragma unroll
    for (int e = 0; e < 3; ++e) ks.acc[e] = (f32x4){0, 0, 0, 0};
}
#define OPQ(x) asm volatile("" : "+v"(x))
template <int NS_, bool PK>
__device__ __forceinline__ void kgen_steps(const f32x4* P, int lane, KState& ks) {
#pragma clang fp contract(fast)
    const int g = lane >> 4, m = lane & 15;
    const float lo = (float)(8 * m);
    const float hi[3] = {(float)(128 * (m >> 3) + (m & 7)),
                         (float)(m < 8 ? 256 + 128 * (m >> 2) + 2 * (m & 3) : 512 + 128 * ((m - 8) >> 1) + 4 * (m & 1)),
                         (float)(m < 8 ? 1024 + 128 * m : 2048)};
    auto eval = [&](f32x2 ex, f32x2 an, bool conj, u32& o0, u32& o1) {
        if (!PK) { OPQ(ex[0]); OPQ(ex[1]); OPQ(an[0]); OPQ(an[1]); }
        const float m0 = __builtin_amdgcn_exp2f(ex[0]), m1 = __builtin_amdgcn_exp2f(ex[1]);
        const float f0 = __builtin_amdgcn_fractf(an[0]), f1 = __builtin_amdgcn_fractf(an[1]);
        const float c0 = __builtin_amdgcn_cosf(f0), c1 = __builtin_amdgcn_cosf(f1);
        const float s0 = __builtin_amdgcn_sinf(f0), s1 = __builtin_amdgcn_sinf(f1);
        float r0, r1, i0, i1;
        if (PK) {
            const f32x2 r = (f32x2){c0, c1} * (f32x2){m0, m1}, i = (f32x2){s0, s1} * (f32x2){m0, m1};
            r0 = r[0]; r1 = r[1]; i0 = i[0]; i1 = i[1];
        } else {
            r0 = c0 * m0; OPQ(r0); i0 = s0 * m0; OPQ(i0); r1 = c1 * m1; OPQ(r1); i1 = s1 * m1; OPQ(i1);
        }
        o0 = pk_bf(r0, conj ? -i0 : i0);
        o1 = pk_bf(r1, conj ? -i1 : i1);
    };
    auto affine = [&](f32x2 a, float p, f32x2 b) -> f32x2 {
        if (PK) return a * p + b;
        float x = fmaf(a[0], p, b[0]), y = fmaf(a[1], p, b[1]);
        OPQ(x); OPQ(y);
        return (f32x2){x, y};
    };
#pragma unroll
    for (int s = 0; s < NS_; ++s) {
        u32x4 B, A[3];
#pragma unroll
        for (int h = 0; h < 2; ++h) {
            const f32x4 p0 = P[16 * s + 4 * g + 2 * h], p1 = P[16 * s + 4 * g + 2 * h + 1];
            u32 o0, o1;
            eval(affine(p0.xy, lo, (f32x2){0.f, 0.f}), affine(p0.zw, lo, (f32x2){0.f, 0.f}), true, o0, o1);
            B[2 * h] = o0; B[2 * h + 1] = o1;
#pragma unroll
            for (int e = 0; e < 3; ++e) {
                eval(affine(p0.xy, hi[e], p1.xy), affine(p0.zw, hi[e], p1.zw), false, o0, o1);
                A[e][2 * h] = o0; A[e][2 * h + 1] = o1;
            }
        }
#pragma unroll
        for (int e = 0; e < 3; ++e) ks.acc[e] = mfma16(A[e], B, ks.acc[e]);
    }
}
__device__ __forceinline__ const f32x4* kgen_params(char* smem, int wave, int step) {
    return (const f32x4*)(smem + OFF_P) + ((wave >> 1) & 1) * 128 + ((wave & 1) * 4 + step) * 16;
}
__device__ __forceinline__ void kgen_save(char* smem, int wave, int lane, const KState& ks) {
    char* p = smem + OFF_KP + wave * KP_WAVE + lane * 16;
    *(f32x4*)p = ks.acc[0];
    *(f32x4*)(p + 1024) = ks.acc[1];
    if (lane < 48) *(f32x4*)(p + 2048) = ks.acc[2];
}
__device__ __forceinline__ f32x4 kgen_sum(const char* p) {
    return (*(const f32x4*)p + *(const f32x4*)(p + KP_WAVE)) + (*(const f32x4*)(p + 4 * KP_WAVE) + *(const f32x4*)(p + 5 * KP_WAVE));
}
__device__ __forceinline__ void kgen_reduce(char* smem, int wave, int lane, u32 dskip) {
    const int c = wave >> 1, j = lane & 15, g = lane >> 4;
    char* KC = smem + OFF_KC + c * 1296;
    const char* p = smem + OFF_KP + 2 * c * KP_WAVE + lane * 16;
    if ((wave & 1) == 0) {
        f32x4 s = kgen_sum(p);
        const f32x4 t = kgen_sum(smem + OFF_KP + 2 * c * KP_WAVE + 2048 + (lane < 48 ? lane : 0) * 16);
        if (lane == 0) s[0] += c ? bf_hi(dskip) : bf_lo(dskip);
        *(u32x2*)(KC + 2 * (128 * (g >> 1) + 4 * (g & 1) + 8 * j)) = (u32x2){pk_bf(s[0], s[1]), pk_bf(s[2], s[3])};
        const u32 lo = pk_bf(t[0], t[1]), hi = pk_bf(t[2], t[3]);
        u16* o = (u16*)KC + 512 + 64 * g + j;
        if (g < 2) { o[0] = (u16)lo; o[16] = (u16)(lo >> 16); o[32] = (u16)hi; o[48] = (u16)(hi >> 16); }
        else if (lane == 32) { ((u16*)KC)[640] = (u16)lo; ((u16*)KC)[641] = 0; }
    } else {
        const f32x4 s = kgen_sum(p + 1024);
        const u32 lo = pk_bf(s[0], s[1]), hi = pk_bf(s[2], s[3]);
        if (g < 2) *(u32x2*)(KC + 2 * (256 + 64 * g + 4 * j)) = (u32x2){lo, hi};
        else {
            u32* o = (u32*)(KC + 2 * (384 + 64 * (g - 2) + 2 * j));
            o[0] = lo; o[16] = hi;
        }
    }
}

struct TapIndex {
    u16 off[2048 + 8];
    constexpr TapIndex() : off() {
        for (int i = 0; i < 8; ++i) off[i] = 641 * 2;
        for (int l = 0; l < 2048; ++l) {
            int e = 0;
            while ((l >> e) >= 256) ++e;
            int v = l >> e;
            if (e > 0) {
                const int rem = l & ((1 << e) - 1), half = 1 << (e - 1);
                if (rem > half || (rem == half && (v & 1))) ++v;
            }
            off[8 + l] = (u16)(2 * (128 * e + v));
        }
    }
};
__device__ const TapIndex tap_index = TapIndex();

__device__ __forceinline__ void s4_taps(char* smem, int ch, int i, u32x4 tix0, u32x4 tix1) {
    const char* kc = smem + OFF_KC + ch * 1296;
    u32 d[8];
#pragma unroll
    for (int j = 0; j < 8; ++j) {
        const u32 o = j < 4 ? tix0[j] : tix1[j - 4];
        d[j] = (u32)*(const u16*)(kc + (o & 0xffffu)) | ((u32)*(const u16*)(kc + (o >> 16)) << 16);
    }
    stage_taps(smem, ch, i, (u32x4){d[0], d[1], d[2], d[3]}, (u32x4){d[4], d[5], d[6], d[7]});
}

struct Frags { u32x4 A[8], B[8]; };
template <bool INIT> __device__ __forceinline__ void mma(f32x4& acc, const u32x4& a, const u32x4& b) {
    if constexpr (INIT) asm volatile("v_mfma_f32_16x16x32_bf16 %0, %1, %2, 0" : "=&v"(acc) : "v"(a), "v"(b));
    else asm volatile("v_mfma_f32_16x16x32_bf16 %0, %1, %2, %0" : "+v"(acc) : "v"(a), "v"(b));
}
template <int I> __device__ __forceinline__ void load_one(Frags& f, const char* ap, const char* bp) {
    if constexpr (I == 0) f.A[0] = *(const u32x4*)ap;
    else if constexpr (I <= 8) f.B[I - 1] = *(const u32x4*)(bp + 512 * (8 - I));
    else f.A[I - 8] = *(const u32x4*)(ap + 512 * (I - 8));
}
template <int I> __device__ __forceinline__ void load_all(Frags& f, const char* ap, const char* bp) {
    if constexpr (I < 16) { load_one<I>(f, ap, bp); SB(); load_all<I + 1>(f, ap, bp); }
}
template <int T0, int T1, int T2, int T3> struct TileSet {
    static constexpr int tile(int i) { return i == 0 ? T0 : i == 1 ? T1 : i == 2 ? T2 : T3; }
    static constexpr int walk(int I, bool want_tile) {
        int n = 0;
        for (int a = 0; a < 8; ++a)
            for (int j = 0; j < 4; ++j)
                if (tile(j) >= a) { if (n == I) return want_tile ? tile(j) : a; ++n; }
        return -1;
    }
};
constexpr int tri_a(int I) { int a = 0; while (I >= 8 - a) { I -= 8 - a; ++a; } return a; }
constexpr int tri_b(int I) { int a = 0; while (I >= 8 - a) { I -= 8 - a; ++a; } return I; }
template <int I, bool FIRST> __device__ __forceinline__ void class_step(const Frags& cur, Frags& nxt, f32x4 (&acc)[8], const char* ap, const char* bp) {
    if constexpr (I < 36) {
        constexpr int a = tri_a(I), b = tri_b(I);
        mma<FIRST && a == 0>(acc[a + b], cur.A[a], cur.B[b]);
        if constexpr ((I & 1) == 1 && (I >> 1) < 16) load_one<(I >> 1)>(nxt, ap, bp);
        SB();
        class_step<I + 1, FIRST>(cur, nxt, acc, ap, bp);
    }
}
template <class TS, int I, int TO, int PRE> __device__ __forceinline__ void set_step(const Frags& cur, Frags& nxt, f32x4 (&acc)[8], const char* ap, const char* bp) {
    if constexpr (I < TO) {
        constexpr int t = TS::walk(I, true), a = TS::walk(I, false);
        mma<false>(acc[t], cur.A[a], cur.B[t - a]);
        if constexpr (PRE == 1 && (I * 11) / 18 != ((I + 1) * 11) / 18) load_one<(I * 11) / 18>(nxt, ap, bp);
        if constexpr (PRE == 2 && I < 5) load_one<11 + I>(nxt, ap, bp);
        SB();
        set_step<TS, I + 1, TO, PRE>(cur, nxt, acc, ap, bp);
    }
}
template <class TS, int J> __device__ __forceinline__ void tile_step(const Frags& cur, f32x4 (&acc)[8]) {
    constexpr int t = TS::tile(J);
#pragma unroll
    for (int a = 0; a <= t; ++a) { mma<false>(acc[t], cur.A[a], cur.B[t - a]); SB(); }
}
template <class TS, int J> __device__ __forceinline__ void tile_out(char* Y, f32x4 (&acc)[8], const f32x4 (&o)[4]) {
    constexpr int t = TS::tile(J);
    const f32x4 s = acc[t] + o[J];
    const u32x2 pk = {pk_bf(s[0], s[1]), pk_bf(s[2], s[3])};
    *(u32x2*)(Y + 512 * t) = pk;
    SB();
}
template <class SEND, class KEEP>
__device__ __forceinline__ void conv_half(char* smem, const u32x4 (&A0)[8], const char* ap, const char* bp, int wave, int lane, int ch, u32 seq) {
    f32x4 acc[8];
    Frags f0, f1;
#pragma unroll
    for (int a = 0; a < 8; ++a) f0.A[a] = A0[a];
#pragma unroll
    for (int b = 0; b < 8; ++b) { f0.B[b] = *(const u32x4*)(bp + 512 * (7 - b)); SB(); }
    class_step<0, true>(f0, f1, acc, ap + 64, bp + 64);
    class_step<0, false>(f1, f0, acc, ap + 128, bp + 128);
    set_step<SEND, 0, 18, 1>(f0, f1, acc, ap + 192, bp + 192);
    set_step<SEND, 0, 18, 2>(f1, f1, acc, ap + 192, bp + 192);
    set_step<KEEP, 0, 4, 0>(f0, f1, acc, ap, bp);
    asm volatile("s_nop 3" : "+v"(acc[SEND::tile(0)]), "+v"(acc[SEND::tile(1)]), "+v"(acc[SEND::tile(2)]), "+v"(acc[SEND::tile(3)]));
    f32x4* X = (f32x4*)(smem + OFF_X);
#pragma unroll
    for (int i = 0; i < 4; ++i) X[(wave * 4 + i) * 64 + lane] = acc[SEND::tile(i)];
    volatile LAS u32* xf = (volatile LAS u32*)(LAS char*)(smem + OFF_CNT + 32);
    xf[wave] = seq;
    SB();
    set_step<KEEP, 4, 14, 0>(f0, f1, acc, ap, bp);
    u32 seen = xf[wave ^ 1];
    SB();
    set_step<KEEP, 14, 18, 0>(f0, f1, acc, ap, bp);
    for (u32 spins = 0; seen < seq && spins < 50 * SPIN_CAP; ++spins) seen = xf[wave ^ 1];
    SB();
    f32x4 o[4];
#pragma unroll
    for (int i = 0; i < 4; ++i) o[i] = X[((wave ^ 1) * 4 + i) * 64 + lane];
    SB();
    char* Y = smem + OFF_Y + ch * 4096 + (16 * (lane & 15) + 4 * (lane >> 4)) * 2;
    tile_step<KEEP, 0>(f1, acc);
    tile_step<KEEP, 1>(f1, acc);
    tile_step<KEEP, 2>(f1, acc);
    asm volatile("s_nop 3" : "+v"(acc[KEEP::tile(0)]), "+v"(acc[KEEP::tile(1)]));
    tile_out<KEEP, 0>(Y, acc, o);
    tile_out<KEEP, 1>(Y, acc, o);
    tile_step<KEEP, 3>(f1, acc);
    asm volatile("" : "+v"(acc[KEEP::tile(2)]));
    tile_out<KEEP, 2>(Y, acc, o);
    asm volatile("s_nop 7" : "+v"(acc[KEEP::tile(3)]));
    tile_out<KEEP, 3>(Y, acc, o);
}
struct ConvAddr { const char* ap; const char* bp; };
__device__ __forceinline__ ConvAddr conv_addr(char* smem, int wave, int lane) {
    const int ch = (wave >> 1) & 1, hf = wave & 1;
    const int m = lane & 15, g = lane >> 4;
    ConvAddr c;
    c.ap = smem + OFF_T + ch * T_CH + (m & 7) * T_COPY + 16 * (m >> 3) + 16 * g + 256 * hf;
    c.bp = smem + OFF_U + ch * U_STRIDE + 4064 - 32 * m + 16 * g + 256 * hf - 512 * 7;
    return c;
}
__device__ __forceinline__ void conv_preload(u32x4 (&A0)[8], const ConvAddr& c) {
#pragma unroll
    for (int a = 0; a < 8; ++a) A0[a] = *(const u32x4*)(c.ap + 512 * a);
}
template <bool EARLY>
__device__ __forceinline__ void conv_col(char* smem, const ConvAddr& c, int wave, int lane, u32 seq) {
    u32x4 A0[8];
    if (EARLY) conv_preload(A0, c);
    BAR();
    if (!EARLY) conv_preload(A0, c);
    if ((wave & 1) == 0) conv_half<TileSet<1, 3, 4, 6>, TileSet<0, 2, 5, 7>>(smem, A0, c.ap, c.bp, wave, lane, wave >> 1, seq);
    else conv_half<TileSet<0, 2, 5, 7>, TileSet<1, 3, 4, 6>>(smem, A0, c.ap, c.bp, wave, lane, wave >> 1, seq);
    STAMP(26 + seq);
    BAR();
}
template <class PRE>
__device__ __forceinline__ void conv_row(char* smem, int wave, int lane, rsrc_t rs, int id, int fill0, int st, PRE pre) {
    const u32x4 sent = {SENT, SENT, SENT, SENT};
    const u32 off = WS_H + (fill0 < 0 ? 0 : fill0) * WS_HS + id * 8192 + ((wave - 4) * 64 + lane) * 16;
    const u64 t0 = __builtin_amdgcn_s_memrealtime();
    if (fill0 >= 0) { st_dev(rs, off, sent); st_dev(rs, off + 4096, sent); }
    pre();
    if (fill0 >= 0) {
        wait_until(t0 + FILL_GAP);
        st_dev(rs, off + WS_HS, sent); st_dev(rs, off + WS_HS + 4096, sent);
        WAIT_VM(2);
    }
    STAMP(st);
    BAR();
}

__device__ __forceinline__ void row_dma(char* smem, const char* q, const char* k, const char* v, int id, int wave, int lane, int s) {
    const int slot = wave + 8 * s;
    int row = 8 * id + (slot < 12 ? slot - 4 : slot < 20 ? slot - 12 : slot - 20);
    row = row < 0 ? 0 : row;
    const char* src = slot < 12 ? k : slot < 20 ? v : q;
    lds_dma16(src + row * 1024 + lane * 16, (u32)(size_t)(LAS char*)(smem + OFF_R) + slot * R_ROW);
}

__global__ void __launch_bounds__(512, 2)
h3s4_kernel(const char* __restrict__ q, const char* __restrict__ k, const char* __restrict__ v,
            const u32x2* __restrict__ shiftk, const u32x4* __restrict__ ssmk, const u32* __restrict__ ssmD,
            const u16* __restrict__ Are, const u16* __restrict__ Aim, const u16* __restrict__ Bre, const u16* __restrict__ Bim,
            const u16* __restrict__ Cre, const u16* __restrict__ Cim, const u16* __restrict__ logdt, const u32* __restrict__ Dskip,
            u16* __restrict__ out, char* ws
            ) {
    __shared__ __attribute__((aligned(16))) char smem[LDS_BYTES];
    const int tid = threadIdx.x, lane = tid & 63, wave = __builtin_amdgcn_readfirstlane(tid >> 6);
    const int id = (blockIdx.x & 7) * 32 + (blockIdx.x >> 3);
    const rsrc_t rs = __builtin_amdgcn_make_buffer_rsrc(ws, 0, WS_USED, 0x00020000);
    const u32x4 desc = {(u32)(size_t)ws, (u32)((size_t)ws >> 32) & 0xffffu, WS_USED, 0x00020000u};
    STAMP(0);

    u32 code_pf = 0;
    {
        u64 pc; u32 len;
        asm volatile("s_getpc_b64 %0\nh3s4_text_a:\n\ts_mov_b32 %1, h3s4_text_b - h3s4_text_a" : "=s"(pc), "=s"(len));
        const u32 off = ((blockIdx.x >> 3) * 8 + wave) * 256 + lane * 4;
        if (off + 4 <= len) asm volatile("global_load_dword %0, %1, %2" : "=v"(code_pf) : "v"(off), "s"(pc) : "memory");
    }
    const u32x4 sent = {SENT, SENT, SENT, SENT};
    const u32 dA = ssmD[id], dB = Dskip[id];
    MEMFENCE(); SB();
    st_dev(rs, WS_H + 0 * WS_HS + id * 8192 + tid * 16, sent);
    MEMFENCE(); SB();
    if (wave < 4) {
        row_dma(smem, q, k, v, id, wave, lane, 0); row_dma(smem, q, k, v, id, wave, lane, 1); row_dma(smem, q, k, v, id, wave, lane, 2);
        MEMFENCE(); SB();
        row_dma(smem, q, k, v, id, wave, lane, 3);
        u32x4 tap_own = ld_b128(ssmk, (2 * id) * 4096 + tid * 16), tap_prev = ld_b128(ssmk, (2 * id) * 4096 + (tid ? tid - 1 : 0) * 16);
        const int pd = 2 * id + (tid >> 7), po = (pd * NS + (tid & 127)) * 2;
        u32 r_dt = ld_u16(logdt, pd * 2), r_are = ld_u16(Are, po), r_aim = ld_u16(Aim, po), r_bre = ld_u16(Bre, po);
        u32 r_bim = ld_u16(Bim, po), r_cre = ld_u16(Cre, po), r_cim = ld_u16(Cim, po);
        u32x4 tix0 = ld_b128(tap_index.off, tid * 16), tix1 = ld_b128(tap_index.off, tid * 16 + 16);
        MEMFENCE(); SB();
        st_dev(rs, WS_H + 1 * WS_HS + id * 8192 + tid * 16, sent);
        MEMFENCE(); SB();
        const u32 epoch = launch_tag(ws);
        asm volatile("s_waitcnt vmcnt(15)\n\ts_barrier" ::: "memory");
        if (tid < 32) __hip_atomic_store((GAS u32*)(ws + WS_FLAGS) + tid * 256 + id, epoch, RLX_AGENT);
        STAMP(19);
        if (tid < 60) *(u32x4*)(smem + OFF_U + (tid / 30) * U_STRIDE + 4096 + (tid % 30) * 16) = (u32x4){0, 0, 0, 0};
        else if (tid >= 64 && tid < 80) *(u32x4*)(smem + OFF_T + (tid - 64) * T_COPY) = (u32x4){0, 0, 0, 0};
        asm volatile("s_waitcnt vmcnt(13)" ::: "memory");
        BAR();
        STAMP(1);
        asm volatile("s_cmp_eq_u32 %11, 0\n\ts_cbranch_scc1 1f\n\ts_waitcnt vmcnt(1)\n\ts_branch 2f\n1:\n\ts_waitcnt vmcnt(0)\n2:"
                     : "+v"(tap_own), "+v"(tap_prev), "+v"(r_dt), "+v"(r_are), "+v"(r_aim), "+v"(r_bre), "+v"(r_bim), "+v"(r_cre),
                       "+v"(r_cim), "+v"(tix0), "+v"(tix1)
                     : "s"((0x1u >> wave) & 1u) : "memory", "scc");
        cnt_arrive(smem, 6, lane);

        {
            const float dt = rbf(__builtin_amdgcn_exp2f(bf_lo(r_dt) * 1.4426950408889634f));
            const float are = dt * bf_lo(r_are), aim = dt * bf_lo(r_aim);
            const float br = dt * bf_lo(r_bre), bi = dt * bf_lo(r_bim);
            const float cr = bf_lo(r_cre), ci = bf_lo(r_cim);
            const float wr = cr * br - ci * bi, wi = cr * bi + ci * br;
            const float ar = fabsf(wr), ai = fabsf(wi), mx = fmaxf(ar, ai), mn = fminf(ar, ai);
            const float ratio = mx > 0.f ? mn * __builtin_amdgcn_rcpf(mx) : 0.f, r2 = ratio * ratio;
            float at = 0.0028662257f;
            at = fmaf(at, r2, -0.0161657367f); at = fmaf(at, r2, 0.0429096138f); at = fmaf(at, r2, -0.0752896400f);
            at = fmaf(at, r2, 0.1065626393f); at = fmaf(at, r2, -0.1420889944f); at = fmaf(at, r2, 0.1999355085f);
            at = fmaf(at, r2, -0.3333314528f); at = fmaf(at, r2, 1.0f);
            float ph = at * ratio * 0.15915494309189535f;
            ph = ai > ar ? 0.25f - ph : ph;
            ph = wr < 0.f ? 0.5f - ph : ph;
            ph = wi < 0.f ? -ph : ph;
            float* p = (float*)(smem + OFF_P) + (tid >> 1) * 8 + (tid & 1);
            p[0] = are * 1.4426950408889634f;
            p[2] = aim * 0.15915494309189535f;
            p[4] = __builtin_amdgcn_logf(mx) + 0.5f * __builtin_amdgcn_logf(r2 + 1.0f);
            p[6] = ph;
        }
        if (tid == 0) {
            tap_prev = (u32x4){0, 0, 0, 0};
            tap_own[0] = (tap_own[0] & 0xffff0000u) | (pk_bf(bf_lo(tap_own[0]) + bf_lo(dA), 0.f) & 0xffffu);
        }
        stage_taps(smem, 0, tid, tap_prev, tap_own);
        cnt_arrive(smem, 0, lane);
        STAMP(2);

        const ConvAddr ca = conv_addr(smem, wave, lane);
        asm volatile("" :: "v"(code_pf));
        cnt_wait(smem, 0, 8);
        STAMP(4);
#pragma nounroll
        for (u32 seq = 1; seq <= 3; ++seq) {
            if (seq == 2) {
                {
                    KState ks;
                    kgen_init(ks);
                    kgen_steps<4 - R_STEPS, true>(kgen_params(smem, wave, 0), lane, ks);
                    kgen_save(smem, wave, lane, ks);
                }
                STAMP(7);
                cnt_arrive(smem, 1, lane);
                cnt_wait(smem, 1, 8);
                kgen_reduce(smem, wave, lane, dB);
                cnt_arrive(smem, 2, lane);
                cnt_wait(smem, 2, 4);
                STAMP(23);
                s4_taps(smem, 0, tid, tix0, tix1);
                cnt_arrive(smem, 3, lane);
                cnt_wait(smem, 3, 8);
                STAMP(25);
            }
            conv_col<true>(smem, ca, wave, lane, seq);
            STAMP(seq == 1 ? 6 : seq == 2 ? 11 : 15);
        }
    } else {
        const int r = tid - 256, w = wave - 4;
        u32x2 skA = ld_b64(shiftk, r * 8), skB = ld_b64(shiftk, (r + 256) * 8);
        row_dma(smem, q, k, v, id, wave, lane, 0); row_dma(smem, q, k, v, id, wave, lane, 1);
        MEMFENCE(); SB();
        row_dma(smem, q, k, v, id, wave, lane, 2);
        u32x4 tap_own = ld_b128(ssmk, (2 * id + 1) * 4096 + r * 16), tap_prev = ld_b128(ssmk, (2 * id + 1) * 4096 + (r ? r - 1 : 0) * 16);
        u32x4 tix0 = ld_b128(tap_index.off, r * 16), tix1 = ld_b128(tap_index.off, r * 16 + 16);
        MEMFENCE(); SB();
        st_dev(rs, WS_H + 1 * WS_HS + id * 8192 + tid * 16, sent);
        MEMFENCE(); SB();
        const u32 epoch = launch_tag(ws);
        asm volatile("s_waitcnt vmcnt(9)\n\ts_barrier" ::: "memory");
        __builtin_amdgcn_s_setprio(2);
        ((u32*)(smem + OFF_RED))[r] = SENT;
        if (r < 16) ((u32*)(smem + OFF_CNT))[r] = 0u;
        asm volatile("s_waitcnt vmcnt(6)" : "+v"(skA), "+v"(skB) :: "memory");
        BAR();
        STAMP(1);

        const u32 own_a = id * 8192 + r * 16, own_b = own_a + 4096;
        const u32 col_a = (r >> 1) * 8192 + (2 * id + (r & 1)) * 16, col_b = col_a + 128 * 8192;
        const u32 row_a = (r >> 1) * 8192 + id * 32 + (r & 1) * 16, row_b = row_a + 128 * 8192;
        char* const u_dst = smem + OFF_U + (r & 1) * U_STRIDE + 4080 - (r >> 1) * 16;
        const char* const y_src = smem + OFF_Y + (r & 1) * 4096 + (r >> 1) * 16;
        float* red = (float*)(smem + OFF_RED);

        Rows x, u0;
        u64 t_s;
        const u32 flag_a = WS_FLAGS + (blockIdx.x & 31) * 1024 + (r >> 1) * 4;
        u32 fl_a, fl_b;
        {
            const u32 rp = (u32)(size_t)(LAS char*)(smem + OFF_R + ((lane & 15) >> 2) * R_ROW + (64 * w + 16 * (lane >> 4) + 4 * (lane & 3)) * 2);
            u32x2 kk[2][3], vv[2][2], qq[2][2];
            static_assert(4 * R_ROW == 4352, "offsets below");
            asm volatile("ds_read_b64_tr_b16 %0, %10\n\tds_read_b64_tr_b16 %1, %10 offset:4352\n\tds_read_b64_tr_b16 %2, %10 offset:8704\n\t"
                         "ds_read_b64_tr_b16 %3, %10 offset:13056\n\tds_read_b64_tr_b16 %4, %10 offset:17408\n\t"
                         "ds_read_b64_tr_b16 %5, %10 offset:512\n\tds_read_b64_tr_b16 %6, %10 offset:4864\n\tds_read_b64_tr_b16 %7, %10 offset:9216\n\t"
                         "ds_read_b64_tr_b16 %8, %10 offset:13568\n\tds_read_b64_tr_b16 %9, %10 offset:17920\n\ts_waitcnt lgkmcnt(0)"
                         : "=&v"(kk[0][0]), "=&v"(kk[0][1]), "=&v"(kk[0][2]), "=&v"(vv[0][0]), "=&v"(vv[0][1]),
                           "=&v"(kk[1][0]), "=&v"(kk[1][1]), "=&v"(kk[1][2]), "=&v"(vv[1][0]), "=&v"(vv[1][1]) : "v"(rp) : "memory");
            const u32 first = id ? 0xffffffffu : 0u;
            u32x4 o[2];
#pragma unroll
            for (int c = 0; c < 2; ++c) {
                const u32x2 sk = c ? skB : skA;
                const u32 kp = kk[c][0][0] & first, kq = kk[c][0][1] & first;
                const float kf[12] = {0.f, bf_hi(kp), bf_lo(kq), bf_hi(kq), bf_lo(kk[c][1][0]), bf_hi(kk[c][1][0]),
                                      bf_lo(kk[c][1][1]), bf_hi(kk[c][1][1]), bf_lo(kk[c][2][0]), bf_hi(kk[c][2][0]),
                                      bf_lo(kk[c][2][1]), bf_hi(kk[c][2][1])};
                float vf[8];
                unpack8((u32x4){vv[c][0][0], vv[c][0][1], vv[c][1][0], vv[c][1][1]}, vf);
                const float w0 = bf_lo(sk[0]), w1 = bf_hi(sk[0]), w2 = bf_lo(sk[1]), w3 = bf_hi(sk[1]);
                float kv[8];
#pragma unroll
                for (int j = 0; j < 8; ++j) kv[j] = fmaf(kf[j + 4], w0, fmaf(kf[j + 3], w1, fmaf(kf[j + 2], w2, kf[j + 1] * w3))) * vf[j];
                o[c] = pack8(kv);
            }
            publish(rs, WS_H + 0 * WS_HS + own_a, o[0]);
            publish(rs, WS_H + 0 * WS_HS + own_b, o[1]);
            t_s = __builtin_amdgcn_s_memrealtime();
            asm volatile("s_waitcnt vmcnt(2)" : "+v"(tap_own), "+v"(tap_prev), "+v"(tix0), "+v"(tix1) :: "memory");
            fl_a = ld_flag(ws, flag_a); fl_b = ld_flag(ws, flag_a + 512);
            cnt_arrive(smem, 6, lane);
            cnt_wait(smem, 6, 8);
            asm volatile("ds_read_b64_tr_b16 %0, %4 offset:21760\n\tds_read_b64_tr_b16 %1, %4 offset:26112\n\t"
                         "ds_read_b64_tr_b16 %2, %4 offset:22272\n\tds_read_b64_tr_b16 %3, %4 offset:26624\n\ts_waitcnt lgkmcnt(0)"
                         : "=&v"(qq[0][0]), "=&v"(qq[0][1]), "=&v"(qq[1][0]), "=&v"(qq[1][1]) : "v"(rp) : "memory");
            unpack_rows((u32x4){qq[0][0][0], qq[0][0][1], qq[0][1][0], qq[0][1][1]}, x.a);
            unpack_rows((u32x4){qq[1][0][0], qq[1][0][1], qq[1][1][0], qq[1][1][1]}, x.b);
        }
        STAMP(2);
        u32 touch = 0;
        if (wave == 6) {
            if (lane < 4) touch = __builtin_amdgcn_raw_buffer_load_b32(rs, WS_H + (2 + lane) * WS_HS + id * 8192, 0, 16);
            else if (lane == 4) touch = ((const u32*)out)[id * 2048];
        }
        if (r == 0) {
            tap_prev = (u32x4){0, 0, 0, 0};
            tap_own[0] = (tap_own[0] & 0xffff0000u) | (pk_bf(bf_lo(tap_own[0]) + bf_hi(dA), 0.f) & 0xffffu);
        }
        stage_taps(smem, 1, r, tap_prev, tap_own);
        cnt_arrive(smem, 0, lane);
        if (wave == 7 && lane < 32) __hip_atomic_store((GAS u32*)(ws + WS_FLAGS + 32768) + lane * 256 + id, epoch, RLX_AGENT);
        STAMP(3);

        Piece2 pc, pr;
        Rows yA, u;
        auto stage_input = [&](u32 h) {
            poll2_finish(pc, desc, WS_H + h * WS_HS + col_a, WS_H + h * WS_HS + col_b);
            *(u32x4*)u_dst = reverse8(pc.a);
            *(u32x4*)(u_dst - 2048) = reverse8(pc.b);
        };
        auto send_result = [&](u32 h) {
            publish(rs, WS_H + h * WS_HS + own_a, *(const u32x4*)y_src);
            publish(rs, WS_H + h * WS_HS + own_b, *(const u32x4*)(y_src + 2048));
            t_s = __builtin_amdgcn_s_memrealtime();
        };
        auto fetch_rows = [&](u32 h) {
            wait_until(t_s + POLL_DELAY);
            pr = poll2_issue(rs, WS_H + h * WS_HS + row_a, WS_H + h * WS_HS + row_b);
            poll2_finish(pr, desc, WS_H + h * WS_HS + row_a, WS_H + h * WS_HS + row_b);
        };

        wait_until(t_s + POLL_DELAY);
        {
            asm volatile("" :: "v"(fl_a), "v"(fl_b));
            const bool ok = fl_a == epoch && fl_b == epoch;
            pc = poll2_issue(rs, WS_H + col_a, WS_H + col_b);
            poll2_finish_flagged(pc, desc, WS_H + col_a, WS_H + col_b, flag_a, flag_a + 512, ok, epoch);
            *(u32x4*)u_dst = reverse8(pc.a);
            *(u32x4*)(u_dst - 2048) = reverse8(pc.b);
        }
        STAMP(21);
        asm volatile("" :: "v"(touch));
        asm volatile("" :: "v"(code_pf));
        BAR();
        STAMP(5);
        if (blockIdx.x == 0 && r == 0) __hip_atomic_store((GAS u32*)(ws + WS_EPOCH), epoch, RLX_AGENT);
        conv_row(smem, wave, lane, rs, id, 2, 27, [&] {
            __builtin_amdgcn_s_setprio(0);
            KState ks;
            kgen_init(ks);
            kgen_steps<R_STEPS, false>(kgen_params(smem, wave, 4 - R_STEPS), lane, ks);
            kgen_save(smem, wave, lane, ks);
            cnt_arrive(smem, 1, lane);
            __builtin_amdgcn_s_setprio(2);
        });
        send_result(1);
        STAMP(6);

#pragma nounroll
        for (u32 it = 0; it < 2; ++it) {
            MEMFENCE();
            const u32 fb_a = ld_flag(ws, flag_a + 32768), fb_b = ld_flag(ws, flag_a + 32768 + 512);
            MEMFENCE();
            layer_norm(x, u0, red + 128 * it, lane, w);
            if (it == 0) {
                wait_until(t_s + POLL_DELAY);
                asm volatile("" :: "v"(fb_a), "v"(fb_b));
                const bool ok = fb_a == epoch && fb_b == epoch;
                pr = poll2_issue(rs, WS_H + WS_HS + row_a, WS_H + WS_HS + row_b);
                poll2_finish_flagged(pr, desc, WS_H + WS_HS + row_a, WS_H + WS_HS + row_b, flag_a + 32768, flag_a + 32768 + 512, ok, epoch);
                STAMP(8);
                unpack_rows(pr.a, yA.a);
                unpack_rows(pr.b, yA.b);
            }
#pragma unroll
            for (int i = 0; i < 4; ++i) { x.a[i] += u0.a[i] * yA.a[i]; x.b[i] += u0.b[i] * yA.b[i]; }
            round_rows(x.a);
            round_rows(x.b);
            layer_norm(x, u, red + 128 * it + 64, lane, w);
            publish(rs, WS_H + (2 + 2 * it) * WS_HS + own_a, pack_rows(u.a));
            publish(rs, WS_H + (2 + 2 * it) * WS_HS + own_b, pack_rows(u.b));
            t_s = __builtin_amdgcn_s_memrealtime();
            STAMP(it ? 13 : 9);

            wait_until(t_s + POLL_DELAY);
            pc = poll2_issue(rs, WS_H + (2 + 2 * it) * WS_HS + col_a, WS_H + (2 + 2 * it) * WS_HS + col_b);
            if (it == 0) {
                cnt_wait(smem, 2, 4);
                s4_taps(smem, 1, r, tix0, tix1);
                cnt_arrive(smem, 3, lane);
            }
            stage_input(2 + 2 * it);
            BAR();
            STAMP(it ? 14 : 10);
            conv_row(smem, wave, lane, rs, id, it ? -1 : 4, 28, [] {});
            send_result(3 + 2 * it);
            STAMP(it ? 15 : 11);

            fetch_rows(3 + 2 * it);
            STAMP(it ? 16 : 12);
            Rows y;
            unpack_rows(pr.a, y.a);
            unpack_rows(pr.b, y.b);
#pragma unroll
            for (int i = 0; i < 4; ++i) { x.a[i] += y.a[i]; x.b[i] += y.b[i]; }
            pr.a = round_rows(x.a);
            pr.b = round_rows(x.b);
        }
        {
            u16* dst = out + (8 * id) * DM + r;
#pragma unroll
            for (int j = 0; j < 4; ++j) {
                dst[(2 * j) * DM] = (u16)pr.a[j];
                dst[(2 * j) * DM + 256] = (u16)pr.b[j];
                dst[(2 * j + 1) * DM] = (u16)(pr.a[j] >> 16);
                dst[(2 * j + 1) * DM + 256] = (u16)(pr.b[j] >> 16);
            }
        }
        STAMP(17);
    }
    asm volatile("h3s4_text_b:");
}

extern "C" void kernel_launch(void* const* d_in, const int* in_sizes, int n_in,
                              void* d_out, int out_size, void* d_ws, size_t ws_size,
                              hipStream_t stream) {
    h3s4_kernel<<<256, 512, 0, stream>>>(
        (const char*)d_in[0], (const char*)d_in[1], (const char*)d_in[2], (const u32x2*)d_in[3], (const u32x4*)d_in[4],
        (const u32*)d_in[5], (const u16*)d_in[7], (const u16*)d_in[8], (const u16*)d_in[9], (const u16*)d_in[10],
        (const u16*)d_in[11], (const u16*)d_in[12], (const u16*)d_in[13], (const u32*)d_in[14], (u16*)d_out, (char*)d_ws
        );
}
```

```cpp
#include <hip/hip_runtime.h>
#include <hip/hip_bf16.h>
#include <stdint.h>

typedef unsigned int u32;
typedef unsigned short u16;
typedef unsigned long long u64;
typedef __attribute__((ext_vector_type(4))) float f32x4;
typedef __attribute__((ext_vector_type(2))) float f32x2;
typedef __attribute__((ext_vector_type(4))) u32 u32x4;
typedef __attribute__((ext_vector_type(2))) u32 u32x2;
typedef __attribute__((ext_vector_type(4))) short s16x4;
typedef __attribute__((ext_vector_type(8))) __bf16 bf16x8;
typedef __attribute__((ext_vector_type(2))) __bf16 bf16x2;
#define LAS __attribute__((address_space(3)))
#define GAS __attribute__((address_space(1)))
#define RLX_AGENT __ATOMIC_RELAXED, __HIP_MEMORY_SCOPE_AGENT
#define SB() __builtin_amdgcn_sched_barrier(0)
#define BAR() asm volatile("s_waitcnt lgkmcnt(0)\n\ts_barrier" ::: "memory")
#define MEMFENCE() asm volatile("" ::: "memory")
#define WAIT_VM(n) do { MEMFENCE(); __builtin_amdgcn_s_waitcnt(0x0F70 | ((n) & 15) | (((n) >> 4) << 14)); MEMFENCE(); } while (0)

constexpr int DM = 512;
constexpr int NS = 128;
constexpr u32 SENT = 0xffffffffu;
constexpr u32 SPIN_CAP = 20000;
#define FILL_GAP 70
#define C_PRE 1
#define R_STEPS 1
#define POLL_DELAY 45

constexpr u32 WS_EPOCH = 0;
constexpr u32 WS_FLAGS = 4096;
constexpr u32 WS_H = 1u << 20;
constexpr u32 WS_HS = 2u << 20;
constexpr u32 WS_USED = WS_H + 6 * WS_HS;

constexpr int T_COPY = 4160, T_CH = 8 * T_COPY;
constexpr int U_STRIDE = 4672;
constexpr int R_ROW = 1088;
constexpr int KP_WAVE = 2816;
constexpr int OFF_T = 0;
constexpr int OFF_U = OFF_T + 2 * T_CH;
constexpr int OFF_X = OFF_U + 2 * U_STRIDE;
constexpr int OFF_Y = OFF_X + 16384;
constexpr int OFF_R = OFF_Y + 8192;
constexpr int OFF_P = OFF_R + 28 * R_ROW;
constexpr int OFF_KP = OFF_P + 4096;
constexpr int OFF_KC = OFF_KP + 8 * KP_WAVE;
constexpr int OFF_RED = OFF_KC + 2 * 648 * 2;
constexpr int OFF_CNT = OFF_RED + 1024;
constexpr int OFF_ST = OFF_CNT + 64;
constexpr int LDS_BYTES = OFF_ST + 512;
static_assert(LDS_BYTES <= 163840, "LDS");

#define STAMP(i)

__device__ __forceinline__ float bf_lo(u32 p) { return __builtin_bit_cast(float, p << 16); }
__device__ __forceinline__ float bf_hi(u32 p) { return __builtin_bit_cast(float, p & 0xffff0000u); }
__device__ __forceinline__ float bf_u16(u16 p) { return __builtin_bit_cast(float, (u32)p << 16); }
__device__ __forceinline__ u32 pk_bf(float lo, float hi) {
    bf16x2 v = {(__bf16)lo, (__bf16)hi};
    return __builtin_bit_cast(u32, v);
}
__device__ __forceinline__ float as_f(u32 x) { return __builtin_bit_cast(float, x); }
__device__ __forceinline__ u32 as_u(float x) { return __builtin_bit_cast(u32, x); }
__device__ __forceinline__ float rbf(float x) { return bf_lo(pk_bf(x, x)); }
__device__ __forceinline__ void unpack8(u32x4 p, float (&f)[8]) {
#pragma unroll
    for (int i = 0; i < 4; ++i) { f[2 * i] = bf_lo(p[i]); f[2 * i + 1] = bf_hi(p[i]); }
}
__device__ __forceinline__ u32x4 pack8(const float (&f)[8]) {
    return (u32x4){pk_bf(f[0], f[1]), pk_bf(f[2], f[3]), pk_bf(f[4], f[5]), pk_bf(f[6], f[7])};
}
__device__ __forceinline__ u32x4 reverse8(u32x4 p) {
    return (u32x4){__builtin_amdgcn_alignbit(p[3], p[3], 16), __builtin_amdgcn_alignbit(p[2], p[2], 16),
                   __builtin_amdgcn_alignbit(p[1], p[1], 16), __builtin_amdgcn_alignbit(p[0], p[0], 16)};
}
template <int CTRL> __device__ __forceinline__ float dppf(float v) {
    return __builtin_bit_cast(float, __builtin_amdgcn_update_dpp(0, __builtin_bit_cast(int, v), CTRL, 0xf, 0xf, true));
}

__device__ __forceinline__ void lds_dma16(const char* src, u32 lds) {
    u32 keep;
    asm volatile("s_mov_b32 %0, m0\n\ts_mov_b32 m0, %2\n\ts_nop 0\n\tglobal_load_lds_dwordx4 %1, off\n\ts_mov_b32 m0, %0"
                 : "=&s"(keep) : "v"(src), "s"(lds) : "memory");
}

__device__ __forceinline__ u32 ld_u16(const void* base, u32 off) {
    u32 r;
    asm volatile("global_load_ushort %0, %1, %2" : "=v"(r) : "v"(off), "s"(base) : "memory");
    return r;
}
__device__ __forceinline__ u32x2 ld_b64(const void* base, u32 off) {
    u32x2 r;
    asm volatile("global_load_dwordx2 %0, %1, %2" : "=v"(r) : "v"(off), "s"(base) : "memory");
    return r;
}
__device__ __forceinline__ u32x4 ld_b128(const void* base, u32 off) {
    u32x4 r;
    asm volatile("global_load_dwordx4 %0, %1, %2" : "=v"(r) : "v"(off), "s"(base) : "memory");
    return r;
}

typedef __amdgpu_buffer_rsrc_t rsrc_t;
__device__ __forceinline__ u32x4 ld_dev(rsrc_t rs, u32 off) { return __builtin_amdgcn_raw_buffer_load_b128(rs, off, 0, 16); }
__device__ __forceinline__ void st_dev(rsrc_t rs, u32 off, u32x4 v) { __builtin_amdgcn_raw_buffer_store_b128(v, rs, off, 0, 16); }
__device__ __forceinline__ void publish(rsrc_t rs, u32 off, u32x4 v) {
#pragma unroll
    for (int i = 0; i < 4; ++i) v[i] = v[i] < SENT - 1u ? v[i] : SENT - 1u;
    st_dev(rs, off, v);
}
__device__ __forceinline__ u32 launch_tag(const char* ws) {
    u32 e;
    asm volatile("s_load_dword %0, %1, 0x0\n\ts_waitcnt lgkmcnt(0)" : "=&s"(e) : "s"(ws + WS_EPOCH) : "memory");
    return e + 1u;
}
__device__ __forceinline__ void wait_until(u64 t) {
    while (__builtin_amdgcn_s_memrealtime() < t) __builtin_amdgcn_s_sleep(1);
}
__device__ __forceinline__ u32x4 poll_issue(rsrc_t rs, u32 off) {
    MEMFENCE();
    u32x4 x = ld_dev(rs, off);
    MEMFENCE();
    return x;
}
struct Piece2 { u32x4 a, b; };
__device__ __forceinline__ Piece2 poll2_issue(rsrc_t rs, u32 off_a, u32 off_b) {
    MEMFENCE();
    Piece2 p = {ld_dev(rs, off_a), ld_dev(rs, off_b)};
    MEMFENCE();
    return p;
}
__device__ __forceinline__ bool has_sentinel(const u32x4& x) {
    u32 mx = x[0] > x[1] ? x[0] : x[1];
    mx = mx > x[2] ? mx : x[2];
    mx = mx > x[3] ? mx : x[3];
    return mx == SENT;
}
__device__ __forceinline__ void poll2_finish(Piece2& p, u32x4 desc, u32 off_a, u32 off_b) {
#pragma nounroll
    for (u32 spins = 0; spins < SPIN_CAP; ++spins) {
        const bool bad = has_sentinel(p.a) || has_sentinel(p.b);
        if (__builtin_amdgcn_ballot_w64(bad) == 0ull) break;
        __builtin_amdgcn_s_sleep(1);
        if (bad) asm volatile("buffer_load_dwordx4 %0, %2, %4, 0 offen sc1\n\tbuffer_load_dwordx4 %1, %3, %4, 0 offen sc1\n\ts_waitcnt vmcnt(0)"
                              : "+v"(p.a), "+v"(p.b) : "v"(off_a), "v"(off_b), "s"(desc) : "memory");
    }
}
__device__ __forceinline__ u32 ld_flag(const char* ws, u32 off) { return __hip_atomic_load((const GAS u32*)(ws + off), RLX_AGENT); }
__device__ __forceinline__ void poll2_finish_flagged(Piece2& p, u32x4 desc, u32 off_a, u32 off_b, u32 fo_a, u32 fo_b, bool ok, u32 epoch) {
#pragma nounroll
    for (u32 spins = 0; spins < SPIN_CAP; ++spins) {
        const bool bad = !ok || has_sentinel(p.a) || has_sentinel(p.b);
        if (__builtin_amdgcn_ballot_w64(bad) == 0ull) break;
        if (!ok) {
            u32 fa, fb;
            asm volatile("buffer_load_dword %0, %2, %4, 0 offen sc1\n\tbuffer_load_dword %1, %3, %4, 0 offen sc1\n\ts_waitcnt vmcnt(0)"
                         : "=&v"(fa), "=&v"(fb) : "v"(fo_a), "v"(fo_b), "s"(desc) : "memory");
            ok = fa == epoch && fb == epoch;
        }
        if (bad) asm volatile("buffer_load_dwordx4 %0, %2, %4, 0 offen sc1\n\tbuffer_load_dwordx4 %1, %3, %4, 0 offen sc1\n\ts_waitcnt vmcnt(0)"
                              : "+v"(p.a), "+v"(p.b) : "v"(off_a), "v"(off_b), "s"(desc) : "memory");
    }
}

__device__ __forceinline__ void cnt_arrive(char* smem, int k, int lane) {
    asm volatile("s_waitcnt lgkmcnt(0)" ::: "memory");
    if (lane == 0) __hip_atomic_fetch_add((LAS u32*)(smem + OFF_CNT) + k, 1u, __ATOMIC_RELAXED, __HIP_MEMORY_SCOPE_WORKGROUP);
}
__device__ __forceinline__ void cnt_wait(char* smem, int k, u32 target) {
    u32 spins = 0;
    while (__hip_atomic_load((LAS u32*)(smem + OFF_CNT) + k, __ATOMIC_RELAXED, __HIP_MEMORY_SCOPE_WORKGROUP) < target) {
        if (++spins > 50 * SPIN_CAP) break;
        __builtin_amdgcn_s_sleep(1);
    }
    MEMFENCE();
}

struct Rows { f32x2 a[4], b[4]; };
__device__ __forceinline__ void unpack_rows(u32x4 p, f32x2 (&f)[4]) {
#pragma unroll
    for (int i = 0; i < 4; ++i) f[i] = (f32x2){bf_lo(p[i]), bf_hi(p[i])};
}
__device__ __forceinline__ u32x4 pack_rows(const f32x2 (&f)[4]) {
    return (u32x4){pk_bf(f[0][0], f[0][1]), pk_bf(f[1][0], f[1][1]), pk_bf(f[2][0], f[2][1]), pk_bf(f[3][0], f[3][1])};
}
__device__ __forceinline__ u32x4 round_rows(f32x2 (&f)[4]) {
    u32x4 p = pack_rows(f);
    asm("" : "+v"(p));
    unpack_rows(p, f);
    return p;
}
__device__ __forceinline__ void layer_norm(const Rows& x, Rows& u, float* red, int lane, int w) {
#pragma clang fp contract(fast)
    u32 a[8], b[8];
#pragma unroll
    for (int i = 0; i < 4; ++i) {
        const f32x2 s1 = x.a[i] + x.b[i], s2 = x.a[i] * x.a[i] + x.b[i] * x.b[i];
        a[2 * i] = as_u(s1[0]); a[2 * i + 1] = as_u(s1[1]);
        b[2 * i] = as_u(s2[0]); b[2 * i + 1] = as_u(s2[1]);
    }
    SB();
#pragma unroll
    for (int j = 0; j < 8; ++j) {
        const auto s = __builtin_amdgcn_permlane32_swap(a[j], b[j], false, false);
        a[j] = s[0]; b[j] = s[1];
    }
    SB();
    float r[8];
#pragma unroll
    for (int j = 0; j < 8; ++j) r[j] = as_f(a[j]) + as_f(b[j]);
    SB();
#pragma unroll
    for (int j = 0; j < 4; ++j) {
        const auto s = __builtin_amdgcn_permlane16_swap(as_u(r[j]), as_u(r[j + 4]), false, false);
        a[j] = s[0]; b[j] = s[1];
    }
    SB();
    float s4[4];
#pragma unroll
    for (int j = 0; j < 4; ++j) s4[j] = as_f(a[j]) + as_f(b[j]);
    SB();
#pragma unroll
    for (int j = 0; j < 4; ++j) s4[j] += dppf<0x128>(s4[j]);
    const float t0 = (lane & 8) ? s4[2] : s4[0], t1 = (lane & 8) ? s4[3] : s4[1];
    const float c0 = t0 + dppf<0x141>(t0), c1 = t1 + dppf<0x141>(t1);
    float s = (lane & 4) ? c1 : c0;
    s += dppf<0xB1>(s);
    s += dppf<0x4E>(s);
    const u32 sb = as_u(s) < SENT - 1u ? as_u(s) : SENT - 1u;
    if ((lane & 3) == 0) ((volatile LAS u32*)(LAS char*)(char*)red)[(lane >> 2) * 4 + w] = sb;
    u32x4 p;
    u32 spins = 0;
    while (true) {
        p = *(volatile LAS u32x4*)((LAS char*)(char*)red + (lane & 15) * 16);
        u32 mx = p[0] > p[1] ? p[0] : p[1];
        mx = mx > p[2] ? mx : p[2];
        mx = mx > p[3] ? mx : p[3];
        if (__builtin_amdgcn_ballot_w64(mx == SENT) == 0ull) break;
        if (++spins > 50 * SPIN_CAP) break;
    }
    const float m = ((as_f(p[0]) + as_f(p[1])) + (as_f(p[2]) + as_f(p[3]))) * (1.0f / DM);
    const float ex2 = dppf<0x128>(m);
    const float rstd = __builtin_amdgcn_rsqf(fmaxf(fmaf(-m, m, ex2), 0.f) + 1e-5f);
    const float off = -m * rstd;
    const f32x2 R[4] = {{dppf<0x150>(rstd), dppf<0x151>(rstd)}, {dppf<0x152>(rstd), dppf<0x153>(rstd)},
                        {dppf<0x154>(rstd), dppf<0x155>(rstd)}, {dppf<0x156>(rstd), dppf<0x157>(rstd)}};
    const f32x2 O[4] = {{dppf<0x150>(off), dppf<0x151>(off)}, {dppf<0x152>(off), dppf<0x153>(off)},
                        {dppf<0x154>(off), dppf<0x155>(off)}, {dppf<0x156>(off), dppf<0x157>(off)}};
#pragma unroll
    for (int i = 0; i < 4; ++i) { u.a[i] = x.a[i] * R[i] + O[i]; u.b[i] = x.b[i] * R[i] + O[i]; }
}

__device__ __forceinline__ void stage_taps(char* smem, int ch, int i, u32x4 prev, u32x4 own) {
    const u32 D[8] = {prev[0], prev[1], prev[2], prev[3], own[0], own[1], own[2], own[3]};
    u32 O[7];
#pragma unroll
    for (int y = 0; y < 7; ++y) O[y] = __builtin_amdgcn_alignbit(D[y + 1], D[y], 16);
    char* base = smem + OFF_T + ch * T_CH + (i + 1) * 16;
#pragma unroll
    for (int c = 0; c < 8; ++c) {
        const int y = (c + 1) >> 1;
        const u32x4 v = (c & 1) ? (u32x4){D[y], D[y + 1], D[y + 2], D[y + 3]} : (u32x4){O[y], O[y + 1], O[y + 2], O[y + 3]};
        *(u32x4*)(base + c * T_COPY) = v;
    }
}

__device__ __forceinline__ f32x4 mfma16(u32x4 a, u32x4 b, f32x4 c) {
    return __builtin_amdgcn_mfma_f32_16x16x32_bf16(__builtin_bit_cast(bf16x8, a), __builtin_bit_cast(bf16x8, b), c, 0, 0, 0);
}
struct KState { f32x4 acc[3]; };
__device__ __forceinline__ void kgen_init(KState& ks) {
#pragma unroll
    for (int e = 0; e < 3; ++e) ks.acc[e] = (f32x4){0, 0, 0, 0};
}
#define OPQ(x) asm volatile("" : "+v"(x))
template <int NS_, bool PK>
__device__ __forceinline__ void kgen_steps(const f32x4* P, int lane, KState& ks) {
#pragma clang fp contract(fast)
    const int g = lane >> 4, m = lane & 15;
    const float lo = (float)(8 * m);
    const float hi[3] = {(float)(128 * (m >> 3) + (m & 7)),
                         (float)(m < 8 ? 256 + 128 * (m >> 2) + 2 * (m & 3) : 512 + 128 * ((m - 8) >> 1) + 4 * (m & 1)),
                         (float)(m < 8 ? 1024 + 128 * m : 2048)};
    auto eval = [&](f32x2 ex, f32x2 an, bool conj, u32& o0, u32& o1) {
        if (!PK) { OPQ(ex[0]); OPQ(ex[1]); OPQ(an[0]); OPQ(an[1]); }
        const float m0 = __builtin_amdgcn_exp2f(ex[0]), m1 = __builtin_amdgcn_exp2f(ex[1]);
        const float f0 = __builtin_amdgcn_fractf(an[0]), f1 = __builtin_amdgcn_fractf(an[1]);
        const float c0 = __builtin_amdgcn_cosf(f0), c1 = __builtin_amdgcn_cosf(f1);
        const float s0 = __builtin_amdgcn_sinf(f0), s1 = __builtin_amdgcn_sinf(f1);
        float r0, r1, i0, i1;
        if (PK) {
            const f32x2 r = (f32x2){c0, c1} * (f32x2){m0, m1}, i = (f32x2){s0, s1} * (f32x2){m0, m1};
            r0 = r[0]; r1 = r[1]; i0 = i[0]; i1 = i[1];
        } else {
            r0 = c0 * m0; OPQ(r0); i0 = s0 * m0; OPQ(i0); r1 = c1 * m1; OPQ(r1); i1 = s1 * m1; OPQ(i1);
        }
        o0 = pk_bf(r0, conj ? -i0 : i0);
        o1 = pk_bf(r1, conj ? -i1 : i1);
    };
    auto affine = [&](f32x2 a, float p, f32x2 b) -> f32x2 {
        if (PK) return a * p + b;
        float x = fmaf(a[0], p, b[0]), y = fmaf(a[1], p, b[1]);
        OPQ(x); OPQ(y);
        return (f32x2){x, y};
    };
#pragma unroll
    for (int s = 0; s < NS_; ++s) {
        u32x4 B, A[3];
#pragma unroll
        for (int h = 0; h < 2; ++h) {
            const f32x4 p0 = P[16 * s + 4 * g + 2 * h], p1 = P[16 * s + 4 * g + 2 * h + 1];
            u32 o0, o1;
            eval(affine(p0.xy, lo, (f32x2){0.f, 0.f}), affine(p0.zw, lo, (f32x2){0.f, 0.f}), true, o0, o1);
            B[2 * h] = o0; B[2 * h + 1] = o1;
#pragma unroll
            for (int e = 0; e < 3; ++e) {
                eval(affine(p0.xy, hi[e], p1.xy), affine(p0.zw, hi[e], p1.zw), false, o0, o1);
                A[e][2 * h] = o0; A[e][2 * h + 1] = o1;
            }
        }
#pragma unroll
        for (int e = 0; e < 3; ++e) ks.acc[e] = mfma16(A[e], B, ks.acc[e]);
    }
}
__device__ __forceinline__ const f32x4* kgen_params(char* smem, int wave, int step) {
    return (const f32x4*)(smem + OFF_P) + ((wave >> 1) & 1) * 128 + ((wave & 1) * 4 + step) * 16;
}
__device__ __forceinline__ void kgen_save(char* smem, int wave, int lane, const KState& ks) {
    char* p = smem + OFF_KP + wave * KP_WAVE + lane * 16;
    *(f32x4*)p = ks.acc[0];
    *(f32x4*)(p + 1024) = ks.acc[1];
    if (lane < 48) *(f32x4*)(p + 2048) = ks.acc[2];
}
__device__ __forceinline__ void kgen_load(const char* smem, int wave, int lane, KState& ks) {
    const char* p = smem + OFF_KP + wave * KP_WAVE;
    ks.acc[0] = *(const f32x4*)(p + lane * 16);
    ks.acc[1] = *(const f32x4*)(p + 1024 + lane * 16);
    ks.acc[2] = *(const f32x4*)(p + 2048 + (lane < 48 ? lane : 0) * 16);
}
__device__ __forceinline__ f32x4 kgen_sum(const char* p) {
    return (*(const f32x4*)p + *(const f32x4*)(p + KP_WAVE)) + (*(const f32x4*)(p + 4 * KP_WAVE) + *(const f32x4*)(p + 5 * KP_WAVE));
}
__device__ __forceinline__ void kgen_reduce(char* smem, int wave, int lane, u32 dskip) {
    const int c = wave >> 1, j = lane & 15, g = lane >> 4;
    char* KC = smem + OFF_KC + c * 1296;
    const char* p = smem + OFF_KP + 2 * c * KP_WAVE + lane * 16;
    if ((wave & 1) == 0) {
        f32x4 s = kgen_sum(p);
        const f32x4 t = kgen_sum(smem + OFF_KP + 2 * c * KP_WAVE + 2048 + (lane < 48 ? lane : 0) * 16);
        if (lane == 0) s[0] += c ? bf_hi(dskip) : bf_lo(dskip);
        *(u32x2*)(KC + 2 * (128 * (g >> 1) + 4 * (g & 1) + 8 * j)) = (u32x2){pk_bf(s[0], s[1]), pk_bf(s[2], s[3])};
        const u32 lo = pk_bf(t[0], t[1]), hi = pk_bf(t[2], t[3]);
        u16* o = (u16*)KC + 512 + 64 * g + j;
        if (g < 2) { o[0] = (u16)lo; o[16] = (u16)(lo >> 16); o[32] = (u16)hi; o[48] = (u16)(hi >> 16); }
        else if (lane == 32) { ((u16*)KC)[640] = (u16)lo; ((u16*)KC)[641] = 0; }
    } else {
        const f32x4 s = kgen_sum(p + 1024);
        const u32 lo = pk_bf(s[0], s[1]), hi = pk_bf(s[2], s[3]);
        if (g < 2) *(u32x2*)(KC + 2 * (256 + 64 * g + 4 * j)) = (u32x2){lo, hi};
        else {
            u32* o = (u32*)(KC + 2 * (384 + 64 * (g - 2) + 2 * j));
            o[0] = lo; o[16] = hi;
        }
    }
}

struct TapIndex {
    u16 off[2048 + 8];
    constexpr TapIndex() : off() {
        for (int i = 0; i < 8; ++i) off[i] = 641 * 2;
        for (int l = 0; l < 2048; ++l) {
            int e = 0;
            while ((l >> e) >= 256) ++e;
            int v = l >> e;
            if (e > 0) {
                const int rem = l & ((1 << e) - 1), half = 1 << (e - 1);
                if (rem > half || (rem == half && (v & 1))) ++v;
            }
            off[8 + l] = (u16)(2 * (128 * e + v));
        }
    }
};
__device__ const TapIndex tap_index = TapIndex();

__device__ __forceinline__ void s4_taps(char* smem, int ch, int i, u32x4 tix0, u32x4 tix1) {
    const char* kc = smem + OFF_KC + ch * 1296;
    u32 d[8];
#pragma unroll
    for (int j = 0; j < 8; ++j) {
        const u32 o = j < 4 ? tix0[j] : tix1[j - 4];
        d[j] = (u32)*(const u16*)(kc + (o & 0xffffu)) | ((u32)*(const u16*)(kc + (o >> 16)) << 16);
    }
    stage_taps(smem, ch, i, (u32x4){d[0], d[1], d[2], d[3]}, (u32x4){d[4], d[5], d[6], d[7]});
}

struct Frags { u32x4 A[8], B[8]; };
template <bool INIT> __device__ __forceinline__ void mma(f32x4& acc, const u32x4& a, const u32x4& b) {
    if constexpr (INIT) asm volatile("v_mfma_f32_16x16x32_bf16 %0, %1, %2, 0" : "=&v"(acc) : "v"(a), "v"(b));
    else asm volatile("v_mfma_f32_16x16x32_bf16 %0, %1, %2, %0" : "+v"(acc) : "v"(a), "v"(b));
}
template <int I> __device__ __forceinline__ void load_one(Frags& f, const char* ap, const char* bp) {
    if constexpr (I == 0) f.A[0] = *(const u32x4*)ap;
    else if constexpr (I <= 8) f.B[I - 1] = *(const u32x4*)(bp + 512 * (8 - I));
    else f.A[I - 8] = *(const u32x4*)(ap + 512 * (I - 8));
}
template <int I> __device__ __forceinline__ void load_all(Frags& f, const char* ap, const char* bp) {
    if constexpr (I < 16) { load_one<I>(f, ap, bp); SB(); load_all<I + 1>(f, ap, bp); }
}
template <int T0, int T1, int T2, int T3> struct TileSet {
    static constexpr int tile(int i) { return i == 0 ? T0 : i == 1 ? T1 : i == 2 ? T2 : T3; }
    static constexpr int walk(int I, bool want_tile) {
        int n = 0;
        for (int a = 0; a < 8; ++a)
            for (int j = 0; j < 4; ++j)
                if (tile(j) >= a) { if (n == I) return want_tile ? tile(j) : a; ++n; }
        return -1;
    }
};
constexpr int tri_a(int I) { int a = 0; while (I >= 8 - a) { I -= 8 - a; ++a; } return a; }
constexpr int tri_b(int I) { int a = 0; while (I >= 8 - a) { I -= 8 - a; ++a; } return I; }
template <int I, bool FIRST> __device__ __forceinline__ void class_step(const Frags& cur, Frags& nxt, f32x4 (&acc)[8], const char* ap, const char* bp) {
    if constexpr (I < 36) {
        constexpr int a = tri_a(I), b = tri_b(I);
        mma<FIRST && a == 0>(acc[a + b], cur.A[a], cur.B[b]);
        if constexpr ((I & 1) == 1 && (I >> 1) < 16) load_one<(I >> 1)>(nxt, ap, bp);
        SB();
        class_step<I + 1, FIRST>(cur, nxt, acc, ap, bp);
    }
}
template <class TS, int I, int TO, int PRE> __device__ __forceinline__ void set_step(const Frags& cur, Frags& nxt, f32x4 (&acc)[8], const char* ap, const char* bp) {
    if constexpr (I < TO) {
        constexpr int t = TS::walk(I, true), a = TS::walk(I, false);
        mma<false>(acc[t], cur.A[a], cur.B[t - a]);
        if constexpr (PRE == 1 && (I * 11) / 18 != ((I + 1) * 11) / 18) load_one<(I * 11) / 18>(nxt, ap, bp);
        if constexpr (PRE == 2 && I < 5) load_one<11 + I>(nxt, ap, bp);
        SB();
        set_step<TS, I + 1, TO, PRE>(cur, nxt, acc, ap, bp);
    }
}
template <class TS, int J> __device__ __forceinline__ void tile_step(const Frags& cur, f32x4 (&acc)[8]) {
    constexpr int t = TS::tile(J);
#pragma unroll
    for (int a = 0; a <= t; ++a) { mma<false>(acc[t], cur.A[a], cur.B[t - a]); SB(); }
}
template <class TS, int J> __device__ __forceinline__ void tile_out(char* Y, f32x4 (&acc)[8], const f32x4 (&o)[4]) {
    constexpr int t = TS::tile(J);
    const f32x4 s = acc[t] + o[J];
    const u32x2 pk = {pk_bf(s[0], s[1]), pk_bf(s[2], s[3])};
    *(u32x2*)(Y + 512 * t) = pk;
    SB();
}
template <class SEND, class KEEP>
__device__ __forceinline__ void conv_half(char* smem, const u32x4 (&A0)[8], const char* ap, const char* bp, int wave, int lane, int ch, u32 seq) {
    f32x4 acc[8];
    Frags f0, f1;
#pragma unroll
    for (int a = 0; a < 8; ++a) f0.A[a] = A0[a];
#pragma unroll
    for (int b = 0; b < 8; ++b) { f0.B[b] = *(const u32x4*)(bp + 512 * (7 - b)); SB(); }
    class_step<0, true>(f0, f1, acc, ap + 64, bp + 64);
    class_step<0, false>(f1, f0, acc, ap + 128, bp + 128);
    set_step<SEND, 0, 18, 1>(f0, f1, acc, ap + 192, bp + 192);
    set_step<SEND, 0, 18, 2>(f1, f1, acc, ap + 192, bp + 192);
    set_step<KEEP, 0, 4, 0>(f0, f1, acc, ap, bp);
    asm volatile("s_nop 3" : "+v"(acc[SEND::tile(0)]), "+v"(acc[SEND::tile(1)]), "+v"(acc[SEND::tile(2)]), "+v"(acc[SEND::tile(3)]));
    f32x4* X = (f32x4*)(smem + OFF_X);
#pragma unroll
    for (int i = 0; i < 4; ++i) X[(wave * 4 + i) * 64 + lane] = acc[SEND::tile(i)];
    volatile LAS u32* xf = (volatile LAS u32*)(LAS char*)(smem + OFF_CNT + 32);
    xf[wave] = seq;
    SB();
    set_step<KEEP, 4, 14, 0>(f0, f1, acc, ap, bp);
    u32 seen = xf[wave ^ 1];
    SB();
    set_step<KEEP, 14, 18, 0>(f0, f1, acc, ap, bp);
    for (u32 spins = 0; seen < seq && spins < 50 * SPIN_CAP; ++spins) seen = xf[wave ^ 1];
    SB();
    f32x4 o[4];
#pragma unroll
    for (int i = 0; i < 4; ++i) o[i] = X[((wave ^ 1) * 4 + i) * 64 + lane];
    SB();
    char* Y = smem + OFF_Y + ch * 4096 + (16 * (lane & 15) + 4 * (lane >> 4)) * 2;
    tile_step<KEEP, 0>(f1, acc);
    tile_step<KEEP, 1>(f1, acc);
    tile_step<KEEP, 2>(f1, acc);
    asm volatile("s_nop 3" : "+v"(acc[KEEP::tile(0)]), "+v"(acc[KEEP::tile(1)]));
    tile_out<KEEP, 0>(Y, acc, o);
    tile_out<KEEP, 1>(Y, acc, o);
    tile_step<KEEP, 3>(f1, acc);
    asm volatile("" : "+v"(acc[KEEP::tile(2)]));
    tile_out<KEEP, 2>(Y, acc, o);
    asm volatile("s_nop 7" : "+v"(acc[KEEP::tile(3)]));
    tile_out<KEEP, 3>(Y, acc, o);
}
struct ConvAddr { const char* ap; const char* bp; };
__device__ __forceinline__ ConvAddr conv_addr(char* smem, int wave, int lane) {
    const int ch = (wave >> 1) & 1, hf = wave & 1;
    const int m = lane & 15, g = lane >> 4;
    ConvAddr c;
    c.ap = smem + OFF_T + ch * T_CH + (m & 7) * T_COPY + 16 * (m >> 3) + 16 * g + 256 * hf;
    c.bp = smem + OFF_U + ch * U_STRIDE + 4064 - 32 * m + 16 * g + 256 * hf - 512 * 7;
    return c;
}
__device__ __forceinline__ void conv_preload(u32x4 (&A0)[8], const ConvAddr& c) {
#pragma unroll
    for (int a = 0; a < 8; ++a) A0[a] = *(const u32x4*)(c.ap + 512 * a);
}
template <bool EARLY>
__device__ __forceinline__ void conv_col(char* smem, const ConvAddr& c, int wave, int lane, u32 seq) {
    u32x4 A0[8];
    if (EARLY) conv_preload(A0, c);
    BAR();
    if (!EARLY) conv_preload(A0, c);
    if ((wave & 1) == 0) conv_half<TileSet<1, 3, 4, 6>, TileSet<0, 2, 5, 7>>(smem, A0, c.ap, c.bp, wave, lane, wave >> 1, seq);
    else conv_half<TileSet<0, 2, 5, 7>, TileSet<1, 3, 4, 6>>(smem, A0, c.ap, c.bp, wave, lane, wave >> 1, seq);
    STAMP(26 + seq);
    BAR();
}
template <class PRE>
__device__ __forceinline__ void conv_row(char* smem, int wave, int lane, rsrc_t rs, int id, int fill0, int st, PRE pre) {
    const u32x4 sent = {SENT, SENT, SENT, SENT};
    const u32 off = WS_H + (fill0 < 0 ? 0 : fill0) * WS_HS + id * 8192 + ((wave - 4) * 64 + lane) * 16;
    const u64 t0 = __builtin_amdgcn_s_memrealtime();
    if (fill0 >= 0) { st_dev(rs, off, sent); st_dev(rs, off + 4096, sent); }
    pre();
    if (fill0 >= 0) {
        wait_until(t0 + FILL_GAP);
        st_dev(rs, off + WS_HS, sent); st_dev(rs, off + WS_HS + 4096, sent);
        WAIT_VM(2);
    }
    STAMP(st);
    BAR();
}

__device__ __forceinline__ void row_dma(char* smem, const char* q, const char* k, const char* v, int id, int wave, int lane, int s) {
    const int slot = wave + 8 * s;
    int row = 8 * id + (slot < 12 ? slot - 4 : slot < 20 ? slot - 12 : slot - 20);
    row = row < 0 ? 0 : row;
    const char* src = slot < 12 ? k : slot < 20 ? v : q;
    lds_dma16(src + row * 1024 + lane * 16, (u32)(size_t)(LAS char*)(smem + OFF_R) + slot * R_ROW);
}

__device__ __forceinline__ u32x4 kv_piece(u32 rp, u32x2 sk, int id) {
    u32x2 k0, k1, k2, v0, v1;
    static_assert(4 * R_ROW == 4352, "offsets below");
    asm volatile("ds_read_b64_tr_b16 %0, %5\n\tds_read_b64_tr_b16 %1, %5 offset:4352\n\tds_read_b64_tr_b16 %2, %5 offset:8704\n\t"
                 "ds_read_b64_tr_b16 %3, %5 offset:13056\n\tds_read_b64_tr_b16 %4, %5 offset:17408\n\ts_waitcnt lgkmcnt(0)"
                 : "=&v"(k0), "=&v"(k1), "=&v"(k2), "=&v"(v0), "=&v"(v1) : "v"(rp) : "memory");
    const u32 first = id ? 0xffffffffu : 0u;
    const u32 kp = k0[0] & first, kq = k0[1] & first;
    const float kf[12] = {0.f, bf_hi(kp), bf_lo(kq), bf_hi(kq), bf_lo(k1[0]), bf_hi(k1[0]),
                          bf_lo(k1[1]), bf_hi(k1[1]), bf_lo(k2[0]), bf_hi(k2[0]), bf_lo(k2[1]), bf_hi(k2[1])};
    float vf[8];
    unpack8((u32x4){v0[0], v0[1], v1[0], v1[1]}, vf);
    const float w0 = bf_lo(sk[0]), w1 = bf_hi(sk[0]), w2 = bf_lo(sk[1]), w3 = bf_hi(sk[1]);
    float kv[8];
#pragma unroll
    for (int j = 0; j < 8; ++j) kv[j] = fmaf(kf[j + 4], w0, fmaf(kf[j + 3], w1, fmaf(kf[j + 2], w2, kf[j + 1] * w3))) * vf[j];
    return pack8(kv);
}

__global__ void __launch_bounds__(512, 2)
h3s4_kernel(const char* __restrict__ q, const char* __restrict__ k, const char* __restrict__ v,
            const u32x2* __restrict__ shiftk, const u32x4* __restrict__ ssmk, const u32* __restrict__ ssmD,
            const u16* __restrict__ Are, const u16* __restrict__ Aim, const u16* __restrict__ Bre, const u16* __restrict__ Bim,
            const u16* __restrict__ Cre, const u16* __restrict__ Cim, const u16* __restrict__ logdt, const u32* __restrict__ Dskip,
            u16* __restrict__ out, char* ws
            ) {
    __shared__ __attribute__((aligned(16))) char smem[LDS_BYTES];
    const int tid = threadIdx.x, lane = tid & 63, wave = __builtin_amdgcn_readfirstlane(tid >> 6);
    const int id = (blockIdx.x & 7) * 32 + (blockIdx.x >> 3);
    const rsrc_t rs = __builtin_amdgcn_make_buffer_rsrc(ws, 0, WS_USED, 0x00020000);
    const u32x4 desc = {(u32)(size_t)ws, (u32)((size_t)ws >> 32) & 0xffffu, WS_USED, 0x00020000u};
    STAMP(0);

    u32 code_pf = 0;
    {
        u64 pc; u32 len;
        asm volatile("s_getpc_b64 %0\nh3s4_text_a:\n\ts_mov_b32 %1, h3s4_text_b - h3s4_text_a" : "=s"(pc), "=s"(len));
        const u32 off = ((blockIdx.x >> 3) * 8 + wave) * 256 + lane * 4;
        if (off + 4 <= len) asm volatile("global_load_dword %0, %1, %2" : "=v"(code_pf) : "v"(off), "s"(pc) : "memory");
    }
    const u32x4 sent = {SENT, SENT, SENT, SENT};
    const u32 dA = ssmD[id], dB = Dskip[id];
    MEMFENCE(); SB();
    st_dev(rs, WS_H + 0 * WS_HS + id * 8192 + tid * 16, sent);
    MEMFENCE(); SB();
    if (wave < 4) {
        u32x2 skB = ld_b64(shiftk, (256 + tid) * 8);
        row_dma(smem, q, k, v, id, wave, lane, 0); row_dma(smem, q, k, v, id, wave, lane, 1); row_dma(smem, q, k, v, id, wave, lane, 2);
        MEMFENCE(); SB();
        row_dma(smem, q, k, v, id, wave, lane, 3);
        u32x4 tap_own = ld_b128(ssmk, (2 * id) * 4096 + tid * 16), tap_prev = ld_b128(ssmk, (2 * id) * 4096 + (tid ? tid - 1 : 0) * 16);
        const int pd = 2 * id + (tid >> 7), po = (pd * NS + (tid & 127)) * 2;
        u32 r_dt = ld_u16(logdt, pd * 2), r_are = ld_u16(Are, po), r_aim = ld_u16(Aim, po), r_bre = ld_u16(Bre, po);
        u32 r_bim = ld_u16(Bim, po), r_cre = ld_u16(Cre, po), r_cim = ld_u16(Cim, po);
        u32x4 tix0 = ld_b128(tap_index.off, tid * 16), tix1 = ld_b128(tap_index.off, tid * 16 + 16);
        MEMFENCE(); SB();
        st_dev(rs, WS_H + 1 * WS_HS + id * 8192 + tid * 16, sent);
        MEMFENCE(); SB();
        const u32 epoch = launch_tag(ws);
        asm volatile("s_waitcnt vmcnt(16)\n\ts_barrier" ::: "memory");
        if (tid < 32) __hip_atomic_store((GAS u32*)(ws + WS_FLAGS) + tid * 256 + id, epoch, RLX_AGENT);
        STAMP(19);
        if (tid < 60) *(u32x4*)(smem + OFF_U + (tid / 30) * U_STRIDE + 4096 + (tid % 30) * 16) = (u32x4){0, 0, 0, 0};
        else if (tid >= 64 && tid < 80) *(u32x4*)(smem + OFF_T + (tid - 64) * T_COPY) = (u32x4){0, 0, 0, 0};
        asm volatile("s_waitcnt vmcnt(13)" : "+v"(skB) :: "memory");
        BAR();
        STAMP(1);
        publish(rs, WS_H + id * 8192 + (256 + tid) * 16,
                kv_piece((u32)(size_t)(LAS char*)(smem + OFF_R + ((lane & 15) >> 2) * R_ROW + (256 + 64 * wave + 16 * (lane >> 4) + 4 * (lane & 3)) * 2), skB, id));
        asm volatile("s_waitcnt vmcnt(1)"
                     : "+v"(tap_own), "+v"(tap_prev), "+v"(r_dt), "+v"(r_are), "+v"(r_aim), "+v"(r_bre), "+v"(r_bim), "+v"(r_cre),
                       "+v"(r_cim), "+v"(tix0), "+v"(tix1) :: "memory");
        cnt_arrive(smem, 6, lane);

        {
            const float dt = rbf(__builtin_amdgcn_exp2f(bf_lo(r_dt) * 1.4426950408889634f));
            const float are = dt * bf_lo(r_are), aim = dt * bf_lo(r_aim);
            const float br = dt * bf_lo(r_bre), bi = dt * bf_lo(r_bim);
            const float cr = bf_lo(r_cre), ci = bf_lo(r_cim);
            const float wr = cr * br - ci * bi, wi = cr * bi + ci * br;
            const float ar = fabsf(wr), ai = fabsf(wi), mx = fmaxf(ar, ai), mn = fminf(ar, ai);
            const float ratio = mx > 0.f ? mn * __builtin_amdgcn_rcpf(mx) : 0.f, r2 = ratio * ratio;
            float at = 0.0028662257f;
            at = fmaf(at, r2, -0.0161657367f); at = fmaf(at, r2, 0.0429096138f); at = fmaf(at, r2, -0.0752896400f);
            at = fmaf(at, r2, 0.1065626393f); at = fmaf(at, r2, -0.1420889944f); at = fmaf(at, r2, 0.1999355085f);
            at = fmaf(at, r2, -0.3333314528f); at = fmaf(at, r2, 1.0f);
            float ph = at * ratio * 0.15915494309189535f;
            ph = ai > ar ? 0.25f - ph : ph;
            ph = wr < 0.f ? 0.5f - ph : ph;
            ph = wi < 0.f ? -ph : ph;
            float* p = (float*)(smem + OFF_P) + (tid >> 1) * 8 + (tid & 1);
            p[0] = are * 1.4426950408889634f;
            p[2] = aim * 0.15915494309189535f;
            p[4] = __builtin_amdgcn_logf(mx) + 0.5f * __builtin_amdgcn_logf(r2 + 1.0f);
            p[6] = ph;
        }
        cnt_arrive(smem, 7, lane);
        if (tid == 0) {
            tap_prev = (u32x4){0, 0, 0, 0};
            tap_own[0] = (tap_own[0] & 0xffff0000u) | (pk_bf(bf_lo(tap_own[0]) + bf_lo(dA), 0.f) & 0xffffu);
        }
        stage_taps(smem, 0, tid, tap_prev, tap_own);
        cnt_arrive(smem, 0, lane);
        STAMP(2);
        if (C_PRE) {
            cnt_wait(smem, 7, 4);
            KState ks;
            kgen_init(ks);
            kgen_steps<C_PRE, true>(kgen_params(smem, wave, 0), lane, ks);
            kgen_save(smem, wave, lane, ks);
        }

        const ConvAddr ca = conv_addr(smem, wave, lane);
        asm volatile("" :: "v"(code_pf));
        cnt_wait(smem, 0, 8);
        STAMP(4);
#pragma nounroll
        for (u32 seq = 1; seq <= 3; ++seq) {
            if (seq == 2) {
                {
                    KState ks;
                    if (C_PRE) kgen_load(smem, wave, lane, ks); else kgen_init(ks);
                    kgen_steps<4 - R_STEPS - C_PRE, true>(kgen_params(smem, wave, C_PRE), lane, ks);
                    kgen_save(smem, wave, lane, ks);
                }
                STAMP(7);
                cnt_arrive(smem, 1, lane);
                cnt_wait(smem, 1, 8);
                kgen_reduce(smem, wave, lane, dB);
                cnt_arrive(smem, 2, lane);
                cnt_wait(smem, 2, 4);
                STAMP(23);
                s4_taps(smem, 0, tid, tix0, tix1);
                cnt_arrive(smem, 3, lane);
                cnt_wait(smem, 3, 8);
                STAMP(25);
            }
            conv_col<true>(smem, ca, wave, lane, seq);
            STAMP(seq == 1 ? 6 : seq == 2 ? 11 : 15);
        }
    } else {
        const int r = tid - 256, w = wave - 4;
        u32x2 skA = ld_b64(shiftk, r * 8);
        row_dma(smem, q, k, v, id, wave, lane, 0); row_dma(smem, q, k, v, id, wave, lane, 1);
        MEMFENCE(); SB();
        row_dma(smem, q, k, v, id, wave, lane, 2);
        u32x4 tap_own = ld_b128(ssmk, (2 * id + 1) * 4096 + r * 16), tap_prev = ld_b128(ssmk, (2 * id + 1) * 4096 + (r ? r - 1 : 0) * 16);
        u32x4 tix0 = ld_b128(tap_index.off, r * 16), tix1 = ld_b128(tap_index.off, r * 16 + 16);
        MEMFENCE(); SB();
        st_dev(rs, WS_H + 1 * WS_HS + id * 8192 + tid * 16, sent);
        MEMFENCE(); SB();
        const u32 epoch = launch_tag(ws);
        asm volatile("s_waitcnt vmcnt(8)\n\ts_barrier" ::: "memory");
        ((u32*)(smem + OFF_RED))[r] = SENT;
        if (r < 16) ((u32*)(smem + OFF_CNT))[r] = 0u;
        asm volatile("s_waitcnt vmcnt(6)" : "+v"(skA) :: "memory");
        BAR();
        STAMP(1);

        const u32 own_a = id * 8192 + r * 16, own_b = own_a + 4096;
        const u32 col_a = (r >> 1) * 8192 + (2 * id + (r & 1)) * 16, col_b = col_a + 128 * 8192;
        const u32 row_a = (r >> 1) * 8192 + id * 32 + (r & 1) * 16, row_b = row_a + 128 * 8192;
        char* const u_dst = smem + OFF_U + (r & 1) * U_STRIDE + 4080 - (r >> 1) * 16;
        const char* const y_src = smem + OFF_Y + (r & 1) * 4096 + (r >> 1) * 16;
        float* red = (float*)(smem + OFF_RED);

        Rows x, u0;
        u64 t_s;
        const u32 flag_a = WS_FLAGS + (blockIdx.x & 31) * 1024 + (r >> 1) * 4;
        u32 fl_a, fl_b;
        const u32 rp = (u32)(size_t)(LAS char*)(smem + OFF_R + ((lane & 15) >> 2) * R_ROW + (64 * w + 16 * (lane >> 4) + 4 * (lane & 3)) * 2);
        publish(rs, WS_H + own_a, kv_piece(rp, skA, id));
        t_s = __builtin_amdgcn_s_memrealtime();
        __builtin_amdgcn_s_setprio(2);
        asm volatile("s_waitcnt vmcnt(1)" : "+v"(tap_own), "+v"(tap_prev), "+v"(tix0), "+v"(tix1) :: "memory");
        fl_a = ld_flag(ws, flag_a); fl_b = ld_flag(ws, flag_a + 512);
        cnt_arrive(smem, 6, lane);
        STAMP(2);
        u32 touch = 0;
        if (wave == 6) {
            if (lane < 4) touch = __builtin_amdgcn_raw_buffer_load_b32(rs, WS_H + (2 + lane) * WS_HS + id * 8192, 0, 16);
            else if (lane == 4) touch = ((const u32*)out)[id * 2048];
        }
        if (r == 0) {
            tap_prev = (u32x4){0, 0, 0, 0};
            tap_own[0] = (tap_own[0] & 0xffff0000u) | (pk_bf(bf_lo(tap_own[0]) + bf_hi(dA), 0.f) & 0xffffu);
        }
        stage_taps(smem, 1, r, tap_prev, tap_own);
        cnt_arrive(smem, 0, lane);
        STAMP(3);

        Piece2 pc, pr;
        Rows yA, u;
        auto stage_input = [&](u32 h) {
            poll2_finish(pc, desc, WS_H + h * WS_HS + col_a, WS_H + h * WS_HS + col_b);
            *(u32x4*)u_dst = reverse8(pc.a);
            *(u32x4*)(u_dst - 2048) = reverse8(pc.b);
        };
        auto send_result = [&](u32 h) {
            publish(rs, WS_H + h * WS_HS + own_a, *(const u32x4*)y_src);
            publish(rs, WS_H + h * WS_HS + own_b, *(const u32x4*)(y_src + 2048));
            t_s = __builtin_amdgcn_s_memrealtime();
        };
        auto fetch_rows = [&](u32 h) {
            wait_until(t_s + POLL_DELAY);
            pr = poll2_issue(rs, WS_H + h * WS_HS + row_a, WS_H + h * WS_HS + row_b);
            poll2_finish(pr, desc, WS_H + h * WS_HS + row_a, WS_H + h * WS_HS + row_b);
        };

        wait_until(t_s + POLL_DELAY);
        {
            asm volatile("" :: "v"(fl_a), "v"(fl_b));
            const bool ok = fl_a == epoch && fl_b == epoch;
            pc = poll2_issue(rs, WS_H + col_a, WS_H + col_b);
            poll2_finish_flagged(pc, desc, WS_H + col_a, WS_H + col_b, flag_a, flag_a + 512, ok, epoch);
            *(u32x4*)u_dst = reverse8(pc.a);
            *(u32x4*)(u_dst - 2048) = reverse8(pc.b);
        }
        STAMP(21);
        asm volatile("" :: "v"(touch));
        asm volatile("" :: "v"(code_pf));
        BAR();
        STAMP(5);
        if (blockIdx.x == 0 && r == 0) __hip_atomic_store((GAS u32*)(ws + WS_EPOCH), epoch, RLX_AGENT);
        conv_row(smem, wave, lane, rs, id, 2, 27, [&] {
            cnt_wait(smem, 6, 8);
            if (wave == 7 && lane < 32) __hip_atomic_store((GAS u32*)(ws + WS_FLAGS + 32768) + lane * 256 + id, epoch, RLX_AGENT);
            u32x2 qq[2][2];
            asm volatile("ds_read_b64_tr_b16 %0, %4 offset:21760\n\tds_read_b64_tr_b16 %1, %4 offset:26112\n\t"
                         "ds_read_b64_tr_b16 %2, %4 offset:22272\n\tds_read_b64_tr_b16 %3, %4 offset:26624\n\ts_waitcnt lgkmcnt(0)"
                         : "=&v"(qq[0][0]), "=&v"(qq[0][1]), "=&v"(qq[1][0]), "=&v"(qq[1][1]) : "v"(rp) : "memory");
            unpack_rows((u32x4){qq[0][0][0], qq[0][0][1], qq[0][1][0], qq[0][1][1]}, x.a);
            unpack_rows((u32x4){qq[1][0][0], qq[1][0][1], qq[1][1][0], qq[1][1][1]}, x.b);
            __builtin_amdgcn_s_setprio(0);
            KState ks;
            kgen_init(ks);
            kgen_steps<R_STEPS, false>(kgen_params(smem, wave, 4 - R_STEPS), lane, ks);
            kgen_save(smem, wave, lane, ks);
            cnt_arrive(smem, 1, lane);
            __builtin_amdgcn_s_setprio(2);
        });
        send_result(1);
        STAMP(6);

#pragma nounroll
        for (u32 it = 0; it < 2; ++it) {
            MEMFENCE();
            const u32 fb_a = ld_flag(ws, flag_a + 32768), fb_b = ld_flag(ws, flag_a + 32768 + 512);
            MEMFENCE();
            layer_norm(x, u0, red + 128 * it, lane, w);
            if (it == 0) {
                wait_until(t_s + POLL_DELAY);
                asm volatile("" :: "v"(fb_a), "v"(fb_b));
                const bool ok = fb_a == epoch && fb_b == epoch;
                pr = poll2_issue(rs, WS_H + WS_HS + row_a, WS_H + WS_HS + row_b);
                poll2_finish_flagged(pr, desc, WS_H + WS_HS + row_a, WS_H + WS_HS + row_b, flag_a + 32768, flag_a + 32768 + 512, ok, epoch);
                STAMP(8);
                unpack_rows(pr.a, yA.a);
                unpack_rows(pr.b, yA.b);
            }
#pragma unroll
            for (int i = 0; i < 4; ++i) { x.a[i] += u0.a[i] * yA.a[i]; x.b[i] += u0.b[i] * yA.b[i]; }
            round_rows(x.a);
            round_rows(x.b);
            layer_norm(x, u, red + 128 * it + 64, lane, w);
            publish(rs, WS_H + (2 + 2 * it) * WS_HS + own_a, pack_rows(u.a));
            publish(rs, WS_H + (2 + 2 * it) * WS_HS + own_b, pack_rows(u.b));
            t_s = __builtin_amdgcn_s_memrealtime();
            STAMP(it ? 13 : 9);

            wait_until(t_s + POLL_DELAY);
            pc = poll2_issue(rs, WS_H + (2 + 2 * it) * WS_HS + col_a, WS_H + (2 + 2 * it) * WS_HS + col_b);
            if (it == 0) {
                cnt_wait(smem, 2, 4);
                s4_taps(smem, 1, r, tix0, tix1);
                cnt_arrive(smem, 3, lane);
            }
            stage_input(2 + 2 * it);
            BAR();
            STAMP(it ? 14 : 10);
            conv_row(smem, wave, lane, rs, id, it ? -1 : 4, 28, [] {});
            send_result(3 + 2 * it);
            STAMP(it ? 15 : 11);

            fetch_rows(3 + 2 * it);
            STAMP(it ? 16 : 12);
            Rows y;
            unpack_rows(pr.a, y.a);
            unpack_rows(pr.b, y.b);
#pragma unroll
            for (int i = 0; i < 4; ++i) { x.a[i] += y.a[i]; x.b[i] += y.b[i]; }
            pr.a = round_rows(x.a);
            pr.b = round_rows(x.b);
        }
        {
            u16* dst = out + (8 * id) * DM + r;
#pragma unroll
            for (int j = 0; j < 4; ++j) {
                dst[(2 * j) * DM] = (u16)pr.a[j];
                dst[(2 * j) * DM + 256] = (u16)pr.b[j];
                dst[(2 * j + 1) * DM] = (u16)(pr.a[j] >> 16);
                dst[(2 * j + 1) * DM + 256] = (u16)(pr.b[j] >> 16);
            }
        }
        STAMP(17);
    }
    asm volatile("h3s4_text_b:");
}

extern "C" void kernel_launch(void* const* d_in, const int* in_sizes, int n_in,
                              void* d_out, int out_size, void* d_ws, size_t ws_size,
                              hipStream_t stream) {
    h3s4_kernel<<<256, 512, 0, stream>>>(
        (const char*)d_in[0], (const char*)d_in[1], (const char*)d_in[2], (const u32x2*)d_in[3], (const u32x4*)d_in[4],
        (const u32*)d_in[5], (const u16*)d_in[7], (const u16*)d_in[8], (const u16*)d_in[9], (const u16*)d_in[10],
        (const u16*)d_in[11], (const u16*)d_in[12], (const u16*)d_in[13], (const u32*)d_in[14], (u16*)d_out, (char*)d_ws
        );
}
```

```cpp
#define DIAG 0
#define DIAG_A 0
#define DIAG_B 0
#include <hip/hip_runtime.h>
#include <hip/hip_bf16.h>
#include <stdint.h>

typedef unsigned int u32;
typedef unsigned short u16;
typedef unsigned long long u64;
typedef __attribute__((ext_vector_type(4))) float f32x4;
typedef __attribute__((ext_vector_type(2))) float f32x2;
typedef __attribute__((ext_vector_type(4))) u32 u32x4;
typedef __attribute__((ext_vector_type(2))) u32 u32x2;
typedef __attribute__((ext_vector_type(4))) short s16x4;
typedef __attribute__((ext_vector_type(8))) __bf16 bf16x8;
typedef __attribute__((ext_vector_type(2))) __bf16 bf16x2;
#define LAS __attribute__((address_space(3)))
#define GAS __attribute__((address_space(1)))
#define RLX_AGENT __ATOMIC_RELAXED, __HIP_MEMORY_SCOPE_AGENT
#define SB() __builtin_amdgcn_sched_barrier(0)
#define BAR() asm volatile("s_waitcnt lgkmcnt(0)\n\ts_barrier" ::: "memory")
#define MEMFENCE() asm volatile("" ::: "memory")
#define WAIT_VM(n) do { MEMFENCE(); __builtin_amdgcn_s_waitcnt(0x0F70 | ((n) & 15) | (((n) >> 4) << 14)); MEMFENCE(); } while (0)

constexpr int DM = 512;
constexpr int NS = 128;
constexpr u32 SENT = 0xffffffffu;
constexpr u32 SPIN_CAP = 20000;
#define FILL_GAP 70
#define C_PRE 1
#define R_STEPS 1
#define POLL_DELAY 45

constexpr u32 WS_EPOCH = 0;
constexpr u32 WS_FLAGS = 4096;
constexpr u32 WS_H = 1u << 20;
constexpr u32 WS_HS = 2u << 20;
constexpr u32 WS_USED = WS_H + 6 * WS_HS;

constexpr int T_COPY = 4160, T_CH = 8 * T_COPY;
constexpr int U_STRIDE = 4672;
constexpr int R_ROW = 1088;
constexpr int KP_WAVE = 2816;
constexpr int OFF_T = 0;
constexpr int OFF_U = OFF_T + 2 * T_CH;
constexpr int OFF_X = OFF_U + 2 * U_STRIDE;
constexpr int OFF_Y = OFF_X + 16384;
constexpr int OFF_R = OFF_Y + 8192;
constexpr int OFF_P = OFF_R + 28 * R_ROW;
constexpr int OFF_KP = OFF_P + 4096;
constexpr int OFF_KC = OFF_KP + 8 * KP_WAVE;
constexpr int OFF_RED = OFF_KC + 2 * 648 * 2;
constexpr int OFF_CNT = OFF_RED + 1024;
constexpr int OFF_ST = OFF_CNT + 64;
constexpr int LDS_BYTES = OFF_ST + 512;
static_assert(LDS_BYTES <= 163840, "LDS");

#define STAMP(i) do { if ((threadIdx.x & 255) == 0) ((u64*)(smem + OFF_ST))[(i) + (threadIdx.x ? 32 : 0)] = __builtin_amdgcn_s_memrealtime(); } while (0)

__device__ __forceinline__ float bf_lo(u32 p) { return __builtin_bit_cast(float, p << 16); }
__device__ __forceinline__ float bf_hi(u32 p) { return __builtin_bit_cast(float, p & 0xffff0000u); }
__device__ __forceinline__ float bf_u16(u16 p) { return __builtin_bit_cast(float, (u32)p << 16); }
__device__ __forceinline__ u32 pk_bf(float lo, float hi) {
    bf16x2 v = {(__bf16)lo, (__bf16)hi};
    return __builtin_bit_cast(u32, v);
}
__device__ __forceinline__ float as_f(u32 x) { return __builtin_bit_cast(float, x); }
__device__ __forceinline__ u32 as_u(float x) { return __builtin_bit_cast(u32, x); }
__device__ __forceinline__ float rbf(float x) { return bf_lo(pk_bf(x, x)); }
__device__ __forceinline__ void unpack8(u32x4 p, float (&f)[8]) {
#pragma unroll
    for (int i = 0; i < 4; ++i) { f[2 * i] = bf_lo(p[i]); f[2 * i + 1] = bf_hi(p[i]); }
}
__device__ __forceinline__ u32x4 pack8(const float (&f)[8]) {
    return (u32x4){pk_bf(f[0], f[1]), pk_bf(f[2], f[3]), pk_bf(f[4], f[5]), pk_bf(f[6], f[7])};
}
__device__ __forceinline__ u32x4 reverse8(u32x4 p) {
    return (u32x4){__builtin_amdgcn_alignbit(p[3], p[3], 16), __builtin_amdgcn_alignbit(p[2], p[2], 16),
                   __builtin_amdgcn_alignbit(p[1], p[1], 16), __builtin_amdgcn_alignbit(p[0], p[0], 16)};
}
template <int CTRL> __device__ __forceinline__ float dppf(float v) {
    return __builtin_bit_cast(float, __builtin_amdgcn_update_dpp(0, __builtin_bit_cast(int, v), CTRL, 0xf, 0xf, true));
}

__device__ __forceinline__ void lds_dma16(const char* src, u32 lds) {
    u32 keep;
    asm volatile("s_mov_b32 %0, m0\n\ts_mov_b32 m0, %2\n\ts_nop 0\n\tglobal_load_lds_dwordx4 %1, off\n\ts_mov_b32 m0, %0"
                 : "=&s"(keep) : "v"(src), "s"(lds) : "memory");
}

__device__ __forceinline__ u32 ld_u16(const void* base, u32 off) {
    u32 r;
    asm volatile("global_load_ushort %0, %1, %2" : "=v"(r) : "v"(off), "s"(base) : "memory");
    return r;
}
__device__ __forceinline__ u32x2 ld_b64(const void* base, u32 off) {
    u32x2 r;
    asm volatile("global_load_dwordx2 %0, %1, %2" : "=v"(r) : "v"(off), "s"(base) : "memory");
    return r;
}
__device__ __forceinline__ u32x4 ld_b128(const void* base, u32 off) {
    u32x4 r;
    asm volatile("global_load_dwordx4 %0, %1, %2" : "=v"(r) : "v"(off), "s"(base) : "memory");
    return r;
}

typedef __amdgpu_buffer_rsrc_t rsrc_t;
__device__ __forceinline__ u32x4 ld_dev(rsrc_t rs, u32 off) { return __builtin_amdgcn_raw_buffer_load_b128(rs, off, 0, 16); }
__device__ __forceinline__ void st_dev(rsrc_t rs, u32 off, u32x4 v) { __builtin_amdgcn_raw_buffer_store_b128(v, rs, off, 0, 16); }
__device__ __forceinline__ void publish(rsrc_t rs, u32 off, u32x4 v) {
#pragma unroll
    for (int i = 0; i < 4; ++i) v[i] = v[i] < SENT - 1u ? v[i] : SENT - 1u;
    st_dev(rs, off, v);
}
__device__ __forceinline__ u32 launch_tag(const char* ws) {
    u32 e;
    asm volatile("s_load_dword %0, %1, 0x0\n\ts_waitcnt lgkmcnt(0)" : "=&s"(e) : "s"(ws + WS_EPOCH) : "memory");
    return e + 1u;
}
__device__ __forceinline__ void wait_until(u64 t) {
    while (__builtin_amdgcn_s_memrealtime() < t) __builtin_amdgcn_s_sleep(1);
}
__device__ __forceinline__ u32x4 poll_issue(rsrc_t rs, u32 off) {
    MEMFENCE();
    u32x4 x = ld_dev(rs, off);
    MEMFENCE();
    return x;
}
struct Piece2 { u32x4 a, b; };
__device__ __forceinline__ Piece2 poll2_issue(rsrc_t rs, u32 off_a, u32 off_b) {
    MEMFENCE();
    Piece2 p = {ld_dev(rs, off_a), ld_dev(rs, off_b)};
    MEMFENCE();
    return p;
}
__device__ __forceinline__ bool has_sentinel(const u32x4& x) {
    u32 mx = x[0] > x[1] ? x[0] : x[1];
    mx = mx > x[2] ? mx : x[2];
    mx = mx > x[3] ? mx : x[3];
    return mx == SENT;
}
__device__ __forceinline__ void poll2_finish(Piece2& p, u32x4 desc, u32 off_a, u32 off_b) {
#pragma nounroll
    for (u32 spins = 0; spins < SPIN_CAP; ++spins) {
        const bool bad = has_sentinel(p.a) || has_sentinel(p.b);
        if (__builtin_amdgcn_ballot_w64(bad) == 0ull) break;
        __builtin_amdgcn_s_sleep(1);
        if (bad) asm volatile("buffer_load_dwordx4 %0, %2, %4, 0 offen sc1\n\tbuffer_load_dwordx4 %1, %3, %4, 0 offen sc1\n\ts_waitcnt vmcnt(0)"
                              : "+v"(p.a), "+v"(p.b) : "v"(off_a), "v"(off_b), "s"(desc) : "memory");
    }
}
__device__ __forceinline__ u32 ld_flag(const char* ws, u32 off) { return __hip_atomic_load((const GAS u32*)(ws + off), RLX_AGENT); }
__device__ __forceinline__ void poll2_finish_flagged(Piece2& p, u32x4 desc, u32 off_a, u32 off_b, u32 fo_a, u32 fo_b, bool ok, u32 epoch) {
#pragma nounroll
    for (u32 spins = 0; spins < SPIN_CAP; ++spins) {
        const bool bad = !ok || has_sentinel(p.a) || has_sentinel(p.b);
        if (__builtin_amdgcn_ballot_w64(bad) == 0ull) break;
        if (!ok) {
            u32 fa, fb;
            asm volatile("buffer_load_dword %0, %2, %4, 0 offen sc1\n\tbuffer_load_dword %1, %3, %4, 0 offen sc1\n\ts_waitcnt vmcnt(0)"
                         : "=&v"(fa), "=&v"(fb) : "v"(fo_a), "v"(fo_b), "s"(desc) : "memory");
            ok = fa == epoch && fb == epoch;
        }
        if (bad) asm volatile("buffer_load_dwordx4 %0, %2, %4, 0 offen sc1\n\tbuffer_load_dwordx4 %1, %3, %4, 0 offen sc1\n\ts_waitcnt vmcnt(0)"
                              : "+v"(p.a), "+v"(p.b) : "v"(off_a), "v"(off_b), "s"(desc) : "memory");
    }
}

__device__ __forceinline__ void cnt_arrive(char* smem, int k, int lane) {
    asm volatile("s_waitcnt lgkmcnt(0)" ::: "memory");
    if (lane == 0) __hip_atomic_fetch_add((LAS u32*)(smem + OFF_CNT) + k, 1u, __ATOMIC_RELAXED, __HIP_MEMORY_SCOPE_WORKGROUP);
}
__device__ __forceinline__ void cnt_wait(char* smem, int k, u32 target) {
    u32 spins = 0;
    while (__hip_atomic_load((LAS u32*)(smem + OFF_CNT) + k, __ATOMIC_RELAXED, __HIP_MEMORY_SCOPE_WORKGROUP) < target) {
        if (++spins > 50 * SPIN_CAP) break;
        __builtin_amdgcn_s_sleep(1);
    }
    MEMFENCE();
}

struct Rows { f32x2 a[4], b[4]; };
__device__ __forceinline__ void unpack_rows(u32x4 p, f32x2 (&f)[4]) {
#pragma unroll
    for (int i = 0; i < 4; ++i) f[i] = (f32x2){bf_lo(p[i]), bf_hi(p[i])};
}
__device__ __forceinline__ u32x4 pack_rows(const f32x2 (&f)[4]) {
    return (u32x4){pk_bf(f[0][0], f[0][1]), pk_bf(f[1][0], f[1][1]), pk_bf(f[2][0], f[2][1]), pk_bf(f[3][0], f[3][1])};
}
__device__ __forceinline__ u32x4 round_rows(f32x2 (&f)[4]) {
    u32x4 p = pack_rows(f);
    asm("" : "+v"(p));
    unpack_rows(p, f);
    return p;
}
__device__ __forceinline__ void layer_norm(const Rows& x, Rows& u, float* red, int lane, int w) {
#pragma clang fp contract(fast)
    u32 a[8], b[8];
#pragma unroll
    for (int i = 0; i < 4; ++i) {
        const f32x2 s1 = x.a[i] + x.b[i], s2 = x.a[i] * x.a[i] + x.b[i] * x.b[i];
        a[2 * i] = as_u(s1[0]); a[2 * i + 1] = as_u(s1[1]);
        b[2 * i] = as_u(s2[0]); b[2 * i + 1] = as_u(s2[1]);
    }
    SB();
#pragma unroll
    for (int j = 0; j < 8; ++j) {
        const auto s = __builtin_amdgcn_permlane32_swap(a[j], b[j], false, false);
        a[j] = s[0]; b[j] = s[1];
    }
    SB();
    float r[8];
#pragma unroll
    for (int j = 0; j < 8; ++j) r[j] = as_f(a[j]) + as_f(b[j]);
    SB();
#pragma unroll
    for (int j = 0; j < 4; ++j) {
        const auto s = __builtin_amdgcn_permlane16_swap(as_u(r[j]), as_u(r[j + 4]), false, false);
        a[j] = s[0]; b[j] = s[1];
    }
    SB();
    float s4[4];
#pragma unroll
    for (int j = 0; j < 4; ++j) s4[j] = as_f(a[j]) + as_f(b[j]);
    SB();
#pragma unroll
    for (int j = 0; j < 4; ++j) s4[j] += dppf<0x128>(s4[j]);
    const float t0 = (lane & 8) ? s4[2] : s4[0], t1 = (lane & 8) ? s4[3] : s4[1];
    const float c0 = t0 + dppf<0x141>(t0), c1 = t1 + dppf<0x141>(t1);
    float s = (lane & 4) ? c1 : c0;
    s += dppf<0xB1>(s);
    s += dppf<0x4E>(s);
    const u32 sb = as_u(s) < SENT - 1u ? as_u(s) : SENT - 1u;
    if ((lane & 3) == 0) ((volatile LAS u32*)(LAS char*)(char*)red)[(lane >> 2) * 4 + w] = sb;
    u32x4 p;
    u32 spins = 0;
    while (true) {
        p = *(volatile LAS u32x4*)((LAS char*)(char*)red + (lane & 15) * 16);
        u32 mx = p[0] > p[1] ? p[0] : p[1];
        mx = mx > p[2] ? mx : p[2];
        mx = mx > p[3] ? mx : p[3];
        if (__builtin_amdgcn_ballot_w64(mx == SENT) == 0ull) break;
        if (++spins > 50 * SPIN_CAP) break;
    }
    const float m = ((as_f(p[0]) + as_f(p[1])) + (as_f(p[2]) + as_f(p[3]))) * (1.0f / DM);
    const float ex2 = dppf<0x128>(m);
    const float rstd = __builtin_amdgcn_rsqf(fmaxf(fmaf(-m, m, ex2), 0.f) + 1e-5f);
    const float off = -m * rstd;
    const f32x2 R[4] = {{dppf<0x150>(rstd), dppf<0x151>(rstd)}, {dppf<0x152>(rstd), dppf<0x153>(rstd)},
                        {dppf<0x154>(rstd), dppf<0x155>(rstd)}, {dppf<0x156>(rstd), dppf<0x157>(rstd)}};
    const f32x2 O[4] = {{dppf<0x150>(off), dppf<0x151>(off)}, {dppf<0x152>(off), dppf<0x153>(off)},
                        {dppf<0x154>(off), dppf<0x155>(off)}, {dppf<0x156>(off), dppf<0x157>(off)}};
#pragma unroll
    for (int i = 0; i < 4; ++i) { u.a[i] = x.a[i] * R[i] + O[i]; u.b[i] = x.b[i] * R[i] + O[i]; }
}

__device__ __forceinline__ void stage_taps(char* smem, int ch, int i, u32x4 prev, u32x4 own) {
    const u32 D[8] = {prev[0], prev[1], prev[2], prev[3], own[0], own[1], own[2], own[3]};
    u32 O[7];
#pragma unroll
    for (int y = 0; y < 7; ++y) O[y] = __builtin_amdgcn_alignbit(D[y + 1], D[y], 16);
    char* base = smem + OFF_T + ch * T_CH + (i + 1) * 16;
#pragma unroll
    for (int c = 0; c < 8; ++c) {
        const int y = (c + 1) >> 1;
        const u32x4 v = (c & 1) ? (u32x4){D[y], D[y + 1], D[y + 2], D[y + 3]} : (u32x4){O[y], O[y + 1], O[y + 2], O[y + 3]};
        *(u32x4*)(base + c * T_COPY) = v;
    }
}

__device__ __forceinline__ f32x4 mfma16(u32x4 a, u32x4 b, f32x4 c) {
    return __builtin_amdgcn_mfma_f32_16x16x32_bf16(__builtin_bit_cast(bf16x8, a), __builtin_bit_cast(bf16x8, b), c, 0, 0, 0);
}
struct KState { f32x4 acc[3]; };
__device__ __forceinline__ void kgen_init(KState& ks) {
#pragma unroll
    for (int e = 0; e < 3; ++e) ks.acc[e] = (f32x4){0, 0, 0, 0};
}
#define OPQ(x) asm volatile("" : "+v"(x))
template <int NS_, bool PK>
__device__ __forceinline__ void kgen_steps(const f32x4* P, int lane, KState& ks) {
#pragma clang fp contract(fast)
    const int g = lane >> 4, m = lane & 15;
    const float lo = (float)(8 * m);
    const float hi[3] = {(float)(128 * (m >> 3) + (m & 7)),
                         (float)(m < 8 ? 256 + 128 * (m >> 2) + 2 * (m & 3) : 512 + 128 * ((m - 8) >> 1) + 4 * (m & 1)),
                         (float)(m < 8 ? 1024 + 128 * m : 2048)};
    auto eval = [&](f32x2 ex, f32x2 an, bool conj, u32& o0, u32& o1) {
        if (!PK) { OPQ(ex[0]); OPQ(ex[1]); OPQ(an[0]); OPQ(an[1]); }
        const float m0 = __builtin_amdgcn_exp2f(ex[0]), m1 = __builtin_amdgcn_exp2f(ex[1]);
        const float f0 = __builtin_amdgcn_fractf(an[0]), f1 = __builtin_amdgcn_fractf(an[1]);
        const float c0 = __builtin_amdgcn_cosf(f0), c1 = __builtin_amdgcn_cosf(f1);
        const float s0 = __builtin_amdgcn_sinf(f0), s1 = __builtin_amdgcn_sinf(f1);
        float r0, r1, i0, i1;
        if (PK) {
            const f32x2 r = (f32x2){c0, c1} * (f32x2){m0, m1}, i = (f32x2){s0, s1} * (f32x2){m0, m1};
            r0 = r[0]; r1 = r[1]; i0 = i[0]; i1 = i[1];
        } else {
            r0 = c0 * m0; OPQ(r0); i0 = s0 * m0; OPQ(i0); r1 = c1 * m1; OPQ(r1); i1 = s1 * m1; OPQ(i1);
        }
        o0 = pk_bf(r0, conj ? -i0 : i0);
        o1 = pk_bf(r1, conj ? -i1 : i1);
    };
    auto affine = [&](f32x2 a, float p, f32x2 b) -> f32x2 {
        if (PK) return a * p + b;
        float x = fmaf(a[0], p, b[0]), y = fmaf(a[1], p, b[1]);
        OPQ(x); OPQ(y);
        return (f32x2){x, y};
    };
#pragma unroll
    for (int s = 0; s < NS_; ++s) {
        u32x4 B, A[3];
#pragma unroll
        for (int h = 0; h < 2; ++h) {
            const f32x4 p0 = P[16 * s + 4 * g + 2 * h], p1 = P[16 * s + 4 * g + 2 * h + 1];
            u32 o0, o1;
            eval(affine(p0.xy, lo, (f32x2){0.f, 0.f}), affine(p0.zw, lo, (f32x2){0.f, 0.f}), true, o0, o1);
            B[2 * h] = o0; B[2 * h + 1] = o1;
#pragma unroll
            for (int e = 0; e < 3; ++e) {
                eval(affine(p0.xy, hi[e], p1.xy), affine(p0.zw, hi[e], p1.zw), false, o0, o1);
                A[e][2 * h] = o0; A[e][2 * h + 1] = o1;
            }
        }
#pragma unroll
        for (int e = 0; e < 3; ++e) ks.acc[e] = mfma16(A[e], B, ks.acc[e]);
    }
}
__device__ __forceinline__ const f32x4* kgen_params(char* smem, int wave, int step) {
    return (const f32x4*)(smem + OFF_P) + ((wave >> 1) & 1) * 128 + ((wave & 1) * 4 + step) * 16;
}
__device__ __forceinline__ void kgen_save(char* smem, int wave, int lane, const KState& ks) {
    char* p = smem + OFF_KP + wave * KP_WAVE + lane * 16;
    *(f32x4*)p = ks.acc[0];
    *(f32x4*)(p + 1024) = ks.acc[1];
    if (lane < 48) *(f32x4*)(p + 2048) = ks.acc[2];
}
__device__ __forceinline__ void kgen_load(const char* smem, int wave, int lane, KState& ks) {
    const char* p = smem + OFF_KP + wave * KP_WAVE;
    ks.acc[0] = *(const f32x4*)(p + lane * 16);
    ks.acc[1] = *(const f32x4*)(p + 1024 + lane * 16);
    ks.acc[2] = *(const f32x4*)(p + 2048 + (lane < 48 ? lane : 0) * 16);
}
__device__ __forceinline__ f32x4 kgen_sum(const char* p) {
    return (*(const f32x4*)p + *(const f32x4*)(p + KP_WAVE)) + (*(const f32x4*)(p + 4 * KP_WAVE) + *(const f32x4*)(p + 5 * KP_WAVE));
}
__device__ __forceinline__ void kgen_reduce(char* smem, int wave, int lane, u32 dskip) {
    const int c = wave >> 1, j = lane & 15, g = lane >> 4;
    char* KC = smem + OFF_KC + c * 1296;
    const char* p = smem + OFF_KP + 2 * c * KP_WAVE + lane * 16;
    if ((wave & 1) == 0) {
        f32x4 s = kgen_sum(p);
        const f32x4 t = kgen_sum(smem + OFF_KP + 2 * c * KP_WAVE + 2048 + (lane < 48 ? lane : 0) * 16);
        if (lane == 0) s[0] += c ? bf_hi(dskip) : bf_lo(dskip);
        *(u32x2*)(KC + 2 * (128 * (g >> 1) + 4 * (g & 1) + 8 * j)) = (u32x2){pk_bf(s[0], s[1]), pk_bf(s[2], s[3])};
        const u32 lo = pk_bf(t[0], t[1]), hi = pk_bf(t[2], t[3]);
        u16* o = (u16*)KC + 512 + 64 * g + j;
        if (g < 2) { o[0] = (u16)lo; o[16] = (u16)(lo >> 16); o[32] = (u16)hi; o[48] = (u16)(hi >> 16); }
        else if (lane == 32) { ((u16*)KC)[640] = (u16)lo; ((u16*)KC)[641] = 0; }
    } else {
        const f32x4 s = kgen_sum(p + 1024);
        const u32 lo = pk_bf(s[0], s[1]), hi = pk_bf(s[2], s[3]);
        if (g < 2) *(u32x2*)(KC + 2 * (256 + 64 * g + 4 * j)) = (u32x2){lo, hi};
        else {
            u32* o = (u32*)(KC + 2 * (384 + 64 * (g - 2) + 2 * j));
            o[0] = lo; o[16] = hi;
        }
    }
}

struct TapIndex {
    u16 off[2048 + 8];
    constexpr TapIndex() : off() {
        for (int i = 0; i < 8; ++i) off[i] = 641 * 2;
        for (int l = 0; l < 2048; ++l) {
            int e = 0;
            while ((l >> e) >= 256) ++e;
            int v = l >> e;
            if (e > 0) {
                const int rem = l & ((1 << e) - 1), half = 1 << (e - 1);
                if (rem > half || (rem == half && (v & 1))) ++v;
            }
            off[8 + l] = (u16)(2 * (128 * e + v));
        }
    }
};
__device__ const TapIndex tap_index = TapIndex();

__device__ __forceinline__ void s4_taps(char* smem, int ch, int i, u32x4 tix0, u32x4 tix1) {
    const char* kc = smem + OFF_KC + ch * 1296;
    u32 d[8];
#pragma unroll
    for (int j = 0; j < 8; ++j) {
        const u32 o = j < 4 ? tix0[j] : tix1[j - 4];
        d[j] = (u32)*(const u16*)(kc + (o & 0xffffu)) | ((u32)*(const u16*)(kc + (o >> 16)) << 16);
    }
    stage_taps(smem, ch, i, (u32x4){d[0], d[1], d[2], d[3]}, (u32x4){d[4], d[5], d[6], d[7]});
}

struct Frags { u32x4 A[8], B[8]; };
template <bool INIT> __device__ __forceinline__ void mma(f32x4& acc, const u32x4& a, const u32x4& b) {
    if constexpr (INIT) asm volatile("v_mfma_f32_16x16x32_bf16 %0, %1, %2, 0" : "=&v"(acc) : "v"(a), "v"(b));
    else asm volatile("v_mfma_f32_16x16x32_bf16 %0, %1, %2, %0" : "+v"(acc) : "v"(a), "v"(b));
}
template <int I> __device__ __forceinline__ void load_one(Frags& f, const char* ap, const char* bp) {
    if constexpr (I == 0) f.A[0] = *(const u32x4*)ap;
    else if constexpr (I <= 8) f.B[I - 1] = *(const u32x4*)(bp + 512 * (8 - I));
    else f.A[I - 8] = *(const u32x4*)(ap + 512 * (I - 8));
}
template <int I> __device__ __forceinline__ void load_all(Frags& f, const char* ap, const char* bp) {
    if constexpr (I < 16) { load_one<I>(f, ap, bp); SB(); load_all<I + 1>(f, ap, bp); }
}
template <int T0, int T1, int T2, int T3> struct TileSet {
    static constexpr int tile(int i) { return i == 0 ? T0 : i == 1 ? T1 : i == 2 ? T2 : T3; }
    static constexpr int walk(int I, bool want_tile) {
        int n = 0;
        for (int a = 0; a < 8; ++a)
            for (int j = 0; j < 4; ++j)
                if (tile(j) >= a) { if (n == I) return want_tile ? tile(j) : a; ++n; }
        return -1;
    }
};
constexpr int tri_a(int I) { int a = 0; while (I >= 8 - a) { I -= 8 - a; ++a; } return a; }
constexpr int tri_b(int I) { int a = 0; while (I >= 8 - a) { I -= 8 - a; ++a; } return I; }
template <int I, bool FIRST> __device__ __forceinline__ void class_step(const Frags& cur, Frags& nxt, f32x4 (&acc)[8], const char* ap, const char* bp) {
    if constexpr (I < 36) {
        constexpr int a = tri_a(I), b = tri_b(I);
        mma<FIRST && a == 0>(acc[a + b], cur.A[a], cur.B[b]);
        if constexpr ((I & 1) == 1 && (I >> 1) < 16) load_one<(I >> 1)>(nxt, ap, bp);
        SB();
        class_step<I + 1, FIRST>(cur, nxt, acc, ap, bp);
    }
}
template <class TS, int I, int TO, int PRE> __device__ __forceinline__ void set_step(const Frags& cur, Frags& nxt, f32x4 (&acc)[8], const char* ap, const char* bp) {
    if constexpr (I < TO) {
        constexpr int t = TS::walk(I, true), a = TS::walk(I, false);
        mma<false>(acc[t], cur.A[a], cur.B[t - a]);
        if constexpr (PRE == 1 && (I * 11) / 18 != ((I + 1) * 11) / 18) load_one<(I * 11) / 18>(nxt, ap, bp);
        if constexpr (PRE == 2 && I < 5) load_one<11 + I>(nxt, ap, bp);
        SB();
        set_step<TS, I + 1, TO, PRE>(cur, nxt, acc, ap, bp);
    }
}
template <class TS, int J> __device__ __forceinline__ void tile_step(const Frags& cur, f32x4 (&acc)[8]) {
    constexpr int t = TS::tile(J);
#pragma unroll
    for (int a = 0; a <= t; ++a) { mma<false>(acc[t], cur.A[a], cur.B[t - a]); SB(); }
}
template <class TS, int J> __device__ __forceinline__ void tile_out(char* Y, f32x4 (&acc)[8], const f32x4 (&o)[4]) {
    constexpr int t = TS::tile(J);
    const f32x4 s = acc[t] + o[J];
    const u32x2 pk = {pk_bf(s[0], s[1]), pk_bf(s[2], s[3])};
    *(u32x2*)(Y + 512 * t) = pk;
    SB();
}
template <class SEND, class KEEP>
__device__ __forceinline__ void conv_half(char* smem, const u32x4 (&A0)[8], const char* ap, const char* bp, int wave, int lane, int ch, u32 seq) {
    f32x4 acc[8];
    Frags f0, f1;
#pragma unroll
    for (int a = 0; a < 8; ++a) f0.A[a] = A0[a];
#pragma unroll
    for (int b = 0; b < 8; ++b) { f0.B[b] = *(const u32x4*)(bp + 512 * (7 - b)); SB(); }
    class_step<0, true>(f0, f1, acc, ap + 64, bp + 64);
    class_step<0, false>(f1, f0, acc, ap + 128, bp + 128);
    set_step<SEND, 0, 18, 1>(f0, f1, acc, ap + 192, bp + 192);
    set_step<SEND, 0, 18, 2>(f1, f1, acc, ap + 192, bp + 192);
    set_step<KEEP, 0, 4, 0>(f0, f1, acc, ap, bp);
    asm volatile("s_nop 3" : "+v"(acc[SEND::tile(0)]), "+v"(acc[SEND::tile(1)]), "+v"(acc[SEND::tile(2)]), "+v"(acc[SEND::tile(3)]));
    f32x4* X = (f32x4*)(smem + OFF_X);
#pragma unroll
    for (int i = 0; i < 4; ++i) X[(wave * 4 + i) * 64 + lane] = acc[SEND::tile(i)];
    volatile LAS u32* xf = (volatile LAS u32*)(LAS char*)(smem + OFF_CNT + 32);
    xf[wave] = seq;
    SB();
    set_step<KEEP, 4, 14, 0>(f0, f1, acc, ap, bp);
    u32 seen = xf[wave ^ 1];
    SB();
    set_step<KEEP, 14, 18, 0>(f0, f1, acc, ap, bp);
    for (u32 spins = 0; seen < seq && spins < 50 * SPIN_CAP; ++spins) seen = xf[wave ^ 1];
    SB();
    f32x4 o[4];
#pragma unroll
    for (int i = 0; i < 4; ++i) o[i] = X[((wave ^ 1) * 4 + i) * 64 + lane];
    SB();
    char* Y = smem + OFF_Y + ch * 4096 + (16 * (lane & 15) + 4 * (lane >> 4)) * 2;
    tile_step<KEEP, 0>(f1, acc);
    tile_step<KEEP, 1>(f1, acc);
    tile_step<KEEP, 2>(f1, acc);
    asm volatile("s_nop 3" : "+v"(acc[KEEP::tile(0)]), "+v"(acc[KEEP::tile(1)]));
    tile_out<KEEP, 0>(Y, acc, o);
    tile_out<KEEP, 1>(Y, acc, o);
    tile_step<KEEP, 3>(f1, acc);
    asm volatile("" : "+v"(acc[KEEP::tile(2)]));
    tile_out<KEEP, 2>(Y, acc, o);
    asm volatile("s_nop 7" : "+v"(acc[KEEP::tile(3)]));
    tile_out<KEEP, 3>(Y, acc, o);
}
struct ConvAddr { const char* ap; const char* bp; };
__device__ __forceinline__ ConvAddr conv_addr(char* smem, int wave, int lane) {
    const int ch = (wave >> 1) & 1, hf = wave & 1;
    const int m = lane & 15, g = lane >> 4;
    ConvAddr c;
    c.ap = smem + OFF_T + ch * T_CH + (m & 7) * T_COPY + 16 * (m >> 3) + 16 * g + 256 * hf;
    c.bp = smem + OFF_U + ch * U_STRIDE + 4064 - 32 * m + 16 * g + 256 * hf - 512 * 7;
    return c;
}
__device__ __forceinline__ void conv_preload(u32x4 (&A0)[8], const ConvAddr& c) {
#pragma unroll
    for (int a = 0; a < 8; ++a) A0[a] = *(const u32x4*)(c.ap + 512 * a);
}
template <bool EARLY>
__device__ __forceinline__ void conv_col(char* smem, const ConvAddr& c, int wave, int lane, u32 seq) {
    u32x4 A0[8];
    if (EARLY) conv_preload(A0, c);
    BAR();
    if (!EARLY) conv_preload(A0, c);
    if ((wave & 1) == 0) conv_half<TileSet<1, 3, 4, 6>, TileSet<0, 2, 5, 7>>(smem, A0, c.ap, c.bp, wave, lane, wave >> 1, seq);
    else conv_half<TileSet<0, 2, 5, 7>, TileSet<1, 3, 4, 6>>(smem, A0, c.ap, c.bp, wave, lane, wave >> 1, seq);
    STAMP(26 + seq);
    BAR();
}
template <class PRE>
__device__ __forceinline__ void conv_row(char* smem, int wave, int lane, rsrc_t rs, int id, int fill0, int st, PRE pre) {
    const u32x4 sent = {SENT, SENT, SENT, SENT};
    const u32 off = WS_H + (fill0 < 0 ? 0 : fill0) * WS_HS + id * 8192 + ((wave - 4) * 64 + lane) * 16;
    const u64 t0 = __builtin_amdgcn_s_memrealtime();
    if (fill0 >= 0) { st_dev(rs, off, sent); st_dev(rs, off + 4096, sent); }
    pre();
    if (fill0 >= 0) {
        wait_until(t0 + FILL_GAP);
        st_dev(rs, off + WS_HS, sent); st_dev(rs, off + WS_HS + 4096, sent);
        WAIT_VM(2);
    }
    STAMP(st);
    BAR();
}

__device__ __forceinline__ void row_dma(char* smem, const char* q, const char* k, const char* v, int id, int wave, int lane, int s) {
    const int slot = wave + 8 * s;
    int row = 8 * id + (slot < 12 ? slot - 4 : slot < 20 ? slot - 12 : slot - 20);
    row = row < 0 ? 0 : row;
    const char* src = slot < 12 ? k : slot < 20 ? v : q;
    lds_dma16(src + row * 1024 + lane * 16, (u32)(size_t)(LAS char*)(smem + OFF_R) + slot * R_ROW);
}

__device__ __forceinline__ u32x4 kv_piece(u32 rp, u32x2 sk, int id) {
    u32x2 k0, k1, k2, v0, v1;
    static_assert(4 * R_ROW == 4352, "offsets below");
    asm volatile("ds_read_b64_tr_b16 %0, %5\n\tds_read_b64_tr_b16 %1, %5 offset:4352\n\tds_read_b64_tr_b16 %2, %5 offset:8704\n\t"
                 "ds_read_b64_tr_b16 %3, %5 offset:13056\n\tds_read_b64_tr_b16 %4, %5 offset:17408\n\ts_waitcnt lgkmcnt(0)"
                 : "=&v"(k0), "=&v"(k1), "=&v"(k2), "=&v"(v0), "=&v"(v1) : "v"(rp) : "memory");
    const u32 first = id ? 0xffffffffu : 0u;
    const u32 kp = k0[0] & first, kq = k0[1] & first;
    const float kf[12] = {0.f, bf_hi(kp), bf_lo(kq), bf_hi(kq), bf_lo(k1[0]), bf_hi(k1[0]),
                          bf_lo(k1[1]), bf_hi(k1[1]), bf_lo(k2[0]), bf_hi(k2[0]), bf_lo(k2[1]), bf_hi(k2[1])};
    float vf[8];
    unpack8((u32x4){v0[0], v0[1], v1[0], v1[1]}, vf);
    const float w0 = bf_lo(sk[0]), w1 = bf_hi(sk[0]), w2 = bf_lo(sk[1]), w3 = bf_hi(sk[1]);
    float kv[8];
#pragma unroll
    for (int j = 0; j < 8; ++j) kv[j] = fmaf(kf[j + 4], w0, fmaf(kf[j + 3], w1, fmaf(kf[j + 2], w2, kf[j + 1] * w3))) * vf[j];
    return pack8(kv);
}

__global__ void __launch_bounds__(512, 2)
h3s4_kernel(const char* __restrict__ q, const char* __restrict__ k, const char* __restrict__ v,
            const u32x2* __restrict__ shiftk, const u32x4* __restrict__ ssmk, const u32* __restrict__ ssmD,
            const u16* __restrict__ Are, const u16* __restrict__ Aim, const u16* __restrict__ Bre, const u16* __restrict__ Bim,
            const u16* __restrict__ Cre, const u16* __restrict__ Cim, const u16* __restrict__ logdt, const u32* __restrict__ Dskip,
            u16* __restrict__ out, char* ws
            ) {
    __shared__ __attribute__((aligned(16))) char smem[LDS_BYTES];
    const int tid = threadIdx.x, lane = tid & 63, wave = __builtin_amdgcn_readfirstlane(tid >> 6);
    const int id = (blockIdx.x & 7) * 32 + (blockIdx.x >> 3);
    const rsrc_t rs = __builtin_amdgcn_make_buffer_rsrc(ws, 0, WS_USED, 0x00020000);
    const u32x4 desc = {(u32)(size_t)ws, (u32)((size_t)ws >> 32) & 0xffffu, WS_USED, 0x00020000u};
    STAMP(0);

    u32 code_pf = 0;
    {
        u64 pc; u32 len;
        asm volatile("s_getpc_b64 %0\nh3s4_text_a:\n\ts_mov_b32 %1, h3s4_text_b - h3s4_text_a" : "=s"(pc), "=s"(len));
        const u32 off = ((blockIdx.x >> 3) * 8 + wave) * 256 + lane * 4;
        if (off + 4 <= len) asm volatile("global_load_dword %0, %1, %2" : "=v"(code_pf) : "v"(off), "s"(pc) : "memory");
    }
    const u32x4 sent = {SENT, SENT, SENT, SENT};
    const u32 dA = ssmD[id], dB = Dskip[id];
    MEMFENCE(); SB();
    st_dev(rs, WS_H + 0 * WS_HS + id * 8192 + tid * 16, sent);
    MEMFENCE(); SB();
    if (wave < 4) {
        u32x2 skB = ld_b64(shiftk, (256 + tid) * 8);
        row_dma(smem, q, k, v, id, wave, lane, 0); row_dma(smem, q, k, v, id, wave, lane, 1); row_dma(smem, q, k, v, id, wave, lane, 2);
        MEMFENCE(); SB();
        row_dma(smem, q, k, v, id, wave, lane, 3);
        u32x4 tap_own = ld_b128(ssmk, (2 * id) * 4096 + tid * 16), tap_prev = ld_b128(ssmk, (2 * id) * 4096 + (tid ? tid - 1 : 0) * 16);
        const int pd = 2 * id + (tid >> 7), po = (pd * NS + (tid & 127)) * 2;
        u32 r_dt = ld_u16(logdt, pd * 2), r_are = ld_u16(Are, po), r_aim = ld_u16(Aim, po), r_bre = ld_u16(Bre, po);
        u32 r_bim = ld_u16(Bim, po), r_cre = ld_u16(Cre, po), r_cim = ld_u16(Cim, po);
        u32x4 tix0 = ld_b128(tap_index.off, tid * 16), tix1 = ld_b128(tap_index.off, tid * 16 + 16);
        MEMFENCE(); SB();
        st_dev(rs, WS_H + 1 * WS_HS + id * 8192 + tid * 16, sent);
        MEMFENCE(); SB();
        const u32 epoch = launch_tag(ws);
        asm volatile("s_waitcnt vmcnt(16)\n\ts_barrier" ::: "memory");
        if (tid < 32) __hip_atomic_store((GAS u32*)(ws + WS_FLAGS) + tid * 256 + id, epoch, RLX_AGENT);
        STAMP(19);
        if (tid < 60) *(u32x4*)(smem + OFF_U + (tid / 30) * U_STRIDE + 4096 + (tid % 30) * 16) = (u32x4){0, 0, 0, 0};
        else if (tid >= 64 && tid < 80) *(u32x4*)(smem + OFF_T + (tid - 64) * T_COPY) = (u32x4){0, 0, 0, 0};
        asm volatile("s_waitcnt vmcnt(13)" : "+v"(skB) :: "memory");
        BAR();
        STAMP(1);
        publish(rs, WS_H + id * 8192 + (256 + tid) * 16,
                kv_piece((u32)(size_t)(LAS char*)(smem + OFF_R + ((lane & 15) >> 2) * R_ROW + (256 + 64 * wave + 16 * (lane >> 4) + 4 * (lane & 3)) * 2), skB, id));
        asm volatile("s_waitcnt vmcnt(1)"
                     : "+v"(tap_own), "+v"(tap_prev), "+v"(r_dt), "+v"(r_are), "+v"(r_aim), "+v"(r_bre), "+v"(r_bim), "+v"(r_cre),
                       "+v"(r_cim), "+v"(tix0), "+v"(tix1) :: "memory");
        cnt_arrive(smem, 6, lane);

        {
            const float dt = rbf(__builtin_amdgcn_exp2f(bf_lo(r_dt) * 1.4426950408889634f));
            const float are = dt * bf_lo(r_are), aim = dt * bf_lo(r_aim);
            const float br = dt * bf_lo(r_bre), bi = dt * bf_lo(r_bim);
            const float cr = bf_lo(r_cre), ci = bf_lo(r_cim);
            const float wr = cr * br - ci * bi, wi = cr * bi + ci * br;
            const float ar = fabsf(wr), ai = fabsf(wi), mx = fmaxf(ar, ai), mn = fminf(ar, ai);
            const float ratio = mx > 0.f ? mn * __builtin_amdgcn_rcpf(mx) : 0.f, r2 = ratio * ratio;
            float at = 0.0028662257f;
            at = fmaf(at, r2, -0.0161657367f); at = fmaf(at, r2, 0.0429096138f); at = fmaf(at, r2, -0.0752896400f);
            at = fmaf(at, r2, 0.1065626393f); at = fmaf(at, r2, -0.1420889944f); at = fmaf(at, r2, 0.1999355085f);
            at = fmaf(at, r2, -0.3333314528f); at = fmaf(at, r2, 1.0f);
            float ph = at * ratio * 0.15915494309189535f;
            ph = ai > ar ? 0.25f - ph : ph;
            ph = wr < 0.f ? 0.5f - ph : ph;
            ph = wi < 0.f ? -ph : ph;
            float* p = (float*)(smem + OFF_P) + (tid >> 1) * 8 + (tid & 1);
            p[0] = are * 1.4426950408889634f;
            p[2] = aim * 0.15915494309189535f;
            p[4] = __builtin_amdgcn_logf(mx) + 0.5f * __builtin_amdgcn_logf(r2 + 1.0f);
            p[6] = ph;
        }
        cnt_arrive(smem, 7, lane);
        if (tid == 0) {
            tap_prev = (u32x4){0, 0, 0, 0};
            tap_own[0] = (tap_own[0] & 0xffff0000u) | (pk_bf(bf_lo(tap_own[0]) + bf_lo(dA), 0.f) & 0xffffu);
        }
        stage_taps(smem, 0, tid, tap_prev, tap_own);
        cnt_arrive(smem, 0, lane);
        STAMP(2);
        if (C_PRE) {
            cnt_wait(smem, 7, 4);
            KState ks;
            kgen_init(ks);
            kgen_steps<C_PRE, true>(kgen_params(smem, wave, 0), lane, ks);
            kgen_save(smem, wave, lane, ks);
        }

        const ConvAddr ca = conv_addr(smem, wave, lane);
        asm volatile("" :: "v"(code_pf));
        cnt_wait(smem, 0, 8);
        STAMP(4);
#pragma nounroll
        for (u32 seq = 1; seq <= 3; ++seq) {
            if (seq == 2) {
                {
                    KState ks;
                    if (C_PRE) kgen_load(smem, wave, lane, ks); else kgen_init(ks);
                    kgen_steps<4 - R_STEPS - C_PRE, true>(kgen_params(smem, wave, C_PRE), lane, ks);
                    kgen_save(smem, wave, lane, ks);
                }
                STAMP(7);
                cnt_arrive(smem, 1, lane);
                cnt_wait(smem, 1, 8);
                kgen_reduce(smem, wave, lane, dB);
                cnt_arrive(smem, 2, lane);
                cnt_wait(smem, 2, 4);
                STAMP(23);
                s4_taps(smem, 0, tid, tix0, tix1);
                cnt_arrive(smem, 3, lane);
                cnt_wait(smem, 3, 8);
                STAMP(25);
            }
            conv_col<true>(smem, ca, wave, lane, seq);
            STAMP(seq == 1 ? 6 : seq == 2 ? 11 : 15);
        }
    } else {
        const int r = tid - 256, w = wave - 4;
        u32x2 skA = ld_b64(shiftk, r * 8);
        row_dma(smem, q, k, v, id, wave, lane, 0); row_dma(smem, q, k, v, id, wave, lane, 1);
        MEMFENCE(); SB();
        row_dma(smem, q, k, v, id, wave, lane, 2);
        u32x4 tap_own = ld_b128(ssmk, (2 * id + 1) * 4096 + r * 16), tap_prev = ld_b128(ssmk, (2 * id + 1) * 4096 + (r ? r - 1 : 0) * 16);
        u32x4 tix0 = ld_b128(tap_index.off, r * 16), tix1 = ld_b128(tap_index.off, r * 16 + 16);
        MEMFENCE(); SB();
        st_dev(rs, WS_H + 1 * WS_HS + id * 8192 + tid * 16, sent);
        MEMFENCE(); SB();
        const u32 epoch = launch_tag(ws);
        asm volatile("s_waitcnt vmcnt(8)\n\ts_barrier" ::: "memory");
        ((u32*)(smem + OFF_RED))[r] = SENT;
        if (r < 16) ((u32*)(smem + OFF_CNT))[r] = 0u;
        asm volatile("s_waitcnt vmcnt(6)" : "+v"(skA) :: "memory");
        BAR();
        STAMP(1);

        const u32 own_a = id * 8192 + r * 16, own_b = own_a + 4096;
        const u32 col_a = (r >> 1) * 8192 + (2 * id + (r & 1)) * 16, col_b = col_a + 128 * 8192;
        const u32 row_a = (r >> 1) * 8192 + id * 32 + (r & 1) * 16, row_b = row_a + 128 * 8192;
        char* const u_dst = smem + OFF_U + (r & 1) * U_STRIDE + 4080 - (r >> 1) * 16;
        const char* const y_src = smem + OFF_Y + (r & 1) * 4096 + (r >> 1) * 16;
        float* red = (float*)(smem + OFF_RED);

        Rows x, u0;
        u64 t_s;
        const u32 flag_a = WS_FLAGS + (blockIdx.x & 31) * 1024 + (r >> 1) * 4;
        u32 fl_a, fl_b;
        const u32 rp = (u32)(size_t)(LAS char*)(smem + OFF_R + ((lane & 15) >> 2) * R_ROW + (64 * w + 16 * (lane >> 4) + 4 * (lane & 3)) * 2);
        publish(rs, WS_H + own_a, kv_piece(rp, skA, id));
        t_s = __builtin_amdgcn_s_memrealtime();
        __builtin_amdgcn_s_setprio(2);
        asm volatile("s_waitcnt vmcnt(1)" : "+v"(tap_own), "+v"(tap_prev), "+v"(tix0), "+v"(tix1) :: "memory");
        fl_a = ld_flag(ws, flag_a); fl_b = ld_flag(ws, flag_a + 512);
        cnt_arrive(smem, 6, lane);
        STAMP(2);
        u32 touch = 0;
        if (wave == 6) {
            if (lane < 4) touch = __builtin_amdgcn_raw_buffer_load_b32(rs, WS_H + (2 + lane) * WS_HS + id * 8192, 0, 16);
            else if (lane == 4) touch = ((const u32*)out)[id * 2048];
        }
        if (r == 0) {
            tap_prev = (u32x4){0, 0, 0, 0};
            tap_own[0] = (tap_own[0] & 0xffff0000u) | (pk_bf(bf_lo(tap_own[0]) + bf_hi(dA), 0.f) & 0xffffu);
        }
        stage_taps(smem, 1, r, tap_prev, tap_own);
        cnt_arrive(smem, 0, lane);
        STAMP(3);

        Piece2 pc, pr;
        Rows yA, u;
        auto stage_input = [&](u32 h) {
            poll2_finish(pc, desc, WS_H + h * WS_HS + col_a, WS_H + h * WS_HS + col_b);
            *(u32x4*)u_dst = reverse8(pc.a);
            *(u32x4*)(u_dst - 2048) = reverse8(pc.b);
        };
        auto send_result = [&](u32 h) {
            publish(rs, WS_H + h * WS_HS + own_a, *(const u32x4*)y_src);
            publish(rs, WS_H + h * WS_HS + own_b, *(const u32x4*)(y_src + 2048));
            t_s = __builtin_amdgcn_s_memrealtime();
        };
        auto fetch_rows = [&](u32 h) {
            wait_until(t_s + POLL_DELAY);
            pr = poll2_issue(rs, WS_H + h * WS_HS + row_a, WS_H + h * WS_HS + row_b);
            poll2_finish(pr, desc, WS_H + h * WS_HS + row_a, WS_H + h * WS_HS + row_b);
        };

        wait_until(t_s + POLL_DELAY);
        {
            asm volatile("" :: "v"(fl_a), "v"(fl_b));
            const bool ok = fl_a == epoch && fl_b == epoch;
            pc = poll2_issue(rs, WS_H + col_a, WS_H + col_b);
            poll2_finish_flagged(pc, desc, WS_H + col_a, WS_H + col_b, flag_a, flag_a + 512, ok, epoch);
            *(u32x4*)u_dst = reverse8(pc.a);
            *(u32x4*)(u_dst - 2048) = reverse8(pc.b);
        }
        STAMP(21);
        asm volatile("" :: "v"(touch));
        asm volatile("" :: "v"(code_pf));
        BAR();
        STAMP(5);
        if (blockIdx.x == 0 && r == 0) __hip_atomic_store((GAS u32*)(ws + WS_EPOCH), epoch, RLX_AGENT);
        conv_row(smem, wave, lane, rs, id, 2, 27, [&] {
            cnt_wait(smem, 6, 8);
            if (wave == 7 && lane < 32) __hip_atomic_store((GAS u32*)(ws + WS_FLAGS + 32768) + lane * 256 + id, epoch, RLX_AGENT);
            u32x2 qq[2][2];
            asm volatile("ds_read_b64_tr_b16 %0, %4 offset:21760\n\tds_read_b64_tr_b16 %1, %4 offset:26112\n\t"
                         "ds_read_b64_tr_b16 %2, %4 offset:22272\n\tds_read_b64_tr_b16 %3, %4 offset:26624\n\ts_waitcnt lgkmcnt(0)"
                         : "=&v"(qq[0][0]), "=&v"(qq[0][1]), "=&v"(qq[1][0]), "=&v"(qq[1][1]) : "v"(rp) : "memory");
            unpack_rows((u32x4){qq[0][0][0], qq[0][0][1], qq[0][1][0], qq[0][1][1]}, x.a);
            unpack_rows((u32x4){qq[1][0][0], qq[1][0][1], qq[1][1][0], qq[1][1][1]}, x.b);
            __builtin_amdgcn_s_setprio(0);
            KState ks;
            kgen_init(ks);
            kgen_steps<R_STEPS, false>(kgen_params(smem, wave, 4 - R_STEPS), lane, ks);
            kgen_save(smem, wave, lane, ks);
            cnt_arrive(smem, 1, lane);
            __builtin_amdgcn_s_setprio(2);
        });
        send_result(1);
        STAMP(6);

#pragma nounroll
        for (u32 it = 0; it < 2; ++it) {
            MEMFENCE();
            const u32 fb_a = ld_flag(ws, flag_a + 32768), fb_b = ld_flag(ws, flag_a + 32768 + 512);
            MEMFENCE();
            layer_norm(x, u0, red + 128 * it, lane, w);
            if (it == 0) {
                wait_until(t_s + POLL_DELAY);
                asm volatile("" :: "v"(fb_a), "v"(fb_b));
                const bool ok = fb_a == epoch && fb_b == epoch;
                pr = poll2_issue(rs, WS_H + WS_HS + row_a, WS_H + WS_HS + row_b);
                poll2_finish_flagged(pr, desc, WS_H + WS_HS + row_a, WS_H + WS_HS + row_b, flag_a + 32768, flag_a + 32768 + 512, ok, epoch);
                STAMP(8);
                unpack_rows(pr.a, yA.a);
                unpack_rows(pr.b, yA.b);
            }
#pragma unroll
            for (int i = 0; i < 4; ++i) { x.a[i] += u0.a[i] * yA.a[i]; x.b[i] += u0.b[i] * yA.b[i]; }
            round_rows(x.a);
            round_rows(x.b);
            layer_norm(x, u, red + 128 * it + 64, lane, w);
            publish(rs, WS_H + (2 + 2 * it) * WS_HS + own_a, pack_rows(u.a));
            publish(rs, WS_H + (2 + 2 * it) * WS_HS + own_b, pack_rows(u.b));
            t_s = __builtin_amdgcn_s_memrealtime();
            STAMP(it ? 13 : 9);

            wait_until(t_s + POLL_DELAY);
            pc = poll2_issue(rs, WS_H + (2 + 2 * it) * WS_HS + col_a, WS_H + (2 + 2 * it) * WS_HS + col_b);
            if (it == 0) {
                cnt_wait(smem, 2, 4);
                s4_taps(smem, 1, r, tix0, tix1);
                cnt_arrive(smem, 3, lane);
            }
            stage_input(2 + 2 * it);
            BAR();
            STAMP(it ? 14 : 10);
            conv_row(smem, wave, lane, rs, id, it ? -1 : 4, 28, [] {});
            send_result(3 + 2 * it);
            STAMP(it ? 15 : 11);

            fetch_rows(3 + 2 * it);
            STAMP(it ? 16 : 12);
            Rows y;
            unpack_rows(pr.a, y.a);
            unpack_rows(pr.b, y.b);
#pragma unroll
            for (int i = 0; i < 4; ++i) { x.a[i] += y.a[i]; x.b[i] += y.b[i]; }
            pr.a = round_rows(x.a);
            pr.b = round_rows(x.b);
        }
        {
            u16* dst = out + (8 * id) * DM + r;
#pragma unroll
            for (int j = 0; j < 4; ++j) {
                dst[(2 * j) * DM] = (u16)pr.a[j];
                dst[(2 * j) * DM + 256] = (u16)pr.b[j];
                dst[(2 * j + 1) * DM] = (u16)(pr.a[j] >> 16);
                dst[(2 * j + 1) * DM + 256] = (u16)(pr.b[j] >> 16);
            }
        }
        STAMP(17);
    }
    asm volatile("h3s4_text_b:");
    asm volatile("s_waitcnt vmcnt(0)" ::: "memory");
    STAMP(18);
    BAR();
    if (tid == 0) {
        const u64* st = (const u64*)(smem + OFF_ST);
        const u64 dur = st[DIAG_B] - st[DIAG_A];
        const u64 until = __builtin_amdgcn_s_memrealtime() + DIAG * dur;
        while (__builtin_amdgcn_s_memrealtime() < until) __builtin_amdgcn_s_sleep(2);
    }
}

extern "C" void kernel_launch(void* const* d_in, const int* in_sizes, int n_in,
                              void* d_out, int out_size, void* d_ws, size_t ws_size,
                              hipStream_t stream) {
    h3s4_kernel<<<256, 512, 0, stream>>>(
        (const char*)d_in[0], (const char*)d_in[1], (const char*)d_in[2], (const u32x2*)d_in[3], (const u32x4*)d_in[4],
        (const u32*)d_in[5], (const u16*)d_in[7], (const u16*)d_in[8], (const u16*)d_in[9], (const u16*)d_in[10],
        (const u16*)d_in[11], (const u16*)d_in[12], (const u16*)d_in[13], (const u32*)d_in[14], (u16*)d_out, (char*)d_ws
        );
}
```

```cpp
#define DIAG 0
#define DIAG_A 0
#define DIAG_B 0
#include <hip/hip_runtime.h>
#include <hip/hip_bf16.h>
#include <stdint.h>
#include <type_traits>

typedef unsigned int u32;
typedef unsigned short u16;
typedef unsigned long long u64;
typedef __attribute__((ext_vector_type(4))) float f32x4;
typedef __attribute__((ext_vector_type(2))) float f32x2;
typedef __attribute__((ext_vector_type(4))) u32 u32x4;
typedef __attribute__((ext_vector_type(2))) u32 u32x2;
typedef __attribute__((ext_vector_type(4))) short s16x4;
typedef __attribute__((ext_vector_type(8))) __bf16 bf16x8;
typedef __attribute__((ext_vector_type(2))) __bf16 bf16x2;
#define LAS __attribute__((address_space(3)))
#define GAS __attribute__((address_space(1)))
#define RLX_AGENT __ATOMIC_RELAXED, __HIP_MEMORY_SCOPE_AGENT
#define SB() __builtin_amdgcn_sched_barrier(0)
#define BAR() asm volatile("s_waitcnt lgkmcnt(0)\n\ts_barrier" ::: "memory")
#define MEMFENCE() asm volatile("" ::: "memory")
#define WAIT_VM(n) do { MEMFENCE(); __builtin_amdgcn_s_waitcnt(0x0F70 | ((n) & 15) | (((n) >> 4) << 14)); MEMFENCE(); } while (0)

constexpr int DM = 512;
constexpr int NS = 128;
constexpr u32 SENT = 0xffffffffu;
constexpr u32 SPIN_CAP = 20000;
#define FILL_GAP 70
#define C_PRE 1
#define R_STEPS 1
#define POLL_DELAY 45

constexpr u32 WS_EPOCH = 0;
constexpr u32 WS_FLAGS = 4096;
constexpr u32 WS_H = 1u << 20;
constexpr u32 WS_HS = 2u << 20;
constexpr u32 WS_USED = WS_H + 6 * WS_HS;

constexpr int T_COPY = 4160, T_CH = 8 * T_COPY;
constexpr int U_STRIDE = 4672;
constexpr int R_ROW = 1088;
constexpr int KP_WAVE = 2816;
constexpr int OFF_T = 0;
constexpr int OFF_U = OFF_T + 2 * T_CH;
constexpr int OFF_X = OFF_U + 2 * U_STRIDE;
constexpr int OFF_Y = OFF_X + 16384;
constexpr int OFF_R = OFF_Y + 8192;
constexpr int OFF_P = OFF_R + 28 * R_ROW;
constexpr int OFF_KP = OFF_P + 4096;
constexpr int OFF_KC = OFF_KP + 8 * KP_WAVE;
constexpr int OFF_RED = OFF_KC + 2 * 648 * 2;
constexpr int OFF_CNT = OFF_RED + 1024;
constexpr int OFF_ST = OFF_CNT + 64;
constexpr int LDS_BYTES = OFF_ST + 512;
static_assert(LDS_BYTES <= 163840, "LDS");

#define STAMP(i) do { if ((threadIdx.x & 255) == 0) ((u64*)(smem + OFF_ST))[(i) + (threadIdx.x ? 32 : 0)] = __builtin_amdgcn_s_memrealtime(); } while (0)

__device__ __forceinline__ float bf_lo(u32 p) { return __builtin_bit_cast(float, p << 16); }
__device__ __forceinline__ float bf_hi(u32 p) { return __builtin_bit_cast(float, p & 0xffff0000u); }
__device__ __forceinline__ float bf_u16(u16 p) { return __builtin_bit_cast(float, (u32)p << 16); }
__device__ __forceinline__ u32 pk_bf(float lo, float hi) {
    bf16x2 v = {(__bf16)lo, (__bf16)hi};
    return __builtin_bit_cast(u32, v);
}
__device__ __forceinline__ float as_f(u32 x) { return __builtin_bit_cast(float, x); }
__device__ __forceinline__ u32 as_u(float x) { return __builtin_bit_cast(u32, x); }
__device__ __forceinline__ float rbf(float x) { return bf_lo(pk_bf(x, x)); }
__device__ __forceinline__ void unpack8(u32x4 p, float (&f)[8]) {
#pragma unroll
    for (int i = 0; i < 4; ++i) { f[2 * i] = bf_lo(p[i]); f[2 * i + 1] = bf_hi(p[i]); }
}
__device__ __forceinline__ u32x4 pack8(const float (&f)[8]) {
    return (u32x4){pk_bf(f[0], f[1]), pk_bf(f[2], f[3]), pk_bf(f[4], f[5]), pk_bf(f[6], f[7])};
}
__device__ __forceinline__ u32x4 reverse8(u32x4 p) {
    return (u32x4){__builtin_amdgcn_alignbit(p[3], p[3], 16), __builtin_amdgcn_alignbit(p[2], p[2], 16),
                   __builtin_amdgcn_alignbit(p[1], p[1], 16), __builtin_amdgcn_alignbit(p[0], p[0], 16)};
}
template <int CTRL> __device__ __forceinline__ float dppf(float v) {
    return __builtin_bit_cast(float, __builtin_amdgcn_update_dpp(0, __builtin_bit_cast(int, v), CTRL, 0xf, 0xf, true));
}

__device__ __forceinline__ void lds_dma16(const char* src, u32 lds) {
    u32 keep;
    asm volatile("s_mov_b32 %0, m0\n\ts_mov_b32 m0, %2\n\ts_nop 0\n\tglobal_load_lds_dwordx4 %1, off\n\ts_mov_b32 m0, %0"
                 : "=&s"(keep) : "v"(src), "s"(lds) : "memory");
}

__device__ __forceinline__ u32 ld_u16(const void* base, u32 off) {
    u32 r;
    asm volatile("global_load_ushort %0, %1, %2" : "=v"(r) : "v"(off), "s"(base) : "memory");
    return r;
}
__device__ __forceinline__ u32x2 ld_b64(const void* base, u32 off) {
    u32x2 r;
    asm volatile("global_load_dwordx2 %0, %1, %2" : "=v"(r) : "v"(off), "s"(base) : "memory");
    return r;
}
__device__ __forceinline__ u32x4 ld_b128(const void* base, u32 off) {
    u32x4 r;
    asm volatile("global_load_dwordx4 %0, %1, %2" : "=v"(r) : "v"(off), "s"(base) : "memory");
    return r;
}

typedef __amdgpu_buffer_rsrc_t rsrc_t;
__device__ __forceinline__ u32x4 ld_dev(rsrc_t rs, u32 off) { return __builtin_amdgcn_raw_buffer_load_b128(rs, off, 0, 16); }
__device__ __forceinline__ void st_dev(rsrc_t rs, u32 off, u32x4 v) { __builtin_amdgcn_raw_buffer_store_b128(v, rs, off, 0, 16); }
__device__ __forceinline__ void publish(rsrc_t rs, u32 off, u32x4 v) {
#pragma unroll
    for (int i = 0; i < 4; ++i) v[i] = v[i] < SENT - 1u ? v[i] : SENT - 1u;
    st_dev(rs, off, v);
}
__device__ __forceinline__ u32 launch_tag(const char* ws) {
    u32 e;
    asm volatile("s_load_dword %0, %1, 0x0\n\ts_waitcnt lgkmcnt(0)" : "=&s"(e) : "s"(ws + WS_EPOCH) : "memory");
    return e + 1u;
}
__device__ __forceinline__ void wait_until(u64 t) {
    while (__builtin_amdgcn_s_memrealtime() < t) __builtin_amdgcn_s_sleep(1);
}
__device__ __forceinline__ u32x4 poll_issue(rsrc_t rs, u32 off) {
    MEMFENCE();
    u32x4 x = ld_dev(rs, off);
    MEMFENCE();
    return x;
}
struct Piece2 { u32x4 a, b; };
__device__ __forceinline__ Piece2 poll2_issue(rsrc_t rs, u32 off_a, u32 off_b) {
    MEMFENCE();
    Piece2 p = {ld_dev(rs, off_a), ld_dev(rs, off_b)};
    MEMFENCE();
    return p;
}
__device__ __forceinline__ bool has_sentinel(const u32x4& x) {
    u32 mx = x[0] > x[1] ? x[0] : x[1];
    mx = mx > x[2] ? mx : x[2];
    mx = mx > x[3] ? mx : x[3];
    return mx == SENT;
}
__device__ __forceinline__ void poll2_finish(Piece2& p, u32x4 desc, u32 off_a, u32 off_b) {
#pragma nounroll
    for (u32 spins = 0; spins < SPIN_CAP; ++spins) {
        const bool bad = has_sentinel(p.a) || has_sentinel(p.b);
        if (__builtin_amdgcn_ballot_w64(bad) == 0ull) break;
        __builtin_amdgcn_s_sleep(1);
        if (bad) asm volatile("buffer_load_dwordx4 %0, %2, %4, 0 offen sc1\n\tbuffer_load_dwordx4 %1, %3, %4, 0 offen sc1\n\ts_waitcnt vmcnt(0)"
                              : "+v"(p.a), "+v"(p.b) : "v"(off_a), "v"(off_b), "s"(desc) : "memory");
    }
}
__device__ __forceinline__ u32 ld_flag(const char* ws, u32 off) { return __hip_atomic_load((const GAS u32*)(ws + off), RLX_AGENT); }
__device__ __forceinline__ void poll2_finish_flagged(Piece2& p, u32x4 desc, u32 off_a, u32 off_b, u32 fo_a, u32 fo_b, bool ok, u32 epoch) {
#pragma nounroll
    for (u32 spins = 0; spins < SPIN_CAP; ++spins) {
        const bool bad = !ok || has_sentinel(p.a) || has_sentinel(p.b);
        if (__builtin_amdgcn_ballot_w64(bad) == 0ull) break;
        if (!ok) {
            u32 fa, fb;
            asm volatile("buffer_load_dword %0, %2, %4, 0 offen sc1\n\tbuffer_load_dword %1, %3, %4, 0 offen sc1\n\ts_waitcnt vmcnt(0)"
                         : "=&v"(fa), "=&v"(fb) : "v"(fo_a), "v"(fo_b), "s"(desc) : "memory");
            ok = fa == epoch && fb == epoch;
        }
        if (bad) asm volatile("buffer_load_dwordx4 %0, %2, %4, 0 offen sc1\n\tbuffer_load_dwordx4 %1, %3, %4, 0 offen sc1\n\ts_waitcnt vmcnt(0)"
                              : "+v"(p.a), "+v"(p.b) : "v"(off_a), "v"(off_b), "s"(desc) : "memory");
    }
}

__device__ __forceinline__ void cnt_arrive(char* smem, int k, int lane) {
    asm volatile("s_waitcnt lgkmcnt(0)" ::: "memory");
    if (lane == 0) __hip_atomic_fetch_add((LAS u32*)(smem + OFF_CNT) + k, 1u, __ATOMIC_RELAXED, __HIP_MEMORY_SCOPE_WORKGROUP);
}
__device__ __forceinline__ void cnt_wait(char* smem, int k, u32 target) {
    u32 spins = 0;
    while (__hip_atomic_load((LAS u32*)(smem + OFF_CNT) + k, __ATOMIC_RELAXED, __HIP_MEMORY_SCOPE_WORKGROUP) < target) {
        if (++spins > 50 * SPIN_CAP) break;
        __builtin_amdgcn_s_sleep(1);
    }
    MEMFENCE();
}

struct Rows { f32x2 a[4], b[4]; };
__device__ __forceinline__ void unpack_rows(u32x4 p, f32x2 (&f)[4]) {
#pragma unroll
    for (int i = 0; i < 4; ++i) f[i] = (f32x2){bf_lo(p[i]), bf_hi(p[i])};
}
__device__ __forceinline__ u32x4 pack_rows(const f32x2 (&f)[4]) {
    return (u32x4){pk_bf(f[0][0], f[0][1]), pk_bf(f[1][0], f[1][1]), pk_bf(f[2][0], f[2][1]), pk_bf(f[3][0], f[3][1])};
}
__device__ __forceinline__ u32x4 round_rows(f32x2 (&f)[4]) {
    u32x4 p = pack_rows(f);
    asm("" : "+v"(p));
    unpack_rows(p, f);
    return p;
}
__device__ __forceinline__ void layer_norm(const Rows& x, Rows& u, float* red, int lane, int w) {
#pragma clang fp contract(fast)
    u32 a[8], b[8];
#pragma unroll
    for (int i = 0; i < 4; ++i) {
        const f32x2 s1 = x.a[i] + x.b[i], s2 = x.a[i] * x.a[i] + x.b[i] * x.b[i];
        a[2 * i] = as_u(s1[0]); a[2 * i + 1] = as_u(s1[1]);
        b[2 * i] = as_u(s2[0]); b[2 * i + 1] = as_u(s2[1]);
    }
    SB();
#pragma unroll
    for (int j = 0; j < 8; ++j) {
        const auto s = __builtin_amdgcn_permlane32_swap(a[j], b[j], false, false);
        a[j] = s[0]; b[j] = s[1];
    }
    SB();
    float r[8];
#pragma unroll
    for (int j = 0; j < 8; ++j) r[j] = as_f(a[j]) + as_f(b[j]);
    SB();
#pragma unroll
    for (int j = 0; j < 4; ++j) {
        const auto s = __builtin_amdgcn_permlane16_swap(as_u(r[j]), as_u(r[j + 4]), false, false);
        a[j] = s[0]; b[j] = s[1];
    }
    SB();
    float s4[4];
#pragma unroll
    for (int j = 0; j < 4; ++j) s4[j] = as_f(a[j]) + as_f(b[j]);
    SB();
#pragma unroll
    for (int j = 0; j < 4; ++j) s4[j] += dppf<0x128>(s4[j]);
    const float t0 = (lane & 8) ? s4[2] : s4[0], t1 = (lane & 8) ? s4[3] : s4[1];
    const float c0 = t0 + dppf<0x141>(t0), c1 = t1 + dppf<0x141>(t1);
    float s = (lane & 4) ? c1 : c0;
    s += dppf<0xB1>(s);
    s += dppf<0x4E>(s);
    const u32 sb = as_u(s) < SENT - 1u ? as_u(s) : SENT - 1u;
    if ((lane & 3) == 0) ((volatile LAS u32*)(LAS char*)(char*)red)[(lane >> 2) * 4 + w] = sb;
    u32x4 p;
    u32 spins = 0;
    while (true) {
        p = *(volatile LAS u32x4*)((LAS char*)(char*)red + (lane & 15) * 16);
        u32 mx = p[0] > p[1] ? p[0] : p[1];
        mx = mx > p[2] ? mx : p[2];
        mx = mx > p[3] ? mx : p[3];
        if (__builtin_amdgcn_ballot_w64(mx == SENT) == 0ull) break;
        if (++spins > 50 * SPIN_CAP) break;
    }
    const float m = ((as_f(p[0]) + as_f(p[1])) + (as_f(p[2]) + as_f(p[3]))) * (1.0f / DM);
    const float ex2 = dppf<0x128>(m);
    const float rstd = __builtin_amdgcn_rsqf(fmaxf(fmaf(-m, m, ex2), 0.f) + 1e-5f);
    const float off = -m * rstd;
    const f32x2 R[4] = {{dppf<0x150>(rstd), dppf<0x151>(rstd)}, {dppf<0x152>(rstd), dppf<0x153>(rstd)},
                        {dppf<0x154>(rstd), dppf<0x155>(rstd)}, {dppf<0x156>(rstd), dppf<0x157>(rstd)}};
    const f32x2 O[4] = {{dppf<0x150>(off), dppf<0x151>(off)}, {dppf<0x152>(off), dppf<0x153>(off)},
                        {dppf<0x154>(off), dppf<0x155>(off)}, {dppf<0x156>(off), dppf<0x157>(off)}};
#pragma unroll
    for (int i = 0; i < 4; ++i) { u.a[i] = x.a[i] * R[i] + O[i]; u.b[i] = x.b[i] * R[i] + O[i]; }
}

__device__ __forceinline__ void stage_taps(char* smem, int ch, int i, u32x4 prev, u32x4 own) {
    const u32 D[8] = {prev[0], prev[1], prev[2], prev[3], own[0], own[1], own[2], own[3]};
    u32 O[7];
#pragma unroll
    for (int y = 0; y < 7; ++y) O[y] = __builtin_amdgcn_alignbit(D[y + 1], D[y], 16);
    char* base = smem + OFF_T + ch * T_CH + (i + 1) * 16;
#pragma unroll
    for (int c = 0; c < 8; ++c) {
        const int y = (c + 1) >> 1;
        const u32x4 v = (c & 1) ? (u32x4){D[y], D[y + 1], D[y + 2], D[y + 3]} : (u32x4){O[y], O[y + 1], O[y + 2], O[y + 3]};
        *(u32x4*)(base + c * T_COPY) = v;
    }
}

__device__ __forceinline__ f32x4 mfma16(u32x4 a, u32x4 b, f32x4 c) {
    return __builtin_amdgcn_mfma_f32_16x16x32_bf16(__builtin_bit_cast(bf16x8, a), __builtin_bit_cast(bf16x8, b), c, 0, 0, 0);
}
struct KState { f32x4 acc[3]; };
__device__ __forceinline__ void kgen_init(KState& ks) {
#pragma unroll
    for (int e = 0; e < 3; ++e) ks.acc[e] = (f32x4){0, 0, 0, 0};
}
#define OPQ(x) asm volatile("" : "+v"(x))
template <int NS_, bool PK>
__device__ __forceinline__ void kgen_steps(const f32x4* P, int lane, KState& ks) {
#pragma clang fp contract(fast)
    const int g = lane >> 4, m = lane & 15;
    const float lo = (float)(8 * m);
    const float hi[3] = {(float)(128 * (m >> 3) + (m & 7)),
                         (float)(m < 8 ? 256 + 128 * (m >> 2) + 2 * (m & 3) : 512 + 128 * ((m - 8) >> 1) + 4 * (m & 1)),
                         (float)(m < 8 ? 1024 + 128 * m : 2048)};
    auto eval = [&](f32x2 ex, f32x2 an, bool conj, u32& o0, u32& o1) {
        if (!PK) { OPQ(ex[0]); OPQ(ex[1]); OPQ(an[0]); OPQ(an[1]); }
        const float m0 = __builtin_amdgcn_exp2f(ex[0]), m1 = __builtin_amdgcn_exp2f(ex[1]);
        const float f0 = __builtin_amdgcn_fractf(an[0]), f1 = __builtin_amdgcn_fractf(an[1]);
        const float c0 = __builtin_amdgcn_cosf(f0), c1 = __builtin_amdgcn_cosf(f1);
        const float s0 = __builtin_amdgcn_sinf(f0), s1 = __builtin_amdgcn_sinf(f1);
        float r0, r1, i0, i1;
        if (PK) {
            const f32x2 r = (f32x2){c0, c1} * (f32x2){m0, m1}, i = (f32x2){s0, s1} * (f32x2){m0, m1};
            r0 = r[0]; r1 = r[1]; i0 = i[0]; i1 = i[1];
        } else {
            r0 = c0 * m0; OPQ(r0); i0 = s0 * m0; OPQ(i0); r1 = c1 * m1; OPQ(r1); i1 = s1 * m1; OPQ(i1);
        }
        o0 = pk_bf(r0, conj ? -i0 : i0);
        o1 = pk_bf(r1, conj ? -i1 : i1);
    };
    auto affine = [&](f32x2 a, float p, f32x2 b) -> f32x2 {
        if (PK) return a * p + b;
        float x = fmaf(a[0], p, b[0]), y = fmaf(a[1], p, b[1]);
        OPQ(x); OPQ(y);
        return (f32x2){x, y};
    };
#pragma unroll
    for (int s = 0; s < NS_; ++s) {
        u32x4 B, A[3];
#pragma unroll
        for (int h = 0; h < 2; ++h) {
            const f32x4 p0 = P[16 * s + 4 * g + 2 * h], p1 = P[16 * s + 4 * g + 2 * h + 1];
            u32 o0, o1;
            eval(affine(p0.xy, lo, (f32x2){0.f, 0.f}), affine(p0.zw, lo, (f32x2){0.f, 0.f}), true, o0, o1);
            B[2 * h] = o0; B[2 * h + 1] = o1;
#pragma unroll
            for (int e = 0; e < 3; ++e) {
                eval(affine(p0.xy, hi[e], p1.xy), affine(p0.zw, hi[e], p1.zw), false, o0, o1);
                A[e][2 * h] = o0; A[e][2 * h + 1] = o1;
            }
        }
#pragma unroll
        for (int e = 0; e < 3; ++e) ks.acc[e] = mfma16(A[e], B, ks.acc[e]);
    }
}
__device__ __forceinline__ const f32x4* kgen_params(char* smem, int wave, int step) {
    return (const f32x4*)(smem + OFF_P) + ((wave >> 1) & 1) * 128 + ((wave & 1) * 4 + step) * 16;
}
__device__ __forceinline__ void kgen_save(char* smem, int wave, int lane, const KState& ks) {
    char* p = smem + OFF_KP + wave * KP_WAVE + lane * 16;
    *(f32x4*)p = ks.acc[0];
    *(f32x4*)(p + 1024) = ks.acc[1];
    if (lane < 48) *(f32x4*)(p + 2048) = ks.acc[2];
}
__device__ __forceinline__ void kgen_load(const char* smem, int wave, int lane, KState& ks) {
    const char* p = smem + OFF_KP + wave * KP_WAVE;
    ks.acc[0] = *(const f32x4*)(p + lane * 16);
    ks.acc[1] = *(const f32x4*)(p + 1024 + lane * 16);
    ks.acc[2] = *(const f32x4*)(p + 2048 + (lane < 48 ? lane : 0) * 16);
}
__device__ __forceinline__ f32x4 kgen_sum(const char* p) {
    return (*(const f32x4*)p + *(const f32x4*)(p + KP_WAVE)) + (*(const f32x4*)(p + 4 * KP_WAVE) + *(const f32x4*)(p + 5 * KP_WAVE));
}
__device__ __forceinline__ void kgen_reduce(char* smem, int wave, int lane, u32 dskip) {
    const int c = wave >> 1, j = lane & 15, g = lane >> 4;
    char* KC = smem + OFF_KC + c * 1296;
    const char* p = smem + OFF_KP + 2 * c * KP_WAVE + lane * 16;
    if ((wave & 1) == 0) {
        f32x4 s = kgen_sum(p);
        const f32x4 t = kgen_sum(smem + OFF_KP + 2 * c * KP_WAVE + 2048 + (lane < 48 ? lane : 0) * 16);
        if (lane == 0) s[0] += c ? bf_hi(dskip) : bf_lo(dskip);
        *(u32x2*)(KC + 2 * (128 * (g >> 1) + 4 * (g & 1) + 8 * j)) = (u32x2){pk_bf(s[0], s[1]), pk_bf(s[2], s[3])};
        const u32 lo = pk_bf(t[0], t[1]), hi = pk_bf(t[2], t[3]);
        u16* o = (u16*)KC + 512 + 64 * g + j;
        if (g < 2) { o[0] = (u16)lo; o[16] = (u16)(lo >> 16); o[32] = (u16)hi; o[48] = (u16)(hi >> 16); }
        else if (lane == 32) { ((u16*)KC)[640] = (u16)lo; ((u16*)KC)[641] = 0; }
    } else {
        const f32x4 s = kgen_sum(p + 1024);
        const u32 lo = pk_bf(s[0], s[1]), hi = pk_bf(s[2], s[3]);
        if (g < 2) *(u32x2*)(KC + 2 * (256 + 64 * g + 4 * j)) = (u32x2){lo, hi};
        else {
            u32* o = (u32*)(KC + 2 * (384 + 64 * (g - 2) + 2 * j));
            o[0] = lo; o[16] = hi;
        }
    }
}

struct TapIndex {
    u16 off[2048 + 8];
    constexpr TapIndex() : off() {
        for (int i = 0; i < 8; ++i) off[i] = 641 * 2;
        for (int l = 0; l < 2048; ++l) {
            int e = 0;
            while ((l >> e) >= 256) ++e;
            int v = l >> e;
            if (e > 0) {
                const int rem = l & ((1 << e) - 1), half = 1 << (e - 1);
                if (rem > half || (rem == half && (v & 1))) ++v;
            }
            off[8 + l] = (u16)(2 * (128 * e + v));
        }
    }
};
__device__ const TapIndex tap_index = TapIndex();

__device__ __forceinline__ void s4_taps(char* smem, int ch, int i, u32x4 tix0, u32x4 tix1) {
    const char* kc = smem + OFF_KC + ch * 1296;
    u32 d[8];
#pragma unroll
    for (int j = 0; j < 8; ++j) {
        const u32 o = j < 4 ? tix0[j] : tix1[j - 4];
        d[j] = (u32)*(const u16*)(kc + (o & 0xffffu)) | ((u32)*(const u16*)(kc + (o >> 16)) << 16);
    }
    stage_taps(smem, ch, i, (u32x4){d[0], d[1], d[2], d[3]}, (u32x4){d[4], d[5], d[6], d[7]});
}

struct Frags { u32x4 A[8], B[8]; };
template <bool INIT> __device__ __forceinline__ void mma(f32x4& acc, const u32x4& a, const u32x4& b) {
    if constexpr (INIT) asm volatile("v_mfma_f32_16x16x32_bf16 %0, %1, %2, 0" : "=&v"(acc) : "v"(a), "v"(b));
    else asm volatile("v_mfma_f32_16x16x32_bf16 %0, %1, %2, %0" : "+v"(acc) : "v"(a), "v"(b));
}
template <int I> __device__ __forceinline__ void load_one(Frags& f, const char* ap, const char* bp) {
    if constexpr (I == 0) f.A[0] = *(const u32x4*)ap;
    else if constexpr (I <= 8) f.B[I - 1] = *(const u32x4*)(bp + 512 * (8 - I));
    else f.A[I - 8] = *(const u32x4*)(ap + 512 * (I - 8));
}
template <int T0, int T1, int T2, int T3> struct TileSet {
    static constexpr int tile(int i) { return i == 0 ? T0 : i == 1 ? T1 : i == 2 ? T2 : T3; }
    static constexpr int walk(int I, bool want_tile) {
        int n = 0;
        for (int a = 0; a < 8; ++a)
            for (int j = 0; j < 4; ++j)
                if (tile(j) >= a) { if (n == I) return want_tile ? tile(j) : a; ++n; }
        return -1;
    }
};
constexpr int tri_a(int I) { int a = 0; while (I >= 8 - a) { I -= 8 - a; ++a; } return a; }
constexpr int tri_b(int I) { int a = 0; while (I >= 8 - a) { I -= 8 - a; ++a; } return I; }
template <int I, bool FIRST> __device__ __forceinline__ void class_step(const Frags& cur, Frags& nxt, f32x4 (&acc)[8], const char* ap, const char* bp) {
    if constexpr (I < 36) {
        constexpr int a = tri_a(I), b = tri_b(I);
        mma<FIRST && a == 0>(acc[a + b], cur.A[a], cur.B[b]);
        if constexpr ((I & 1) == 1 && (I >> 1) < 16) load_one<(I >> 1)>(nxt, ap, bp);
        SB();
        class_step<I + 1, FIRST>(cur, nxt, acc, ap, bp);
    }
}
template <class TS, int I> __device__ __forceinline__ void set_step(const Frags& cur, Frags& nxt, f32x4 (&acc)[8], const char* ap, const char* bp) {
    if constexpr (I < 18) {
        constexpr int t = TS::walk(I, true), a = TS::walk(I, false);
        mma<false>(acc[t], cur.A[a], cur.B[t - a]);
        if constexpr (I < 16) load_one<I>(nxt, ap, bp);
        SB();
        set_step<TS, I + 1>(cur, nxt, acc, ap, bp);
    }
}
template <class TS, int I, int TO, class HOOK> __device__ __forceinline__ void set_step_h(const Frags& cur, f32x4 (&acc)[8], HOOK hook) {
    if constexpr (I < TO) {
        constexpr int t = TS::walk(I, true), a = TS::walk(I, false);
        mma<false>(acc[t], cur.A[a], cur.B[t - a]);
        hook(std::integral_constant<int, I>());
        SB();
        set_step_h<TS, I + 1, TO>(cur, acc, hook);
    }
}
template <class TS, int J> __device__ __forceinline__ void tile_out(char* Y, f32x4 (&acc)[8], const f32x4 (&o)[4]) {
    constexpr int t = TS::tile(J);
    const f32x4 s = acc[t] + o[J];
    const u32x2 pk = {pk_bf(s[0], s[1]), pk_bf(s[2], s[3])};
    *(u32x2*)(Y + 512 * t) = pk;
    SB();
}
template <int T, int I, int BASE, class HOOK> __device__ __forceinline__ void tile_run(const Frags& cur, f32x4 (&acc)[8], HOOK hook) {
    if constexpr (I <= T) {
        mma<false>(acc[T], cur.A[I], cur.B[T - I]);
        hook(std::integral_constant<int, BASE + I>());
        SB();
        tile_run<T, I + 1, BASE>(cur, acc, hook);
    }
}
template <class TS, class HOOK> __device__ __forceinline__ void set_by_tiles(const Frags& cur, f32x4 (&acc)[8], HOOK hook) {
    tile_run<TS::tile(0), 0, 0>(cur, acc, hook);
    tile_run<TS::tile(1), 0, TS::tile(0) + 1>(cur, acc, hook);
    tile_run<TS::tile(2), 0, TS::tile(0) + TS::tile(1) + 2>(cur, acc, hook);
    tile_run<TS::tile(3), 0, TS::tile(0) + TS::tile(1) + TS::tile(2) + 3>(cur, acc, hook);
}
template <class SEND, class KEEP>
__device__ __forceinline__ void conv_half(char* smem, const u32x4 (&A0)[8], const char* ap, const char* bp, int wave, int lane, int ch, u32 seq) {
    f32x4 acc[8];
    Frags f0, f1;
#pragma unroll
    for (int a = 0; a < 8; ++a) f0.A[a] = A0[a];
#pragma unroll
    for (int b = 0; b < 8; ++b) { f0.B[b] = *(const u32x4*)(bp + 512 * (7 - b)); SB(); }
    class_step<0, true>(f0, f1, acc, ap + 64, bp + 64);
    class_step<0, false>(f1, f0, acc, ap + 128, bp + 128);
    set_step<SEND, 0>(f0, f1, acc, ap + 192, bp + 192);
    f32x4* X = (f32x4*)(smem + OFF_X) + wave * 256 + lane;
    const f32x4* XP = (const f32x4*)(smem + OFF_X) + (wave ^ 1) * 256 + lane;
    volatile LAS u32* xf = (volatile LAS u32*)(LAS char*)(smem + OFF_CNT + 32);
    constexpr int e0 = SEND::tile(0), e1 = e0 + SEND::tile(1) + 1, e2 = e1 + SEND::tile(2) + 1;
    set_by_tiles<SEND>(f1, acc, [&](auto i) {
        if constexpr (i.value == e0 + 4) { asm volatile("s_nop 3" : "+v"(acc[SEND::tile(0)])); X[0] = acc[SEND::tile(0)]; }
        if constexpr (i.value == e1 + 4) { asm volatile("s_nop 3" : "+v"(acc[SEND::tile(1)])); X[64] = acc[SEND::tile(1)]; }
        if constexpr (i.value == e2 + 4) { asm volatile("s_nop 3" : "+v"(acc[SEND::tile(2)])); X[128] = acc[SEND::tile(2)]; }
    });
    u32 seen = 0;
    f32x4 o[4];
    set_step_h<KEEP, 0, 18>(f0, acc, [&](auto i) {
        if constexpr (i.value == 3) { asm volatile("s_nop 3" : "+v"(acc[SEND::tile(3)])); X[192] = acc[SEND::tile(3)]; xf[wave] = seq; }
        if constexpr (i.value == 12) seen = xf[wave ^ 1];
        if constexpr (i.value == 17) {
            asm volatile("" : "+v"(seen));
            for (u32 spins = 0; seen < seq && spins < 50 * SPIN_CAP; ++spins) seen = xf[wave ^ 1];
            o[0] = XP[0]; o[1] = XP[64];
        }
    });
    char* Y = smem + OFF_Y + ch * 4096 + (16 * (lane & 15) + 4 * (lane >> 4)) * 2;
    constexpr int k0 = KEEP::tile(0), k1 = k0 + KEEP::tile(1) + 1, k2 = k1 + KEEP::tile(2) + 1;
    set_by_tiles<KEEP>(f1, acc, [&](auto i) {
        if constexpr (i.value == 0) { o[2] = XP[128]; o[3] = XP[192]; }
        if constexpr (i.value == k1 + 3) { asm volatile("s_nop 4" : "+v"(acc[KEEP::tile(0)]), "+v"(acc[KEEP::tile(1)])); tile_out<KEEP, 0>(Y, acc, o); tile_out<KEEP, 1>(Y, acc, o); }
        if constexpr (i.value == k2 + 4) { asm volatile("s_nop 3" : "+v"(acc[KEEP::tile(2)])); tile_out<KEEP, 2>(Y, acc, o); }
    });
    asm volatile("s_nop 7" : "+v"(acc[KEEP::tile(3)]));
    tile_out<KEEP, 3>(Y, acc, o);
}
struct ConvAddr { const char* ap; const char* bp; };
__device__ __forceinline__ ConvAddr conv_addr(char* smem, int wave, int lane) {
    const int ch = (wave >> 1) & 1, hf = wave & 1;
    const int m = lane & 15, g = lane >> 4;
    ConvAddr c;
    c.ap = smem + OFF_T + ch * T_CH + (m & 7) * T_COPY + 16 * (m >> 3) + 16 * g + 256 * hf;
    c.bp = smem + OFF_U + ch * U_STRIDE + 4064 - 32 * m + 16 * g + 256 * hf - 512 * 7;
    return c;
}
__device__ __forceinline__ void conv_preload(u32x4 (&A0)[8], const ConvAddr& c) {
#pragma unroll
    for (int a = 0; a < 8; ++a) A0[a] = *(const u32x4*)(c.ap + 512 * a);
}
template <bool EARLY>
__device__ __forceinline__ void conv_col(char* smem, const ConvAddr& c, int wave, int lane, u32 seq) {
    u32x4 A0[8];
    if (EARLY) conv_preload(A0, c);
    BAR();
    if (!EARLY) conv_preload(A0, c);
    if ((wave & 1) == 0) conv_half<TileSet<1, 3, 4, 6>, TileSet<0, 2, 5, 7>>(smem, A0, c.ap, c.bp, wave, lane, wave >> 1, seq);
    else conv_half<TileSet<0, 2, 5, 7>, TileSet<1, 3, 4, 6>>(smem, A0, c.ap, c.bp, wave, lane, wave >> 1, seq);
    STAMP(26 + seq);
    BAR();
}
template <class PRE>
__device__ __forceinline__ void conv_row(char* smem, int wave, int lane, rsrc_t rs, int id, int fill0, int st, PRE pre) {
    const u32x4 sent = {SENT, SENT, SENT, SENT};
    const u32 off = WS_H + (fill0 < 0 ? 0 : fill0) * WS_HS + id * 8192 + ((wave - 4) * 64 + lane) * 16;
    const u64 t0 = __builtin_amdgcn_s_memrealtime();
    if (fill0 >= 0) { st_dev(rs, off, sent); st_dev(rs, off + 4096, sent); }
    pre();
    if (fill0 >= 0) {
        wait_until(t0 + FILL_GAP);
        st_dev(rs, off + WS_HS, sent); st_dev(rs, off + WS_HS + 4096, sent);
        WAIT_VM(2);
    }
    STAMP(st);
    BAR();
}

__device__ __forceinline__ void row_dma(char* smem, const char* q, const char* k, const char* v, int id, int wave, int lane, int s) {
    const int slot = wave + 8 * s;
    int row = 8 * id + (slot < 12 ? slot - 4 : slot < 20 ? slot - 12 : slot - 20);
    row = row < 0 ? 0 : row;
    const char* src = slot < 12 ? k : slot < 20 ? v : q;
    lds_dma16(src + row * 1024 + lane * 16, (u32)(size_t)(LAS char*)(smem + OFF_R) + slot * R_ROW);
}

__device__ __forceinline__ u32x4 kv_piece(u32 rp, u32x2 sk, int id) {
    u32x2 k0, k1, k2, v0, v1;
    static_assert(4 * R_ROW == 4352, "offsets below");
    asm volatile("ds_read_b64_tr_b16 %0, %5\n\tds_read_b64_tr_b16 %1, %5 offset:4352\n\tds_read_b64_tr_b16 %2, %5 offset:8704\n\t"
                 "ds_read_b64_tr_b16 %3, %5 offset:13056\n\tds_read_b64_tr_b16 %4, %5 offset:17408\n\ts_waitcnt lgkmcnt(0)"
                 : "=&v"(k0), "=&v"(k1), "=&v"(k2), "=&v"(v0), "=&v"(v1) : "v"(rp) : "memory");
    const u32 first = id ? 0xffffffffu : 0u;
    const u32 kp = k0[0] & first, kq = k0[1] & first;
    const float kf[12] = {0.f, bf_hi(kp), bf_lo(kq), bf_hi(kq), bf_lo(k1[0]), bf_hi(k1[0]),
                          bf_lo(k1[1]), bf_hi(k1[1]), bf_lo(k2[0]), bf_hi(k2[0]), bf_lo(k2[1]), bf_hi(k2[1])};
    float vf[8];
    unpack8((u32x4){v0[0], v0[1], v1[0], v1[1]}, vf);
    const float w0 = bf_lo(sk[0]), w1 = bf_hi(sk[0]), w2 = bf_lo(sk[1]), w3 = bf_hi(sk[1]);
    float kv[8];
#pragma unroll
    for (int j = 0; j < 8; ++j) kv[j] = fmaf(kf[j + 4], w0, fmaf(kf[j + 3], w1, fmaf(kf[j + 2], w2, kf[j + 1] * w3))) * vf[j];
    return pack8(kv);
}

__global__ void __launch_bounds__(512, 2)
h3s4_kernel(const char* __restrict__ q, const char* __restrict__ k, const char* __restrict__ v,
            const u32x2* __restrict__ shiftk, const u32x4* __restrict__ ssmk, const u32* __restrict__ ssmD,
            const u16* __restrict__ Are, const u16* __restrict__ Aim, const u16* __restrict__ Bre, const u16* __restrict__ Bim,
            const u16* __restrict__ Cre, const u16* __restrict__ Cim, const u16* __restrict__ logdt, const u32* __restrict__ Dskip,
            u16* __restrict__ out, char* ws
            ) {
    __shared__ __attribute__((aligned(16))) char smem[LDS_BYTES];
    const int tid = threadIdx.x, lane = tid & 63, wave = __builtin_amdgcn_readfirstlane(tid >> 6);
    const int id = (blockIdx.x & 7) * 32 + (blockIdx.x >> 3);
    const rsrc_t rs = __builtin_amdgcn_make_buffer_rsrc(ws, 0, WS_USED, 0x00020000);
    const u32x4 desc = {(u32)(size_t)ws, (u32)((size_t)ws >> 32) & 0xffffu, WS_USED, 0x00020000u};
    STAMP(0);

    u32 code_pf = 0;
    {
        u64 pc; u32 len;
        asm volatile("s_getpc_b64 %0\nh3s4_text_a:\n\ts_mov_b32 %1, h3s4_text_b - h3s4_text_a" : "=s"(pc), "=s"(len));
        const u32 off = ((blockIdx.x >> 3) * 8 + wave) * 256 + lane * 4;
        if (off + 4 <= len) asm volatile("global_load_dword %0, %1, %2" : "=v"(code_pf) : "v"(off), "s"(pc) : "memory");
    }
    const u32x4 sent = {SENT, SENT, SENT, SENT};
    const u32 dA = ssmD[id], dB = Dskip[id];
    MEMFENCE(); SB();
    st_dev(rs, WS_H + 0 * WS_HS + id * 8192 + tid * 16, sent);
    MEMFENCE(); SB();
    if (wave < 4) {
        u32x2 skB = ld_b64(shiftk, (256 + tid) * 8);
        row_dma(smem, q, k, v, id, wave, lane, 0); row_dma(smem, q, k, v, id, wave, lane, 1); row_dma(smem, q, k, v, id, wave, lane, 2);
        MEMFENCE(); SB();
        row_dma(smem, q, k, v, id, wave, lane, 3);
        u32x4 tap_own = ld_b128(ssmk, (2 * id) * 4096 + tid * 16), tap_prev = ld_b128(ssmk, (2 * id) * 4096 + (tid ? tid - 1 : 0) * 16);
        const int pd = 2 * id + (tid >> 7), po = (pd * NS + (tid & 127)) * 2;
        u32 r_dt = ld_u16(logdt, pd * 2), r_are = ld_u16(Are, po), r_aim = ld_u16(Aim, po), r_bre = ld_u16(Bre, po);
        u32 r_bim = ld_u16(Bim, po), r_cre = ld_u16(Cre, po), r_cim = ld_u16(Cim, po);
        u32x4 tix0 = ld_b128(tap_index.off, tid * 16), tix1 = ld_b128(tap_index.off, tid * 16 + 16);
        MEMFENCE(); SB();
        st_dev(rs, WS_H + 1 * WS_HS + id * 8192 + tid * 16, sent);
        MEMFENCE(); SB();
        const u32 epoch = launch_tag(ws);
        asm volatile("s_waitcnt vmcnt(16)\n\ts_barrier" ::: "memory");
        if (tid < 32) __hip_atomic_store((GAS u32*)(ws + WS_FLAGS) + tid * 256 + id, epoch, RLX_AGENT);
        STAMP(19);
        if (tid < 60) *(u32x4*)(smem + OFF_U + (tid / 30) * U_STRIDE + 4096 + (tid % 30) * 16) = (u32x4){0, 0, 0, 0};
        else if (tid >= 64 && tid < 80) *(u32x4*)(smem + OFF_T + (tid - 64) * T_COPY) = (u32x4){0, 0, 0, 0};
        asm volatile("s_waitcnt vmcnt(13)" : "+v"(skB) :: "memory");
        BAR();
        STAMP(1);
        publish(rs, WS_H + id * 8192 + (256 + tid) * 16,
                kv_piece((u32)(size_t)(LAS char*)(smem + OFF_R + ((lane & 15) >> 2) * R_ROW + (256 + 64 * wave + 16 * (lane >> 4) + 4 * (lane & 3)) * 2), skB, id));
        asm volatile("s_waitcnt vmcnt(1)"
                     : "+v"(tap_own), "+v"(tap_prev), "+v"(r_dt), "+v"(r_are), "+v"(r_aim), "+v"(r_bre), "+v"(r_bim), "+v"(r_cre),
                       "+v"(r_cim), "+v"(tix0), "+v"(tix1) :: "memory");
        cnt_arrive(smem, 6, lane);

        {
            const float dt = rbf(__builtin_amdgcn_exp2f(bf_lo(r_dt) * 1.4426950408889634f));
            const float are = dt * bf_lo(r_are), aim = dt * bf_lo(r_aim);
            const float br = dt * bf_lo(r_bre), bi = dt * bf_lo(r_bim);
            const float cr = bf_lo(r_cre), ci = bf_lo(r_cim);
            const float wr = cr * br - ci * bi, wi = cr * bi + ci * br;
            const float ar = fabsf(wr), ai = fabsf(wi), mx = fmaxf(ar, ai), mn = fminf(ar, ai);
            const float ratio = mx > 0.f ? mn * __builtin_amdgcn_rcpf(mx) : 0.f, r2 = ratio * ratio;
            float at = 0.0028662257f;
            at = fmaf(at, r2, -0.0161657367f); at = fmaf(at, r2, 0.0429096138f); at = fmaf(at, r2, -0.0752896400f);
            at = fmaf(at, r2, 0.1065626393f); at = fmaf(at, r2, -0.1420889944f); at = fmaf(at, r2, 0.1999355085f);
            at = fmaf(at, r2, -0.3333314528f); at = fmaf(at, r2, 1.0f);
            float ph = at * ratio * 0.15915494309189535f;
            ph = ai > ar ? 0.25f - ph : ph;
            ph = wr < 0.f ? 0.5f - ph : ph;
            ph = wi < 0.f ? -ph : ph;
            float* p = (float*)(smem + OFF_P) + (tid >> 1) * 8 + (tid & 1);
            p[0] = are * 1.4426950408889634f;
            p[2] = aim * 0.15915494309189535f;
            p[4] = __builtin_amdgcn_logf(mx) + 0.5f * __builtin_amdgcn_logf(r2 + 1.0f);
            p[6] = ph;
        }
        cnt_arrive(smem, 7, lane);
        if (tid == 0) {
            tap_prev = (u32x4){0, 0, 0, 0};
            tap_own[0] = (tap_own[0] & 0xffff0000u) | (pk_bf(bf_lo(tap_own[0]) + bf_lo(dA), 0.f) & 0xffffu);
        }
        stage_taps(smem, 0, tid, tap_prev, tap_own);
        cnt_arrive(smem, 0, lane);
        STAMP(2);
        if (C_PRE) {
            cnt_wait(smem, 7, 4);
            KState ks;
            kgen_init(ks);
            kgen_steps<C_PRE, true>(kgen_params(smem, wave, 0), lane, ks);
            kgen_save(smem, wave, lane, ks);
        }

        const ConvAddr ca = conv_addr(smem, wave, lane);
        asm volatile("" :: "v"(code_pf));
        cnt_wait(smem, 0, 8);
        STAMP(4);
#pragma nounroll
        for (u32 seq = 1; seq <= 3; ++seq) {
            if (seq == 2) {
                {
                    KState ks;
                    if (C_PRE) kgen_load(smem, wave, lane, ks); else kgen_init(ks);
                    kgen_steps<4 - R_STEPS - C_PRE, true>(kgen_params(smem, wave, C_PRE), lane, ks);
                    kgen_save(smem, wave, lane, ks);
                }
                STAMP(7);
                cnt_arrive(smem, 1, lane);
                cnt_wait(smem, 1, 8);
                kgen_reduce(smem, wave, lane, dB);
                cnt_arrive(smem, 2, lane);
                cnt_wait(smem, 2, 4);
                STAMP(23);
                s4_taps(smem, 0, tid, tix0, tix1);
                cnt_arrive(smem, 3, lane);
                cnt_wait(smem, 3, 8);
                STAMP(25);
            }
            conv_col<true>(smem, ca, wave, lane, seq);
            STAMP(seq == 1 ? 6 : seq == 2 ? 11 : 15);
        }
    } else {
        const int r = tid - 256, w = wave - 4;
        u32x2 skA = ld_b64(shiftk, r * 8);
        row_dma(smem, q, k, v, id, wave, lane, 0); row_dma(smem, q, k, v, id, wave, lane, 1);
        MEMFENCE(); SB();
        row_dma(smem, q, k, v, id, wave, lane, 2);
        u32x4 tap_own = ld_b128(ssmk, (2 * id + 1) * 4096 + r * 16), tap_prev = ld_b128(ssmk, (2 * id + 1) * 4096 + (r ? r - 1 : 0) * 16);
        u32x4 tix0 = ld_b128(tap_index.off, r * 16), tix1 = ld_b128(tap_index.off, r * 16 + 16);
        MEMFENCE(); SB();
        st_dev(rs, WS_H + 1 * WS_HS + id * 8192 + tid * 16, sent);
        MEMFENCE(); SB();
        const u32 epoch = launch_tag(ws);
        asm volatile("s_waitcnt vmcnt(8)\n\ts_barrier" ::: "memory");
        ((u32*)(smem + OFF_RED))[r] = SENT;
        if (r < 16) ((u32*)(smem + OFF_CNT))[r] = 0u;
        asm volatile("s_waitcnt vmcnt(6)" : "+v"(skA) :: "memory");
        BAR();
        STAMP(1);

        const u32 own_a = id * 8192 + r * 16, own_b = own_a + 4096;
        const u32 col_a = (r >> 1) * 8192 + (2 * id + (r & 1)) * 16, col_b = col_a + 128 * 8192;
        const u32 row_a = (r >> 1) * 8192 + id * 32 + (r & 1) * 16, row_b = row_a + 128 * 8192;
        char* const u_dst = smem + OFF_U + (r & 1) * U_STRIDE + 4080 - (r >> 1) * 16;
        const char* const y_src = smem + OFF_Y + (r & 1) * 4096 + (r >> 1) * 16;
        float* red = (float*)(smem + OFF_RED);

        Rows x, u0;
        u64 t_s;
        const u32 flag_a = WS_FLAGS + (blockIdx.x & 31) * 1024 + (r >> 1) * 4;
        u32 fl_a, fl_b;
        const u32 rp = (u32)(size_t)(LAS char*)(smem + OFF_R + ((lane & 15) >> 2) * R_ROW + (64 * w + 16 * (lane >> 4) + 4 * (lane & 3)) * 2);
        publish(rs, WS_H + own_a, kv_piece(rp, skA, id));
        t_s = __builtin_amdgcn_s_memrealtime();
        __builtin_amdgcn_s_setprio(2);
        asm volatile("s_waitcnt vmcnt(1)" : "+v"(tap_own), "+v"(tap_prev), "+v"(tix0), "+v"(tix1) :: "memory");
        fl_a = ld_flag(ws, flag_a); fl_b = ld_flag(ws, flag_a + 512);
        cnt_arrive(smem, 6, lane);
        STAMP(2);
        u32 touch = 0;
        if (wave == 6) {
            if (lane < 4) touch = __builtin_amdgcn_raw_buffer_load_b32(rs, WS_H + (2 + lane) * WS_HS + id * 8192, 0, 16);
            else if (lane == 4) touch = ((const u32*)out)[id * 2048];
        }
        if (r == 0) {
            tap_prev = (u32x4){0, 0, 0, 0};
            tap_own[0] = (tap_own[0] & 0xffff0000u) | (pk_bf(bf_lo(tap_own[0]) + bf_hi(dA), 0.f) & 0xffffu);
        }
        stage_taps(smem, 1, r, tap_prev, tap_own);
        cnt_arrive(smem, 0, lane);
        STAMP(3);

        Piece2 pc, pr;
        Rows yA, u;
        auto stage_input = [&](u32 h) {
            poll2_finish(pc, desc, WS_H + h * WS_HS + col_a, WS_H + h * WS_HS + col_b);
            *(u32x4*)u_dst = reverse8(pc.a);
            *(u32x4*)(u_dst - 2048) = reverse8(pc.b);
        };
        auto send_result = [&](u32 h) {
            publish(rs, WS_H + h * WS_HS + own_a, *(const u32x4*)y_src);
            publish(rs, WS_H + h * WS_HS + own_b, *(const u32x4*)(y_src + 2048));
            t_s = __builtin_amdgcn_s_memrealtime();
        };
        auto fetch_rows = [&](u32 h) {
            wait_until(t_s + POLL_DELAY);
            pr = poll2_issue(rs, WS_H + h * WS_HS + row_a, WS_H + h * WS_HS + row_b);
            poll2_finish(pr, desc, WS_H + h * WS_HS + row_a, WS_H + h * WS_HS + row_b);
        };

        wait_until(t_s + POLL_DELAY);
        {
            asm volatile("" :: "v"(fl_a), "v"(fl_b));
            const bool ok = fl_a == epoch && fl_b == epoch;
            pc = poll2_issue(rs, WS_H + col_a, WS_H + col_b);
            poll2_finish_flagged(pc, desc, WS_H + col_a, WS_H + col_b, flag_a, flag_a + 512, ok, epoch);
            *(u32x4*)u_dst = reverse8(pc.a);
            *(u32x4*)(u_dst - 2048) = reverse8(pc.b);
        }
        STAMP(21);
        asm volatile("" :: "v"(touch));
        asm volatile("" :: "v"(code_pf));
        BAR();
        STAMP(5);
        if (blockIdx.x == 0 && r == 0) __hip_atomic_store((GAS u32*)(ws + WS_EPOCH), epoch, RLX_AGENT);
        conv_row(smem, wave, lane, rs, id, 2, 27, [&] {
            cnt_wait(smem, 6, 8);
            if (wave == 7 && lane < 32) __hip_atomic_store((GAS u32*)(ws + WS_FLAGS + 32768) + lane * 256 + id, epoch, RLX_AGENT);
            u32x2 qq[2][2];
            asm volatile("ds_read_b64_tr_b16 %0, %4 offset:21760\n\tds_read_b64_tr_b16 %1, %4 offset:26112\n\t"
                         "ds_read_b64_tr_b16 %2, %4 offset:22272\n\tds_read_b64_tr_b16 %3, %4 offset:26624\n\ts_waitcnt lgkmcnt(0)"
                         : "=&v"(qq[0][0]), "=&v"(qq[0][1]), "=&v"(qq[1][0]), "=&v"(qq[1][1]) : "v"(rp) : "memory");
            unpack_rows((u32x4){qq[0][0][0], qq[0][0][1], qq[0][1][0], qq[0][1][1]}, x.a);
            unpack_rows((u32x4){qq[1][0][0], qq[1][0][1], qq[1][1][0], qq[1][1][1]}, x.b);
            __builtin_amdgcn_s_setprio(0);
            KState ks;
            kgen_init(ks);
            kgen_steps<R_STEPS, false>(kgen_params(smem, wave, 4 - R_STEPS), lane, ks);
            kgen_save(smem, wave, lane, ks);
            cnt_arrive(smem, 1, lane);
            __builtin_amdgcn_s_setprio(2);
        });
        send_result(1);
        STAMP(6);

#pragma nounroll
        for (u32 it = 0; it < 2; ++it) {
            MEMFENCE();
            const u32 fb_a = ld_flag(ws, flag_a + 32768), fb_b = ld_flag(ws, flag_a + 32768 + 512);
            MEMFENCE();
            layer_norm(x, u0, red + 128 * it, lane, w);
            if (it == 0) {
                wait_until(t_s + POLL_DELAY);
                asm volatile("" :: "v"(fb_a), "v"(fb_b));
                const bool ok = fb_a == epoch && fb_b == epoch;
                pr = poll2_issue(rs, WS_H + WS_HS + row_a, WS_H + WS_HS + row_b);
                poll2_finish_flagged(pr, desc, WS_H + WS_HS + row_a, WS_H + WS_HS + row_b, flag_a + 32768, flag_a + 32768 + 512, ok, epoch);
                STAMP(8);
                unpack_rows(pr.a, yA.a);
                unpack_rows(pr.b, yA.b);
            }
#pragma unroll
            for (int i = 0; i < 4; ++i) { x.a[i] += u0.a[i] * yA.a[i]; x.b[i] += u0.b[i] * yA.b[i]; }
            round_rows(x.a);
            round_rows(x.b);
            layer_norm(x, u, red + 128 * it + 64, lane, w);
            publish(rs, WS_H + (2 + 2 * it) * WS_HS + own_a, pack_rows(u.a));
            publish(rs, WS_H + (2 + 2 * it) * WS_HS + own_b, pack_rows(u.b));
            t_s = __builtin_amdgcn_s_memrealtime();
            STAMP(it ? 13 : 9);

            wait_until(t_s + POLL_DELAY);
            pc = poll2_issue(rs, WS_H + (2 + 2 * it) * WS_HS + col_a, WS_H + (2 + 2 * it) * WS_HS + col_b);
            if (it == 0) {
                cnt_wait(smem, 2, 4);
                s4_taps(smem, 1, r, tix0, tix1);
                cnt_arrive(smem, 3, lane);
            }
            stage_input(2 + 2 * it);
            BAR();
            STAMP(it ? 14 : 10);
            conv_row(smem, wave, lane, rs, id, it ? -1 : 4, 28, [] {});
            send_result(3 + 2 * it);
            STAMP(it ? 15 : 11);

            fetch_rows(3 + 2 * it);
            STAMP(it ? 16 : 12);
            Rows y;
            unpack_rows(pr.a, y.a);
            unpack_rows(pr.b, y.b);
#pragma unroll
            for (int i = 0; i < 4; ++i) { x.a[i] += y.a[i]; x.b[i] += y.b[i]; }
            pr.a = round_rows(x.a);
            pr.b = round_rows(x.b);
        }
        {
            u16* dst = out + (8 * id) * DM + r;
#pragma unroll
            for (int j = 0; j < 4; ++j) {
                dst[(2 * j) * DM] = (u16)pr.a[j];
                dst[(2 * j) * DM + 256] = (u16)pr.b[j];
                dst[(2 * j + 1) * DM] = (u16)(pr.a[j] >> 16);
                dst[(2 * j + 1) * DM + 256] = (u16)(pr.b[j] >> 16);
            }
        }
        STAMP(17);
    }
    asm volatile("h3s4_text_b:");
    asm volatile("s_waitcnt vmcnt(0)" ::: "memory");
    STAMP(18);
    BAR();
    if (tid == 0) {
        const u64* st = (const u64*)(smem + OFF_ST);
        const u64 dur = st[DIAG_B] - st[DIAG_A];
        const u64 until = __builtin_amdgcn_s_memrealtime() + DIAG * dur;
        while (__builtin_amdgcn_s_memrealtime() < until) __builtin_amdgcn_s_sleep(2);
    }
}

extern "C" void kernel_launch(void* const* d_in, const int* in_sizes, int n_in,
                              void* d_out, int out_size, void* d_ws, size_t ws_size,
                              hipStream_t stream) {
    h3s4_kernel<<<256, 512, 0, stream>>>(
        (const char*)d_in[0], (const char*)d_in[1], (const char*)d_in[2], (const u32x2*)d_in[3], (const u32x4*)d_in[4],
        (const u32*)d_in[5], (const u16*)d_in[7], (const u16*)d_in[8], (const u16*)d_in[9], (const u16*)d_in[10],
        (const u16*)d_in[11], (const u16*)d_in[12], (const u16*)d_in[13], (const u32*)d_in[14], (u16*)d_out, (char*)d_ws
        );
}
```

```cpp
#define DIAG 0
#define DIAG_A 0
#define DIAG_B 0
#include <hip/hip_runtime.h>
#include <hip/hip_bf16.h>
#include <stdint.h>
#include <type_traits>

typedef unsigned int u32;
typedef unsigned short u16;
typedef unsigned long long u64;
typedef __attribute__((ext_vector_type(4))) float f32x4;
typedef __attribute__((ext_vector_type(2))) float f32x2;
typedef __attribute__((ext_vector_type(4))) u32 u32x4;
typedef __attribute__((ext_vector_type(2))) u32 u32x2;
typedef __attribute__((ext_vector_type(4))) short s16x4;
typedef __attribute__((ext_vector_type(8))) __bf16 bf16x8;
typedef __attribute__((ext_vector_type(2))) __bf16 bf16x2;
#define LAS __attribute__((address_space(3)))
#define GAS __attribute__((address_space(1)))
#define RLX_AGENT __ATOMIC_RELAXED, __HIP_MEMORY_SCOPE_AGENT
#define SB() __builtin_amdgcn_sched_barrier(0)
#define BAR() asm volatile("s_waitcnt lgkmcnt(0)\n\ts_barrier" ::: "memory")
#define MEMFENCE() asm volatile("" ::: "memory")
#define WAIT_VM(n) do { MEMFENCE(); __builtin_amdgcn_s_waitcnt(0x0F70 | ((n) & 15) | (((n) >> 4) << 14)); MEMFENCE(); } while (0)

constexpr int DM = 512;
constexpr int NS = 128;
constexpr u32 SENT = 0xffffffffu;
constexpr u32 SPIN_CAP = 20000;
#define FILL_GAP 70
#define C_PRE 1
#define R_STEPS 1
#define POLL_DELAY 45

constexpr u32 WS_EPOCH = 0;
constexpr u32 WS_FLAGS = 4096;
constexpr u32 WS_H = 1u << 20;
constexpr u32 WS_HS = 2u << 20;
constexpr u32 WS_USED = WS_H + 6 * WS_HS;

constexpr int T_COPY = 4160, T_CH = 8 * T_COPY;
constexpr int U_STRIDE = 4672;
constexpr int R_ROW = 1088;
constexpr int KP_WAVE = 2816;
constexpr int OFF_T = 0;
constexpr int OFF_U = OFF_T + 2 * T_CH;
constexpr int OFF_X = OFF_U + 2 * U_STRIDE;
constexpr int OFF_Y = OFF_X + 16384;
constexpr int OFF_R = OFF_Y + 8192;
constexpr int OFF_P = OFF_R + 28 * R_ROW;
constexpr int OFF_KP = OFF_P + 4096;
constexpr int OFF_KC = OFF_KP + 8 * KP_WAVE;
constexpr int OFF_RED = OFF_KC + 2 * 648 * 2;
constexpr int OFF_CNT = OFF_RED + 1024;
constexpr int OFF_ST = OFF_CNT + 64;
constexpr int LDS_BYTES = OFF_ST + 512;
static_assert(LDS_BYTES <= 163840, "LDS");

#define STAMP(i) do { if ((threadIdx.x & 255) == 0) ((u64*)(smem + OFF_ST))[(i) + (threadIdx.x ? 32 : 0)] = __builtin_amdgcn_s_memrealtime(); } while (0)

__device__ __forceinline__ float bf_lo(u32 p) { return __builtin_bit_cast(float, p << 16); }
__device__ __forceinline__ float bf_hi(u32 p) { return __builtin_bit_cast(float, p & 0xffff0000u); }
__device__ __forceinline__ float bf_u16(u16 p) { return __builtin_bit_cast(float, (u32)p << 16); }
__device__ __forceinline__ u32 pk_bf(float lo, float hi) {
    bf16x2 v = {(__bf16)lo, (__bf16)hi};
    return __builtin_bit_cast(u32, v);
}
__device__ __forceinline__ float as_f(u32 x) { return __builtin_bit_cast(float, x); }
__device__ __forceinline__ u32 as_u(float x) { return __builtin_bit_cast(u32, x); }
__device__ __forceinline__ float rbf(float x) { return bf_lo(pk_bf(x, x)); }
__device__ __forceinline__ void unpack8(u32x4 p, float (&f)[8]) {
#pragma unroll
    for (int i = 0; i < 4; ++i) { f[2 * i] = bf_lo(p[i]); f[2 * i + 1] = bf_hi(p[i]); }
}
__device__ __forceinline__ u32x4 pack8(const float (&f)[8]) {
    return (u32x4){pk_bf(f[0], f[1]), pk_bf(f[2], f[3]), pk_bf(f[4], f[5]), pk_bf(f[6], f[7])};
}
__device__ __forceinline__ u32x4 reverse8(u32x4 p) {
    return (u32x4){__builtin_amdgcn_alignbit(p[3], p[3], 16), __builtin_amdgcn_alignbit(p[2], p[2], 16),
                   __builtin_amdgcn_alignbit(p[1], p[1], 16), __builtin_amdgcn_alignbit(p[0], p[0], 16)};
}
template <int CTRL> __device__ __forceinline__ float dppf(float v) {
    return __builtin_bit_cast(float, __builtin_amdgcn_update_dpp(0, __builtin_bit_cast(int, v), CTRL, 0xf, 0xf, true));
}

__device__ __forceinline__ void lds_dma16(const char* src, u32 lds) {
    u32 keep;
    asm volatile("s_mov_b32 %0, m0\n\ts_mov_b32 m0, %2\n\ts_nop 0\n\tglobal_load_lds_dwordx4 %1, off\n\ts_mov_b32 m0, %0"
                 : "=&s"(keep) : "v"(src), "s"(lds) : "memory");
}

__device__ __forceinline__ u32 ld_u16(const void* base, u32 off) {
    u32 r;
    asm volatile("global_load_ushort %0, %1, %2" : "=v"(r) : "v"(off), "s"(base) : "memory");
    return r;
}
__device__ __forceinline__ u32x2 ld_b64(const void* base, u32 off) {
    u32x2 r;
    asm volatile("global_load_dwordx2 %0, %1, %2" : "=v"(r) : "v"(off), "s"(base) : "memory");
    return r;
}
__device__ __forceinline__ u32x4 ld_b128(const void* base, u32 off) {
    u32x4 r;
    asm volatile("global_load_dwordx4 %0, %1, %2" : "=v"(r) : "v"(off), "s"(base) : "memory");
    return r;
}

typedef __amdgpu_buffer_rsrc_t rsrc_t;
__device__ __forceinline__ u32x4 ld_dev(rsrc_t rs, u32 off) { return __builtin_amdgcn_raw_buffer_load_b128(rs, off, 0, 16); }
__device__ __forceinline__ void st_dev(rsrc_t rs, u32 off, u32x4 v) { __builtin_amdgcn_raw_buffer_store_b128(v, rs, off, 0, 16); }
__device__ __forceinline__ void publish(rsrc_t rs, u32 off, u32x4 v) {
#pragma unroll
    for (int i = 0; i < 4; ++i) v[i] = v[i] < SENT - 1u ? v[i] : SENT - 1u;
    st_dev(rs, off, v);
}
__device__ __forceinline__ u32 launch_tag(const char* ws) {
    u32 e;
    asm volatile("s_load_dword %0, %1, 0x0\n\ts_waitcnt lgkmcnt(0)" : "=&s"(e) : "s"(ws + WS_EPOCH) : "memory");
    return e + 1u;
}
__device__ __forceinline__ void wait_until(u64 t) {
    while (__builtin_amdgcn_s_memrealtime() < t) __builtin_amdgcn_s_sleep(1);
}
__device__ __forceinline__ u32x4 poll_issue(rsrc_t rs, u32 off) {
    MEMFENCE();
    u32x4 x = ld_dev(rs, off);
    MEMFENCE();
    return x;
}
struct Piece2 { u32x4 a, b; };
__device__ __forceinline__ Piece2 poll2_issue(rsrc_t rs, u32 off_a, u32 off_b) {
    MEMFENCE();
    Piece2 p = {ld_dev(rs, off_a), ld_dev(rs, off_b)};
    MEMFENCE();
    return p;
}
__device__ __forceinline__ bool has_sentinel(const u32x4& x) {
    u32 mx = x[0] > x[1] ? x[0] : x[1];
    mx = mx > x[2] ? mx : x[2];
    mx = mx > x[3] ? mx : x[3];
    return mx == SENT;
}
__device__ __forceinline__ void poll2_finish(Piece2& p, u32x4 desc, u32 off_a, u32 off_b) {
#pragma nounroll
    for (u32 spins = 0; spins < SPIN_CAP; ++spins) {
        const bool bad = has_sentinel(p.a) || has_sentinel(p.b);
        if (__builtin_amdgcn_ballot_w64(bad) == 0ull) break;
        __builtin_amdgcn_s_sleep(1);
        if (bad) asm volatile("buffer_load_dwordx4 %0, %2, %4, 0 offen sc1\n\tbuffer_load_dwordx4 %1, %3, %4, 0 offen sc1\n\ts_waitcnt vmcnt(0)"
                              : "+v"(p.a), "+v"(p.b) : "v"(off_a), "v"(off_b), "s"(desc) : "memory");
    }
}
__device__ __forceinline__ u32 ld_flag(const char* ws, u32 off) { return __hip_atomic_load((const GAS u32*)(ws + off), RLX_AGENT); }
__device__ __forceinline__ void poll2_finish_flagged(Piece2& p, u32x4 desc, u32 off_a, u32 off_b, u32 fo_a, u32 fo_b, bool ok, u32 epoch) {
#pragma nounroll
    for (u32 spins = 0; spins < SPIN_CAP; ++spins) {
        const bool bad = !ok || has_sentinel(p.a) || has_sentinel(p.b);
        if (__builtin_amdgcn_ballot_w64(bad) == 0ull) break;
        if (!ok) {
            u32 fa, fb;
            asm volatile("buffer_load_dword %0, %2, %4, 0 offen sc1\n\tbuffer_load_dword %1, %3, %4, 0 offen sc1\n\ts_waitcnt vmcnt(0)"
                         : "=&v"(fa), "=&v"(fb) : "v"(fo_a), "v"(fo_b), "s"(desc) : "memory");
            ok = fa == epoch && fb == epoch;
        }
        if (bad) asm volatile("buffer_load_dwordx4 %0, %2, %4, 0 offen sc1\n\tbuffer_load_dwordx4 %1, %3, %4, 0 offen sc1\n\ts_waitcnt vmcnt(0)"
                              : "+v"(p.a), "+v"(p.b) : "v"(off_a), "v"(off_b), "s"(desc) : "memory");
    }
}

__device__ __forceinline__ void cnt_arrive(char* smem, int k, int lane) {
    asm volatile("s_waitcnt lgkmcnt(0)" ::: "memory");
    if (lane == 0) __hip_atomic_fetch_add((LAS u32*)(smem + OFF_CNT) + k, 1u, __ATOMIC_RELAXED, __HIP_MEMORY_SCOPE_WORKGROUP);
}
__device__ __forceinline__ void cnt_wait(char* smem, int k, u32 target) {
    u32 spins = 0;
    while (__hip_atomic_load((LAS u32*)(smem + OFF_CNT) + k, __ATOMIC_RELAXED, __HIP_MEMORY_SCOPE_WORKGROUP) < target) {
        if (++spins > 50 * SPIN_CAP) break;
        __builtin_amdgcn_s_sleep(1);
    }
    MEMFENCE();
}

struct Rows { f32x2 a[4], b[4]; };
__device__ __forceinline__ void unpack_rows(u32x4 p, f32x2 (&f)[4]) {
#pragma unroll
    for (int i = 0; i < 4; ++i) f[i] = (f32x2){bf_lo(p[i]), bf_hi(p[i])};
}
__device__ __forceinline__ u32x4 pack_rows(const f32x2 (&f)[4]) {
    return (u32x4){pk_bf(f[0][0], f[0][1]), pk_bf(f[1][0], f[1][1]), pk_bf(f[2][0], f[2][1]), pk_bf(f[3][0], f[3][1])};
}
__device__ __forceinline__ u32x4 round_rows(f32x2 (&f)[4]) {
    u32x4 p = pack_rows(f);
    asm("" : "+v"(p));
    unpack_rows(p, f);
    return p;
}
__device__ __forceinline__ void layer_norm(const Rows& x, Rows& u, float* red, int lane, int w) {
#pragma clang fp contract(fast)
    u32 a[8], b[8];
#pragma unroll
    for (int i = 0; i < 4; ++i) {
        const f32x2 s1 = x.a[i] + x.b[i], s2 = x.a[i] * x.a[i] + x.b[i] * x.b[i];
        a[2 * i] = as_u(s1[0]); a[2 * i + 1] = as_u(s1[1]);
        b[2 * i] = as_u(s2[0]); b[2 * i + 1] = as_u(s2[1]);
    }
    SB();
#pragma unroll
    for (int j = 0; j < 8; ++j) {
        const auto s = __builtin_amdgcn_permlane32_swap(a[j], b[j], false, false);
        a[j] = s[0]; b[j] = s[1];
    }
    SB();
    float r[8];
#pragma unroll
    for (int j = 0; j < 8; ++j) r[j] = as_f(a[j]) + as_f(b[j]);
    SB();
#pragma unroll
    for (int j = 0; j < 4; ++j) {
        const auto s = __builtin_amdgcn_permlane16_swap(as_u(r[j]), as_u(r[j + 4]), false, false);
        a[j] = s[0]; b[j] = s[1];
    }
    SB();
    float s4[4];
#pragma unroll
    for (int j = 0; j < 4; ++j) s4[j] = as_f(a[j]) + as_f(b[j]);
    SB();
#pragma unroll
    for (int j = 0; j < 4; ++j) s4[j] += dppf<0x128>(s4[j]);
    const float t0 = (lane & 8) ? s4[2] : s4[0], t1 = (lane & 8) ? s4[3] : s4[1];
    const float c0 = t0 + dppf<0x141>(t0), c1 = t1 + dppf<0x141>(t1);
    float s = (lane & 4) ? c1 : c0;
    s += dppf<0xB1>(s);
    s += dppf<0x4E>(s);
    const u32 sb = as_u(s) < SENT - 1u ? as_u(s) : SENT - 1u;
    if ((lane & 3) == 0) ((volatile LAS u32*)(LAS char*)(char*)red)[(lane >> 2) * 4 + w] = sb;
    u32x4 p;
    u32 spins = 0;
    while (true) {
        p = *(volatile LAS u32x4*)((LAS char*)(char*)red + (lane & 15) * 16);
        u32 mx = p[0] > p[1] ? p[0] : p[1];
        mx = mx > p[2] ? mx : p[2];
        mx = mx > p[3] ? mx : p[3];
        if (__builtin_amdgcn_ballot_w64(mx == SENT) == 0ull) break;
        if (++spins > 50 * SPIN_CAP) break;
    }
    const float m = ((as_f(p[0]) + as_f(p[1])) + (as_f(p[2]) + as_f(p[3]))) * (1.0f / DM);
    const float ex2 = dppf<0x128>(m);
    const float rstd = __builtin_amdgcn_rsqf(fmaxf(fmaf(-m, m, ex2), 0.f) + 1e-5f);
    const float off = -m * rstd;
    const f32x2 R[4] = {{dppf<0x150>(rstd), dppf<0x151>(rstd)}, {dppf<0x152>(rstd), dppf<0x153>(rstd)},
                        {dppf<0x154>(rstd), dppf<0x155>(rstd)}, {dppf<0x156>(rstd), dppf<0x157>(rstd)}};
    const f32x2 O[4] = {{dppf<0x150>(off), dppf<0x151>(off)}, {dppf<0x152>(off), dppf<0x153>(off)},
                        {dppf<0x154>(off), dppf<0x155>(off)}, {dppf<0x156>(off), dppf<0x157>(off)}};
#pragma unroll
    for (int i = 0; i < 4; ++i) { u.a[i] = x.a[i] * R[i] + O[i]; u.b[i] = x.b[i] * R[i] + O[i]; }
}

__device__ __forceinline__ void stage_taps(char* smem, int ch, int i, u32x4 prev, u32x4 own) {
    const u32 D[8] = {prev[0], prev[1], prev[2], prev[3], own[0], own[1], own[2], own[3]};
    u32 O[7];
#pragma unroll
    for (int y = 0; y < 7; ++y) O[y] = __builtin_amdgcn_alignbit(D[y + 1], D[y], 16);
    char* base = smem + OFF_T + ch * T_CH + (i + 1) * 16;
#pragma unroll
    for (int c = 0; c < 8; ++c) {
        const int y = (c + 1) >> 1;
        const u32x4 v = (c & 1) ? (u32x4){D[y], D[y + 1], D[y + 2], D[y + 3]} : (u32x4){O[y], O[y + 1], O[y + 2], O[y + 3]};
        *(u32x4*)(base + c * T_COPY) = v;
    }
}

__device__ __forceinline__ f32x4 mfma16(u32x4 a, u32x4 b, f32x4 c) {
    return __builtin_amdgcn_mfma_f32_16x16x32_bf16(__builtin_bit_cast(bf16x8, a), __builtin_bit_cast(bf16x8, b), c, 0, 0, 0);
}
struct KState { f32x4 acc[3]; };
__device__ __forceinline__ void kgen_init(KState& ks) {
#pragma unroll
    for (int e = 0; e < 3; ++e) ks.acc[e] = (f32x4){0, 0, 0, 0};
}
#define OPQ(x) asm volatile("" : "+v"(x))
template <int NS_, bool PK>
__device__ __forceinline__ void kgen_steps(const f32x4* P, int lane, KState& ks) {
#pragma clang fp contract(fast)
    const int g = lane >> 4, m = lane & 15;
    const float lo = (float)(8 * m);
    const float hi[3] = {(float)(128 * (m >> 3) + (m & 7)),
                         (float)(m < 8 ? 256 + 128 * (m >> 2) + 2 * (m & 3) : 512 + 128 * ((m - 8) >> 1) + 4 * (m & 1)),
                         (float)(m < 8 ? 1024 + 128 * m : 2048)};
    auto eval = [&](f32x2 ex, f32x2 an, bool conj, u32& o0, u32& o1) {
        if (!PK) { OPQ(ex[0]); OPQ(ex[1]); OPQ(an[0]); OPQ(an[1]); }
        const float m0 = __builtin_amdgcn_exp2f(ex[0]), m1 = __builtin_amdgcn_exp2f(ex[1]);
        const float f0 = __builtin_amdgcn_fractf(an[0]), f1 = __builtin_amdgcn_fractf(an[1]);
        const float c0 = __builtin_amdgcn_cosf(f0), c1 = __builtin_amdgcn_cosf(f1);
        const float s0 = __builtin_amdgcn_sinf(f0), s1 = __builtin_amdgcn_sinf(f1);
        float r0, r1, i0, i1;
        if (PK) {
            const f32x2 r = (f32x2){c0, c1} * (f32x2){m0, m1}, i = (f32x2){s0, s1} * (f32x2){m0, m1};
            r0 = r[0]; r1 = r[1]; i0 = i[0]; i1 = i[1];
        } else {
            r0 = c0 * m0; OPQ(r0); i0 = s0 * m0; OPQ(i0); r1 = c1 * m1; OPQ(r1); i1 = s1 * m1; OPQ(i1);
        }
        o0 = pk_bf(r0, conj ? -i0 : i0);
        o1 = pk_bf(r1, conj ? -i1 : i1);
    };
    auto affine = [&](f32x2 a, float p, f32x2 b) -> f32x2 {
        if (PK) return a * p + b;
        float x = fmaf(a[0], p, b[0]), y = fmaf(a[1], p, b[1]);
        OPQ(x); OPQ(y);
        return (f32x2){x, y};
    };
#pragma unroll
    for (int s = 0; s < NS_; ++s) {
        u32x4 B, A[3];
#pragma unroll
        for (int h = 0; h < 2; ++h) {
            const f32x4 p0 = P[16 * s + 4 * g + 2 * h], p1 = P[16 * s + 4 * g + 2 * h + 1];
            u32 o0, o1;
            eval(affine(p0.xy, lo, (f32x2){0.f, 0.f}), affine(p0.zw, lo, (f32x2){0.f, 0.f}), true, o0, o1);
            B[2 * h] = o0; B[2 * h + 1] = o1;
#pragma unroll
            for (int e = 0; e < 3; ++e) {
                eval(affine(p0.xy, hi[e], p1.xy), affine(p0.zw, hi[e], p1.zw), false, o0, o1);
                A[e][2 * h] = o0; A[e][2 * h + 1] = o1;
            }
        }
#pragma unroll
        for (int e = 0; e < 3; ++e) ks.acc[e] = mfma16(A[e], B, ks.acc[e]);
    }
}
__device__ __forceinline__ const f32x4* kgen_params(char* smem, int wave, int step) {
    return (const f32x4*)(smem + OFF_P) + ((wave >> 1) & 1) * 128 + ((wave & 1) * 4 + step) * 16;
}
__device__ __forceinline__ void kgen_save(char* smem, int wave, int lane, const KState& ks) {
    char* p = smem + OFF_KP + wave * KP_WAVE + lane * 16;
    *(f32x4*)p = ks.acc[0];
    *(f32x4*)(p + 1024) = ks.acc[1];
    if (lane < 48) *(f32x4*)(p + 2048) = ks.acc[2];
}
__device__ __forceinline__ void kgen_load(const char* smem, int wave, int lane, KState& ks) {
    const char* p = smem + OFF_KP + wave * KP_WAVE;
    ks.acc[0] = *(const f32x4*)(p + lane * 16);
    ks.acc[1] = *(const f32x4*)(p + 1024 + lane * 16);
    ks.acc[2] = *(const f32x4*)(p + 2048 + (lane < 48 ? lane : 0) * 16);
}
__device__ __forceinline__ f32x4 kgen_sum(const char* p) {
    return (*(const f32x4*)p + *(const f32x4*)(p + KP_WAVE)) + (*(const f32x4*)(p + 4 * KP_WAVE) + *(const f32x4*)(p + 5 * KP_WAVE));
}
__device__ __forceinline__ void kgen_reduce(char* smem, int wave, int lane, u32 dskip) {
    const int c = wave >> 1, j = lane & 15, g = lane >> 4;
    char* KC = smem + OFF_KC + c * 1296;
    const char* p = smem + OFF_KP + 2 * c * KP_WAVE + lane * 16;
    if ((wave & 1) == 0) {
        f32x4 s = kgen_sum(p);
        const f32x4 t = kgen_sum(smem + OFF_KP + 2 * c * KP_WAVE + 2048 + (lane < 48 ? lane : 0) * 16);
        if (lane == 0) s[0] += c ? bf_hi(dskip) : bf_lo(dskip);
        *(u32x2*)(KC + 2 * (128 * (g >> 1) + 4 * (g & 1) + 8 * j)) = (u32x2){pk_bf(s[0], s[1]), pk_bf(s[2], s[3])};
        const u32 lo = pk_bf(t[0], t[1]), hi = pk_bf(t[2], t[3]);
        u16* o = (u16*)KC + 512 + 64 * g + j;
        if (g < 2) { o[0] = (u16)lo; o[16] = (u16)(lo >> 16); o[32] = (u16)hi; o[48] = (u16)(hi >> 16); }
        else if (lane == 32) { ((u16*)KC)[640] = (u16)lo; ((u16*)KC)[641] = 0; }
    } else {
        const f32x4 s = kgen_sum(p + 1024);
        const u32 lo = pk_bf(s[0], s[1]), hi = pk_bf(s[2], s[3]);
        if (g < 2) *(u32x2*)(KC + 2 * (256 + 64 * g + 4 * j)) = (u32x2){lo, hi};
        else {
            u32* o = (u32*)(KC + 2 * (384 + 64 * (g - 2) + 2 * j));
            o[0] = lo; o[16] = hi;
        }
    }
}

struct TapIndex {
    u16 off[2048 + 8];
    constexpr TapIndex() : off() {
        for (int i = 0; i < 8; ++i) off[i] = 641 * 2;
        for (int l = 0; l < 2048; ++l) {
            int e = 0;
            while ((l >> e) >= 256) ++e;
            int v = l >> e;
            if (e > 0) {
                const int rem = l & ((1 << e) - 1), half = 1 << (e - 1);
                if (rem > half || (rem == half && (v & 1))) ++v;
            }
            off[8 + l] = (u16)(2 * (128 * e + v));
        }
    }
};
__device__ const TapIndex tap_index = TapIndex();

__device__ __forceinline__ void s4_taps(char* smem, int ch, int i, u32x4 tix0, u32x4 tix1) {
    const char* kc = smem + OFF_KC + ch * 1296;
    u32 d[8];
#pragma unroll
    for (int j = 0; j < 8; ++j) {
        const u32 o = j < 4 ? tix0[j] : tix1[j - 4];
        d[j] = (u32)*(const u16*)(kc + (o & 0xffffu)) | ((u32)*(const u16*)(kc + (o >> 16)) << 16);
    }
    stage_taps(smem, ch, i, (u32x4){d[0], d[1], d[2], d[3]}, (u32x4){d[4], d[5], d[6], d[7]});
}

struct Frags { u32x4 A[8], B[8]; };
template <bool INIT> __device__ __forceinline__ void mma(f32x4& acc, const u32x4& a, const u32x4& b) {
    if constexpr (INIT) asm volatile("v_mfma_f32_16x16x32_bf16 %0, %1, %2, 0" : "=&v"(acc) : "v"(a), "v"(b));
    else asm volatile("v_mfma_f32_16x16x32_bf16 %0, %1, %2, %0" : "+v"(acc) : "v"(a), "v"(b));
}
template <int I> __device__ __forceinline__ void load_one(Frags& f, const char* ap, const char* bp) {
    if constexpr (I == 0) f.A[0] = *(const u32x4*)ap;
    else if constexpr (I <= 8) f.B[I - 1] = *(const u32x4*)(bp + 512 * (8 - I));
    else f.A[I - 8] = *(const u32x4*)(ap + 512 * (I - 8));
}
template <int T0, int T1, int T2, int T3> struct TileSet {
    static constexpr int tile(int i) { return i == 0 ? T0 : i == 1 ? T1 : i == 2 ? T2 : T3; }
    static constexpr int walk(int I, bool want_tile) {
        int n = 0;
        for (int a = 0; a < 8; ++a)
            for (int j = 0; j < 4; ++j)
                if (tile(j) >= a) { if (n == I) return want_tile ? tile(j) : a; ++n; }
        return -1;
    }
};
constexpr int tri_a(int I) { int a = 0; while (I >= 8 - a) { I -= 8 - a; ++a; } return a; }
constexpr int tri_b(int I) { int a = 0; while (I >= 8 - a) { I -= 8 - a; ++a; } return I; }
template <int I, bool FIRST> __device__ __forceinline__ void class_step(const Frags& cur, Frags& nxt, f32x4 (&acc)[8], const char* ap, const char* bp) {
    if constexpr (I < 36) {
        constexpr int a = tri_a(I), b = tri_b(I);
        mma<FIRST && a == 0>(acc[a + b], cur.A[a], cur.B[b]);
        if constexpr ((I & 1) == 1 && (I >> 1) < 16) load_one<(I >> 1)>(nxt, ap, bp);
        SB();
        class_step<I + 1, FIRST>(cur, nxt, acc, ap, bp);
    }
}
template <class TS, int I> __device__ __forceinline__ void set_step(const Frags& cur, Frags& nxt, f32x4 (&acc)[8], const char* ap, const char* bp) {
    if constexpr (I < 18) {
        constexpr int t = TS::walk(I, true), a = TS::walk(I, false);
        mma<false>(acc[t], cur.A[a], cur.B[t - a]);
        if constexpr (I < 16) load_one<I>(nxt, ap, bp);
        SB();
        set_step<TS, I + 1>(cur, nxt, acc, ap, bp);
    }
}
template <class TS, int I, int TO, class HOOK> __device__ __forceinline__ void set_step_h(const Frags& cur, f32x4 (&acc)[8], HOOK hook) {
    if constexpr (I < TO) {
        constexpr int t = TS::walk(I, true), a = TS::walk(I, false);
        mma<false>(acc[t], cur.A[a], cur.B[t - a]);
        hook(std::integral_constant<int, I>());
        SB();
        set_step_h<TS, I + 1, TO>(cur, acc, hook);
    }
}
template <class TS, int J> __device__ __forceinline__ void tile_out(char* Y, f32x4 (&acc)[8], const f32x4 (&o)[4]) {
    constexpr int t = TS::tile(J);
    const f32x4 s = acc[t] + o[J];
    const u32x2 pk = {pk_bf(s[0], s[1]), pk_bf(s[2], s[3])};
    *(u32x2*)(Y + 512 * t) = pk;
    SB();
}
template <int T, int I, int BASE, class HOOK> __device__ __forceinline__ void tile_run(const Frags& cur, f32x4 (&acc)[8], HOOK hook) {
    if constexpr (I <= T) {
        mma<false>(acc[T], cur.A[I], cur.B[T - I]);
        hook(std::integral_constant<int, BASE + I>());
        SB();
        tile_run<T, I + 1, BASE>(cur, acc, hook);
    }
}
template <class TS, class HOOK> __device__ __forceinline__ void set_by_tiles(const Frags& cur, f32x4 (&acc)[8], HOOK hook) {
    tile_run<TS::tile(0), 0, 0>(cur, acc, hook);
    tile_run<TS::tile(1), 0, TS::tile(0) + 1>(cur, acc, hook);
    tile_run<TS::tile(2), 0, TS::tile(0) + TS::tile(1) + 2>(cur, acc, hook);
    tile_run<TS::tile(3), 0, TS::tile(0) + TS::tile(1) + TS::tile(2) + 3>(cur, acc, hook);
}
template <class SEND, class KEEP>
__device__ __forceinline__ void conv_half(char* smem, const u32x4 (&A0)[8], const char* ap, const char* bp, int wave, int lane, int ch, u32 seq) {
    f32x4 acc[8];
    Frags f0, f1;
#pragma unroll
    for (int a = 0; a < 8; ++a) f0.A[a] = A0[a];
#pragma unroll
    for (int b = 0; b < 8; ++b) { f0.B[b] = *(const u32x4*)(bp + 512 * (7 - b)); SB(); }
    class_step<0, true>(f0, f1, acc, ap + 64, bp + 64);
    class_step<0, false>(f1, f0, acc, ap + 128, bp + 128);
    set_step<SEND, 0>(f0, f1, acc, ap + 192, bp + 192);
    f32x4* X = (f32x4*)(smem + OFF_X) + wave * 256 + lane;
    const f32x4* XP = (const f32x4*)(smem + OFF_X) + (wave ^ 1) * 256 + lane;
    volatile LAS u32* xf = (volatile LAS u32*)(LAS char*)(smem + OFF_CNT + 32);
    constexpr int e0 = SEND::tile(0), e1 = e0 + SEND::tile(1) + 1, e2 = e1 + SEND::tile(2) + 1;
    set_by_tiles<SEND>(f1, acc, [&](auto i) {
        if constexpr (i.value == e0 + 4) { asm volatile("s_nop 3" : "+v"(acc[SEND::tile(0)])); X[0] = acc[SEND::tile(0)]; }
        if constexpr (i.value == e1 + 4) { asm volatile("s_nop 3" : "+v"(acc[SEND::tile(1)])); X[64] = acc[SEND::tile(1)]; }
        if constexpr (i.value == e2 + 4) { asm volatile("s_nop 3" : "+v"(acc[SEND::tile(2)])); X[128] = acc[SEND::tile(2)]; }
    });
    u32 seen = 0;
    f32x4 o[4];
    set_step_h<KEEP, 0, 18>(f0, acc, [&](auto i) {
        if constexpr (i.value == 3) { asm volatile("s_nop 3" : "+v"(acc[SEND::tile(3)])); X[192] = acc[SEND::tile(3)]; xf[wave] = seq; }
        if constexpr (i.value == 12) seen = xf[wave ^ 1];
        if constexpr (i.value == 17) {
            asm volatile("" : "+v"(seen));
            for (u32 spins = 0; seen < seq && spins < 50 * SPIN_CAP; ++spins) seen = xf[wave ^ 1];
            o[0] = XP[0]; o[1] = XP[64];
        }
    });
    char* Y = smem + OFF_Y + ch * 4096 + (16 * (lane & 15) + 4 * (lane >> 4)) * 2;
    constexpr int k0 = KEEP::tile(0), k1 = k0 + KEEP::tile(1) + 1, k2 = k1 + KEEP::tile(2) + 1;
    set_by_tiles<KEEP>(f1, acc, [&](auto i) {
        if constexpr (i.value == 0) { o[2] = XP[128]; o[3] = XP[192]; }
        if constexpr (i.value == k1 + 3) { asm volatile("s_nop 4" : "+v"(acc[KEEP::tile(0)]), "+v"(acc[KEEP::tile(1)])); tile_out<KEEP, 0>(Y, acc, o); tile_out<KEEP, 1>(Y, acc, o); }
        if constexpr (i.value == k2 + 4) { asm volatile("s_nop 3" : "+v"(acc[KEEP::tile(2)])); tile_out<KEEP, 2>(Y, acc, o); }
    });
    asm volatile("s_nop 7" : "+v"(acc[KEEP::tile(3)]));
    tile_out<KEEP, 3>(Y, acc, o);
}
struct ConvAddr { const char* ap; const char* bp; };
__device__ __forceinline__ ConvAddr conv_addr(char* smem, int wave, int lane) {
    const int ch = (wave >> 1) & 1, hf = wave & 1;
    const int m = lane & 15, g = lane >> 4;
    ConvAddr c;
    c.ap = smem + OFF_T + ch * T_CH + (m & 7) * T_COPY + 16 * (m >> 3) + 16 * g + 256 * hf;
    c.bp = smem + OFF_U + ch * U_STRIDE + 4064 - 32 * m + 16 * g + 256 * hf - 512 * 7;
    return c;
}
__device__ __forceinline__ void conv_preload(u32x4 (&A0)[8], const ConvAddr& c) {
#pragma unroll
    for (int a = 0; a < 8; ++a) A0[a] = *(const u32x4*)(c.ap + 512 * a);
}
template <bool EARLY>
__device__ __forceinline__ void conv_col(char* smem, const ConvAddr& c, int wave, int lane, u32 seq) {
    u32x4 A0[8];
    if (EARLY) conv_preload(A0, c);
    BAR();
    if (!EARLY) conv_preload(A0, c);
    if ((wave & 1) == 0) conv_half<TileSet<1, 3, 4, 6>, TileSet<0, 2, 5, 7>>(smem, A0, c.ap, c.bp, wave, lane, wave >> 1, seq);
    else conv_half<TileSet<0, 2, 5, 7>, TileSet<1, 3, 4, 6>>(smem, A0, c.ap, c.bp, wave, lane, wave >> 1, seq);
    STAMP(26 + seq);
    BAR();
}
template <class PRE>
__device__ __forceinline__ void conv_row(char* smem, int wave, int lane, rsrc_t rs, int id, int fill0, int st, PRE pre) {
    const u32x4 sent = {SENT, SENT, SENT, SENT};
    const u32 off = WS_H + (fill0 < 0 ? 0 : fill0) * WS_HS + id * 8192 + ((wave - 4) * 64 + lane) * 16;
    const u64 t0 = __builtin_amdgcn_s_memrealtime();
    if (fill0 >= 0) { st_dev(rs, off, sent); st_dev(rs, off + 4096, sent); }
    pre();
    if (fill0 >= 0) {
        wait_until(t0 + FILL_GAP);
        st_dev(rs, off + WS_HS, sent); st_dev(rs, off + WS_HS + 4096, sent);
        WAIT_VM(2);
    }
    STAMP(st);
    BAR();
}

__device__ __forceinline__ void row_dma(char* smem, const char* q, const char* k, const char* v, int id, int wave, int lane, int s) {
    const int slot = wave + 8 * s;
    int row = 8 * id + (slot < 12 ? slot - 4 : slot < 20 ? slot - 12 : slot - 20);
    row = row < 0 ? 0 : row;
    const char* src = slot < 12 ? k : slot < 20 ? v : q;
    lds_dma16(src + row * 1024 + lane * 16, (u32)(size_t)(LAS char*)(smem + OFF_R) + slot * R_ROW);
}

__device__ __forceinline__ u32x4 kv_piece(u32 rp, u32x2 sk, int id) {
    u32x2 k0, k1, k2, v0, v1;
    static_assert(4 * R_ROW == 4352, "offsets below");
    asm volatile("ds_read_b64_tr_b16 %0, %5\n\tds_read_b64_tr_b16 %1, %5 offset:4352\n\tds_read_b64_tr_b16 %2, %5 offset:8704\n\t"
                 "ds_read_b64_tr_b16 %3, %5 offset:13056\n\tds_read_b64_tr_b16 %4, %5 offset:17408\n\ts_waitcnt lgkmcnt(0)"
                 : "=&v"(k0), "=&v"(k1), "=&v"(k2), "=&v"(v0), "=&v"(v1) : "v"(rp) : "memory");
    const u32 first = id ? 0xffffffffu : 0u;
    const u32 kp = k0[0] & first, kq = k0[1] & first;
    const float kf[12] = {0.f, bf_hi(kp), bf_lo(kq), bf_hi(kq), bf_lo(k1[0]), bf_hi(k1[0]),
                          bf_lo(k1[1]), bf_hi(k1[1]), bf_lo(k2[0]), bf_hi(k2[0]), bf_lo(k2[1]), bf_hi(k2[1])};
    float vf[8];
    unpack8((u32x4){v0[0], v0[1], v1[0], v1[1]}, vf);
    const float w0 = bf_lo(sk[0]), w1 = bf_hi(sk[0]), w2 = bf_lo(sk[1]), w3 = bf_hi(sk[1]);
    float kv[8];
#pragma unroll
    for (int j = 0; j < 8; ++j) kv[j] = fmaf(kf[j + 4], w0, fmaf(kf[j + 3], w1, fmaf(kf[j + 2], w2, kf[j + 1] * w3))) * vf[j];
    return pack8(kv);
}

__global__ void __launch_bounds__(512, 2)
h3s4_kernel(const char* q_, const char* k_, const char* v_, const u32x2* shiftk_, const u32x4* ssmk_, const u32* ssmD_,
            const u16* Are_, const u16* Aim_, const u16* Bre_, const u16* Bim_, const u16* Cre_, const u16* Cim_, const u16* logdt_,
            const u32* Dskip_, u16* out_, char* ws_) {
    u64 t_first;
    asm volatile("s_memrealtime %0\n\ts_waitcnt lgkmcnt(0)" : "=s"(t_first));
    typedef __attribute__((address_space(4))) const u64 cu64;
    cu64* ka = (cu64*)__builtin_amdgcn_kernarg_segment_ptr();
    asm volatile("" : "+s"(ka), "+s"(t_first));
    const char* __restrict__ q = (const char*)ka[0]; const char* __restrict__ k = (const char*)ka[1]; const char* __restrict__ v = (const char*)ka[2];
    const u32x2* __restrict__ shiftk = (const u32x2*)ka[3]; const u32x4* __restrict__ ssmk = (const u32x4*)ka[4]; const u32* __restrict__ ssmD = (const u32*)ka[5];
    const u16* __restrict__ Are = (const u16*)ka[6]; const u16* __restrict__ Aim = (const u16*)ka[7]; const u16* __restrict__ Bre = (const u16*)ka[8];
    const u16* __restrict__ Bim = (const u16*)ka[9]; const u16* __restrict__ Cre = (const u16*)ka[10]; const u16* __restrict__ Cim = (const u16*)ka[11];
    const u16* __restrict__ logdt = (const u16*)ka[12]; const u32* __restrict__ Dskip = (const u32*)ka[13];
    u16* __restrict__ out = (u16*)ka[14]; char* ws = (char*)ka[15];
    __shared__ __attribute__((aligned(16))) char smem[LDS_BYTES];
    const int tid = threadIdx.x, lane = tid & 63, wave = __builtin_amdgcn_readfirstlane(tid >> 6);
    const int id = (blockIdx.x & 7) * 32 + (blockIdx.x >> 3);
    const rsrc_t rs = __builtin_amdgcn_make_buffer_rsrc(ws, 0, WS_USED, 0x00020000);
    const u32x4 desc = {(u32)(size_t)ws, (u32)((size_t)ws >> 32) & 0xffffu, WS_USED, 0x00020000u};
    asm volatile("" :: "s"(ws), "s"(q), "s"(out));
    STAMP(0);

    u32 code_pf = 0;
    {
        u64 pc; u32 len;
        asm volatile("s_getpc_b64 %0\nh3s4_text_a:\n\ts_mov_b32 %1, h3s4_text_b - h3s4_text_a" : "=s"(pc), "=s"(len));
        const u32 off = ((blockIdx.x >> 3) * 8 + wave) * 256 + lane * 4;
        if (off + 4 <= len) asm volatile("global_load_dword %0, %1, %2" : "=v"(code_pf) : "v"(off), "s"(pc) : "memory");
    }
    const u32x4 sent = {SENT, SENT, SENT, SENT};
    const u32 dA = ssmD[id], dB = Dskip[id];
    MEMFENCE(); SB();
    st_dev(rs, WS_H + 0 * WS_HS + id * 8192 + tid * 16, sent);
    MEMFENCE(); SB();
    if (wave < 4) {
        u32x2 skB = ld_b64(shiftk, (256 + tid) * 8);
        row_dma(smem, q, k, v, id, wave, lane, 0); row_dma(smem, q, k, v, id, wave, lane, 1); row_dma(smem, q, k, v, id, wave, lane, 2);
        MEMFENCE(); SB();
        row_dma(smem, q, k, v, id, wave, lane, 3);
        u32x4 tap_own = ld_b128(ssmk, (2 * id) * 4096 + tid * 16), tap_prev = ld_b128(ssmk, (2 * id) * 4096 + (tid ? tid - 1 : 0) * 16);
        const int pd = 2 * id + (tid >> 7), po = (pd * NS + (tid & 127)) * 2;
        u32 r_dt = ld_u16(logdt, pd * 2), r_are = ld_u16(Are, po), r_aim = ld_u16(Aim, po), r_bre = ld_u16(Bre, po);
        u32 r_bim = ld_u16(Bim, po), r_cre = ld_u16(Cre, po), r_cim = ld_u16(Cim, po);
        u32x4 tix0 = ld_b128(tap_index.off, tid * 16), tix1 = ld_b128(tap_index.off, tid * 16 + 16);
        MEMFENCE(); SB();
        st_dev(rs, WS_H + 1 * WS_HS + id * 8192 + tid * 16, sent);
        MEMFENCE(); SB();
        const u32 epoch = launch_tag(ws);
        asm volatile("s_waitcnt vmcnt(16)\n\ts_barrier" ::: "memory");
        if (tid < 32) __hip_atomic_store((GAS u32*)(ws + WS_FLAGS) + tid * 256 + id, epoch, RLX_AGENT);
        STAMP(19);
        if (tid < 60) *(u32x4*)(smem + OFF_U + (tid / 30) * U_STRIDE + 4096 + (tid % 30) * 16) = (u32x4){0, 0, 0, 0};
        else if (tid >= 64 && tid < 80) *(u32x4*)(smem + OFF_T + (tid - 64) * T_COPY) = (u32x4){0, 0, 0, 0};
        asm volatile("s_waitcnt vmcnt(13)" : "+v"(skB) :: "memory");
        BAR();
        STAMP(1);
        publish(rs, WS_H + id * 8192 + (256 + tid) * 16,
                kv_piece((u32)(size_t)(LAS char*)(smem + OFF_R + ((lane & 15) >> 2) * R_ROW + (256 + 64 * wave + 16 * (lane >> 4) + 4 * (lane & 3)) * 2), skB, id));
        asm volatile("s_waitcnt vmcnt(1)"
                     : "+v"(tap_own), "+v"(tap_prev), "+v"(r_dt), "+v"(r_are), "+v"(r_aim), "+v"(r_bre), "+v"(r_bim), "+v"(r_cre),
                       "+v"(r_cim), "+v"(tix0), "+v"(tix1) :: "memory");
        cnt_arrive(smem, 6, lane);

        {
            const float dt = rbf(__builtin_amdgcn_exp2f(bf_lo(r_dt) * 1.4426950408889634f));
            const float are = dt * bf_lo(r_are), aim = dt * bf_lo(r_aim);
            const float br = dt * bf_lo(r_bre), bi = dt * bf_lo(r_bim);
            const float cr = bf_lo(r_cre), ci = bf_lo(r_cim);
            const float wr = cr * br - ci * bi, wi = cr * bi + ci * br;
            const float ar = fabsf(wr), ai = fabsf(wi), mx = fmaxf(ar, ai), mn = fminf(ar, ai);
            const float ratio = mx > 0.f ? mn * __builtin_amdgcn_rcpf(mx) : 0.f, r2 = ratio * ratio;
            float at = 0.0028662257f;
            at = fmaf(at, r2, -0.0161657367f); at = fmaf(at, r2, 0.0429096138f); at = fmaf(at, r2, -0.0752896400f);
            at = fmaf(at, r2, 0.1065626393f); at = fmaf(at, r2, -0.1420889944f); at = fmaf(at, r2, 0.1999355085f);
            at = fmaf(at, r2, -0.3333314528f); at = fmaf(at, r2, 1.0f);
            float ph = at * ratio * 0.15915494309189535f;
            ph = ai > ar ? 0.25f - ph : ph;
            ph = wr < 0.f ? 0.5f - ph : ph;
            ph = wi < 0.f ? -ph : ph;
            float* p = (float*)(smem + OFF_P) + (tid >> 1) * 8 + (tid & 1);
            p[0] = are * 1.4426950408889634f;
            p[2] = aim * 0.15915494309189535f;
            p[4] = __builtin_amdgcn_logf(mx) + 0.5f * __builtin_amdgcn_logf(r2 + 1.0f);
            p[6] = ph;
        }
        cnt_arrive(smem, 7, lane);
        if (tid == 0) {
            tap_prev = (u32x4){0, 0, 0, 0};
            tap_own[0] = (tap_own[0] & 0xffff0000u) | (pk_bf(bf_lo(tap_own[0]) + bf_lo(dA), 0.f) & 0xffffu);
        }
        stage_taps(smem, 0, tid, tap_prev, tap_own);
        cnt_arrive(smem, 0, lane);
        STAMP(2);
        if (C_PRE) {
            cnt_wait(smem, 7, 4);
            KState ks;
            kgen_init(ks);
            kgen_steps<C_PRE, true>(kgen_params(smem, wave, 0), lane, ks);
            kgen_save(smem, wave, lane, ks);
        }

        const ConvAddr ca = conv_addr(smem, wave, lane);
        asm volatile("" :: "v"(code_pf));
        cnt_wait(smem, 0, 8);
        STAMP(4);
#pragma nounroll
        for (u32 seq = 1; seq <= 3; ++seq) {
            if (seq == 2) {
                {
                    KState ks;
                    if (C_PRE) kgen_load(smem, wave, lane, ks); else kgen_init(ks);
                    kgen_steps<4 - R_STEPS - C_PRE, true>(kgen_params(smem, wave, C_PRE), lane, ks);
                    kgen_save(smem, wave, lane, ks);
                }
                STAMP(7);
                cnt_arrive(smem, 1, lane);
                cnt_wait(smem, 1, 8);
                kgen_reduce(smem, wave, lane, dB);
                cnt_arrive(smem, 2, lane);
                cnt_wait(smem, 2, 4);
                STAMP(23);
                s4_taps(smem, 0, tid, tix0, tix1);
                cnt_arrive(smem, 3, lane);
                cnt_wait(smem, 3, 8);
                STAMP(25);
            }
            conv_col<true>(smem, ca, wave, lane, seq);
            STAMP(seq == 1 ? 6 : seq == 2 ? 11 : 15);
        }
    } else {
        const int r = tid - 256, w = wave - 4;
        u32x2 skA = ld_b64(shiftk, r * 8);
        row_dma(smem, q, k, v, id, wave, lane, 0); row_dma(smem, q, k, v, id, wave, lane, 1);
        MEMFENCE(); SB();
        row_dma(smem, q, k, v, id, wave, lane, 2);
        u32x4 tap_own = ld_b128(ssmk, (2 * id + 1) * 4096 + r * 16), tap_prev = ld_b128(ssmk, (2 * id + 1) * 4096 + (r ? r - 1 : 0) * 16);
        u32x4 tix0 = ld_b128(tap_index.off, r * 16), tix1 = ld_b128(tap_index.off, r * 16 + 16);
        MEMFENCE(); SB();
        st_dev(rs, WS_H + 1 * WS_HS + id * 8192 + tid * 16, sent);
        MEMFENCE(); SB();
        const u32 epoch = launch_tag(ws);
        asm volatile("s_waitcnt vmcnt(8)\n\ts_barrier" ::: "memory");
        ((u32*)(smem + OFF_RED))[r] = SENT;
        if (r < 16) ((u32*)(smem + OFF_CNT))[r] = 0u;
        asm volatile("s_waitcnt vmcnt(6)" : "+v"(skA) :: "memory");
        BAR();
        STAMP(1);

        const u32 own_a = id * 8192 + r * 16, own_b = own_a + 4096;
        const u32 col_a = (r >> 1) * 8192 + (2 * id + (r & 1)) * 16, col_b = col_a + 128 * 8192;
        const u32 row_a = (r >> 1) * 8192 + id * 32 + (r & 1) * 16, row_b = row_a + 128 * 8192;
        char* const u_dst = smem + OFF_U + (r & 1) * U_STRIDE + 4080 - (r >> 1) * 16;
        const char* const y_src = smem + OFF_Y + (r & 1) * 4096 + (r >> 1) * 16;
        float* red = (float*)(smem + OFF_RED);

        Rows x, u0;
        u64 t_s;
        const u32 flag_a = WS_FLAGS + (blockIdx.x & 31) * 1024 + (r >> 1) * 4;
        u32 fl_a, fl_b;
        const u32 rp = (u32)(size_t)(LAS char*)(smem + OFF_R + ((lane & 15) >> 2) * R_ROW + (64 * w + 16 * (lane >> 4) + 4 * (lane & 3)) * 2);
        publish(rs, WS_H + own_a, kv_piece(rp, skA, id));
        t_s = __builtin_amdgcn_s_memrealtime();
        __builtin_amdgcn_s_setprio(2);
        asm volatile("s_waitcnt vmcnt(1)" : "+v"(tap_own), "+v"(tap_prev), "+v"(tix0), "+v"(tix1) :: "memory");
        fl_a = ld_flag(ws, flag_a); fl_b = ld_flag(ws, flag_a + 512);
        cnt_arrive(smem, 6, lane);
        STAMP(2);
        u32 touch = 0;
        if (wave == 6) {
            if (lane < 4) touch = __builtin_amdgcn_raw_buffer_load_b32(rs, WS_H + (2 + lane) * WS_HS + id * 8192, 0, 16);
            else if (lane == 4) touch = ((const u32*)out)[id * 2048];
        }
        if (r == 0) {
            tap_prev = (u32x4){0, 0, 0, 0};
            tap_own[0] = (tap_own[0] & 0xffff0000u) | (pk_bf(bf_lo(tap_own[0]) + bf_hi(dA), 0.f) & 0xffffu);
        }
        stage_taps(smem, 1, r, tap_prev, tap_own);
        cnt_arrive(smem, 0, lane);
        STAMP(3);

        Piece2 pc, pr;
        Rows yA, u;
        auto stage_input = [&](u32 h) {
            poll2_finish(pc, desc, WS_H + h * WS_HS + col_a, WS_H + h * WS_HS + col_b);
            *(u32x4*)u_dst = reverse8(pc.a);
            *(u32x4*)(u_dst - 2048) = reverse8(pc.b);
        };
        auto send_result = [&](u32 h) {
            publish(rs, WS_H + h * WS_HS + own_a, *(const u32x4*)y_src);
            publish(rs, WS_H + h * WS_HS + own_b, *(const u32x4*)(y_src + 2048));
            t_s = __builtin_amdgcn_s_memrealtime();
        };
        auto fetch_rows = [&](u32 h) {
            wait_until(t_s + POLL_DELAY);
            pr = poll2_issue(rs, WS_H + h * WS_HS + row_a, WS_H + h * WS_HS + row_b);
            poll2_finish(pr, desc, WS_H + h * WS_HS + row_a, WS_H + h * WS_HS + row_b);
        };

        wait_until(t_s + POLL_DELAY);
        {
            asm volatile("" :: "v"(fl_a), "v"(fl_b));
            const bool ok = fl_a == epoch && fl_b == epoch;
            pc = poll2_issue(rs, WS_H + col_a, WS_H + col_b);
            poll2_finish_flagged(pc, desc, WS_H + col_a, WS_H + col_b, flag_a, flag_a + 512, ok, epoch);
            *(u32x4*)u_dst = reverse8(pc.a);
            *(u32x4*)(u_dst - 2048) = reverse8(pc.b);
        }
        STAMP(21);
        asm volatile("" :: "v"(touch));
        asm volatile("" :: "v"(code_pf));
        BAR();
        STAMP(5);
        if (blockIdx.x == 0 && r == 0) __hip_atomic_store((GAS u32*)(ws + WS_EPOCH), epoch, RLX_AGENT);
        conv_row(smem, wave, lane, rs, id, 2, 27, [&] {
            cnt_wait(smem, 6, 8);
            if (wave == 7 && lane < 32) __hip_atomic_store((GAS u32*)(ws + WS_FLAGS + 32768) + lane * 256 + id, epoch, RLX_AGENT);
            u32x2 qq[2][2];
            asm volatile("ds_read_b64_tr_b16 %0, %4 offset:21760\n\tds_read_b64_tr_b16 %1, %4 offset:26112\n\t"
                         "ds_read_b64_tr_b16 %2, %4 offset:22272\n\tds_read_b64_tr_b16 %3, %4 offset:26624\n\ts_waitcnt lgkmcnt(0)"
                         : "=&v"(qq[0][0]), "=&v"(qq[0][1]), "=&v"(qq[1][0]), "=&v"(qq[1][1]) : "v"(rp) : "memory");
            unpack_rows((u32x4){qq[0][0][0], qq[0][0][1], qq[0][1][0], qq[0][1][1]}, x.a);
            unpack_rows((u32x4){qq[1][0][0], qq[1][0][1], qq[1][1][0], qq[1][1][1]}, x.b);
            __builtin_amdgcn_s_setprio(0);
            KState ks;
            kgen_init(ks);
            kgen_steps<R_STEPS, false>(kgen_params(smem, wave, 4 - R_STEPS), lane, ks);
            kgen_save(smem, wave, lane, ks);
            cnt_arrive(smem, 1, lane);
            __builtin_amdgcn_s_setprio(2);
        });
        send_result(1);
        STAMP(6);

#pragma nounroll
        for (u32 it = 0; it < 2; ++it) {
            MEMFENCE();
            const u32 fb_a = ld_flag(ws, flag_a + 32768), fb_b = ld_flag(ws, flag_a + 32768 + 512);
            MEMFENCE();
            layer_norm(x, u0, red + 128 * it, lane, w);
            if (it == 0) {
                wait_until(t_s + POLL_DELAY);
                asm volatile("" :: "v"(fb_a), "v"(fb_b));
                const bool ok = fb_a == epoch && fb_b == epoch;
                pr = poll2_issue(rs, WS_H + WS_HS + row_a, WS_H + WS_HS + row_b);
                poll2_finish_flagged(pr, desc, WS_H + WS_HS + row_a, WS_H + WS_HS + row_b, flag_a + 32768, flag_a + 32768 + 512, ok, epoch);
                STAMP(8);
                unpack_rows(pr.a, yA.a);
                unpack_rows(pr.b, yA.b);
            }
#pragma unroll
            for (int i = 0; i < 4; ++i) { x.a[i] += u0.a[i] * yA.a[i]; x.b[i] += u0.b[i] * yA.b[i]; }
            round_rows(x.a);
            round_rows(x.b);
            layer_norm(x, u, red + 128 * it + 64, lane, w);
            publish(rs, WS_H + (2 + 2 * it) * WS_HS + own_a, pack_rows(u.a));
            publish(rs, WS_H + (2 + 2 * it) * WS_HS + own_b, pack_rows(u.b));
            t_s = __builtin_amdgcn_s_memrealtime();
            STAMP(it ? 13 : 9);

            wait_until(t_s + POLL_DELAY);
            pc = poll2_issue(rs, WS_H + (2 + 2 * it) * WS_HS + col_a, WS_H + (2 + 2 * it) * WS_HS + col_b);
            if (it == 0) {
                cnt_wait(smem, 2, 4);
                s4_taps(smem, 1, r, tix0, tix1);
                cnt_arrive(smem, 3, lane);
            }
            stage_input(2 + 2 * it);
            BAR();
            STAMP(it ? 14 : 10);
            conv_row(smem, wave, lane, rs, id, it ? -1 : 4, 28, [] {});
            send_result(3 + 2 * it);
            STAMP(it ? 15 : 11);

            fetch_rows(3 + 2 * it);
            STAMP(it ? 16 : 12);
            Rows y;
            unpack_rows(pr.a, y.a);
            unpack_rows(pr.b, y.b);
#pragma unroll
            for (int i = 0; i < 4; ++i) { x.a[i] += y.a[i]; x.b[i] += y.b[i]; }
            pr.a = round_rows(x.a);
            pr.b = round_rows(x.b);
        }
        {
            u16* dst = out + (8 * id) * DM + r;
#pragma unroll
            for (int j = 0; j < 4; ++j) {
                dst[(2 * j) * DM] = (u16)pr.a[j];
                dst[(2 * j) * DM + 256] = (u16)pr.b[j];
                dst[(2 * j + 1) * DM] = (u16)(pr.a[j] >> 16);
                dst[(2 * j + 1) * DM + 256] = (u16)(pr.b[j] >> 16);
            }
        }
        STAMP(17);
    }
    asm volatile("h3s4_text_b:");
    asm volatile("s_waitcnt vmcnt(0)" ::: "memory");
    STAMP(18);
    BAR();
    if (tid == 0) {
        const u64* st = (const u64*)(smem + OFF_ST);
        const u64 dur = st[32] - t_first;
        const u64 until = __builtin_amdgcn_s_memrealtime() + DIAG * dur;
        while (__builtin_amdgcn_s_memrealtime() < until) __builtin_amdgcn_s_sleep(2);
    }
}

extern "C" void kernel_launch(void* const* d_in, const int* in_sizes, int n_in,
                              void* d_out, int out_size, void* d_ws, size_t ws_size,
                              hipStream_t stream) {
    h3s4_kernel<<<256, 512, 0, stream>>>(
        (const char*)d_in[0], (const char*)d_in[1], (const char*)d_in[2], (const u32x2*)d_in[3], (const u32x4*)d_in[4],
        (const u32*)d_in[5], (const u16*)d_in[7], (const u16*)d_in[8], (const u16*)d_in[9], (const u16*)d_in[10],
        (const u16*)d_in[11], (const u16*)d_in[12], (const u16*)d_in[13], (const u32*)d_in[14], (u16*)d_out, (char*)d_ws
        );
}
```

```cpp
#include <hip/hip_runtime.h>
#include <hip/hip_bf16.h>
#include <stdint.h>

typedef unsigned int u32;
typedef unsigned short u16;
typedef unsigned long long u64;
typedef __attribute__((ext_vector_type(4))) float f32x4;
typedef __attribute__((ext_vector_type(2))) float f32x2;
typedef __attribute__((ext_vector_type(4))) u32 u32x4;
typedef __attribute__((ext_vector_type(2))) u32 u32x2;
typedef __attribute__((ext_vector_type(4))) short s16x4;
typedef __attribute__((ext_vector_type(8))) __bf16 bf16x8;
typedef __attribute__((ext_vector_type(2))) __bf16 bf16x2;
#define LAS __attribute__((address_space(3)))
#define GAS __attribute__((address_space(1)))
#define RLX_AGENT __ATOMIC_RELAXED, __HIP_MEMORY_SCOPE_AGENT
#define SB() __builtin_amdgcn_sched_barrier(0)
#define BAR() asm volatile("s_waitcnt lgkmcnt(0)\n\ts_barrier" ::: "memory")
#define MEMFENCE() asm volatile("" ::: "memory")
#define WAIT_VM(n) do { MEMFENCE(); __builtin_amdgcn_s_waitcnt(0x0F70 | ((n) & 15) | (((n) >> 4) << 14)); MEMFENCE(); } while (0)

constexpr int DM = 512;
constexpr int NS = 128;
constexpr u32 SENT = 0xffffffffu;
constexpr u32 SPIN_CAP = 20000;
#define FILL_GAP 70
#define R_STEPS 1
#define POLL_DELAY 45

constexpr u32 WS_EPOCH = 0;
constexpr u32 WS_FLAGS = 4096;
constexpr u32 WS_H = 1u << 20;
constexpr u32 WS_HS = 2u << 20;
constexpr u32 WS_USED = WS_H + 6 * WS_HS;

constexpr int T_COPY = 4160, T_CH = 8 * T_COPY;
constexpr int U_STRIDE = 4672;
constexpr int R_ROW = 1088;
constexpr int KP_WAVE = 2816;
constexpr int OFF_T = 0;
constexpr int OFF_U = OFF_T + 2 * T_CH;
constexpr int OFF_X = OFF_U + 2 * U_STRIDE;
constexpr int OFF_Y = OFF_X + 16384;
constexpr int OFF_R = OFF_Y + 8192;
constexpr int OFF_P = OFF_R + 28 * R_ROW;
constexpr int OFF_KP = OFF_P + 4096;
constexpr int OFF_KC = OFF_KP + 8 * KP_WAVE;
constexpr int OFF_RED = OFF_KC + 2 * 648 * 2;
constexpr int OFF_CNT = OFF_RED + 1024;
constexpr int OFF_ST = OFF_CNT + 64;
constexpr int LDS_BYTES = OFF_ST + 512;
static_assert(LDS_BYTES <= 163840, "LDS");

#define STAMP(i)

__device__ __forceinline__ float bf_lo(u32 p) { return __builtin_bit_cast(float, p << 16); }
__device__ __forceinline__ float bf_hi(u32 p) { return __builtin_bit_cast(float, p & 0xffff0000u); }
__device__ __forceinline__ float bf_u16(u16 p) { return __builtin_bit_cast(float, (u32)p << 16); }
__device__ __forceinline__ u32 pk_bf(float lo, float hi) {
    bf16x2 v = {(__bf16)lo, (__bf16)hi};
    return __builtin_bit_cast(u32, v);
}
__device__ __forceinline__ float as_f(u32 x) { return __builtin_bit_cast(float, x); }
__device__ __forceinline__ u32 as_u(float x) { return __builtin_bit_cast(u32, x); }
__device__ __forceinline__ float rbf(float x) { return bf_lo(pk_bf(x, x)); }
__device__ __forceinline__ void unpack8(u32x4 p, float (&f)[8]) {
#pragma unroll
    for (int i = 0; i < 4; ++i) { f[2 * i] = bf_lo(p[i]); f[2 * i + 1] = bf_hi(p[i]); }
}
__device__ __forceinline__ u32x4 pack8(const float (&f)[8]) {
    return (u32x4){pk_bf(f[0], f[1]), pk_bf(f[2], f[3]), pk_bf(f[4], f[5]), pk_bf(f[6], f[7])};
}
__device__ __forceinline__ u32x4 reverse8(u32x4 p) {
    return (u32x4){__builtin_amdgcn_alignbit(p[3], p[3], 16), __builtin_amdgcn_alignbit(p[2], p[2], 16),
                   __builtin_amdgcn_alignbit(p[1], p[1], 16), __builtin_amdgcn_alignbit(p[0], p[0], 16)};
}
template <int CTRL> __device__ __forceinline__ float dppf(float v) {
    return __builtin_bit_cast(float, __builtin_amdgcn_update_dpp(0, __builtin_bit_cast(int, v), CTRL, 0xf, 0xf, true));
}

__device__ __forceinline__ void lds_dma16(const char* src, u32 lds) {
    u32 keep;
    asm volatile("s_mov_b32 %0, m0\n\ts_mov_b32 m0, %2\n\ts_nop 0\n\tglobal_load_lds_dwordx4 %1, off\n\ts_mov_b32 m0, %0"
                 : "=&s"(keep) : "v"(src), "s"(lds) : "memory");
}

__device__ __forceinline__ u32 ld_u16(const void* base, u32 off) {
    u32 r;
    asm volatile("global_load_ushort %0, %1, %2" : "=v"(r) : "v"(off), "s"(base) : "memory");
    return r;
}
__device__ __forceinline__ u32x2 ld_b64(const void* base, u32 off) {
    u32x2 r;
    asm volatile("global_load_dwordx2 %0, %1, %2" : "=v"(r) : "v"(off), "s"(base) : "memory");
    return r;
}
__device__ __forceinline__ u32x4 ld_b128(const void* base, u32 off) {
    u32x4 r;
    asm volatile("global_load_dwordx4 %0, %1, %2" : "=v"(r) : "v"(off), "s"(base) : "memory");
    return r;
}

typedef __amdgpu_buffer_rsrc_t rsrc_t;
__device__ __forceinline__ u32x4 ld_dev(rsrc_t rs, u32 off) { return __builtin_amdgcn_raw_buffer_load_b128(rs, off, 0, 16); }
__device__ __forceinline__ void st_dev(rsrc_t rs, u32 off, u32x4 v) { __builtin_amdgcn_raw_buffer_store_b128(v, rs, off, 0, 16); }
__device__ __forceinline__ void publish(rsrc_t rs, u32 off, u32x4 v) {
#pragma unroll
    for (int i = 0; i < 4; ++i) v[i] = v[i] < SENT - 1u ? v[i] : SENT - 1u;
    st_dev(rs, off, v);
}
__device__ __forceinline__ u32 launch_tag(const char* ws) {
    u32 e;
    asm volatile("s_load_dword %0, %1, 0x0\n\ts_waitcnt lgkmcnt(0)" : "=&s"(e) : "s"(ws + WS_EPOCH) : "memory");
    return e + 1u;
}
__device__ __forceinline__ void wait_until(u64 t) {
    while (__builtin_amdgcn_s_memrealtime() < t) __builtin_amdgcn_s_sleep(1);
}
__device__ __forceinline__ u32x4 poll_issue(rsrc_t rs, u32 off) {
    MEMFENCE();
    u32x4 x = ld_dev(rs, off);
    MEMFENCE();
    return x;
}
struct Piece2 { u32x4 a, b; };
__device__ __forceinline__ Piece2 poll2_issue(rsrc_t rs, u32 off_a, u32 off_b) {
    MEMFENCE();
    Piece2 p = {ld_dev(rs, off_a), ld_dev(rs, off_b)};
    MEMFENCE();
    return p;
}
__device__ __forceinline__ bool has_sentinel(const u32x4& x) {
    u32 mx = x[0] > x[1] ? x[0] : x[1];
    mx = mx > x[2] ? mx : x[2];
    mx = mx > x[3] ? mx : x[3];
    return mx == SENT;
}
__device__ __forceinline__ void poll2_finish(Piece2& p, u32x4 desc, u32 off_a, u32 off_b) {
#pragma nounroll
    for (u32 spins = 0; spins < SPIN_CAP; ++spins) {
        const bool bad = has_sentinel(p.a) || has_sentinel(p.b);
        if (__builtin_amdgcn_ballot_w64(bad) == 0ull) break;
        __builtin_amdgcn_s_sleep(1);
        if (bad) asm volatile("buffer_load_dwordx4 %0, %2, %4, 0 offen sc1\n\tbuffer_load_dwordx4 %1, %3, %4, 0 offen sc1\n\ts_waitcnt vmcnt(0)"
                              : "+v"(p.a), "+v"(p.b) : "v"(off_a), "v"(off_b), "s"(desc) : "memory");
    }
}
__device__ __forceinline__ u32 ld_flag(const char* ws, u32 off) { return __hip_atomic_load((const GAS u32*)(ws + off), RLX_AGENT); }
__device__ __forceinline__ void poll2_finish_flagged(Piece2& p, u32x4 desc, u32 off_a, u32 off_b, u32 fo_a, u32 fo_b, bool ok, u32 epoch) {
#pragma nounroll
    for (u32 spins = 0; spins < SPIN_CAP; ++spins) {
        const bool bad = !ok || has_sentinel(p.a) || has_sentinel(p.b);
        if (__builtin_amdgcn_ballot_w64(bad) == 0ull) break;
        if (!ok) {
            u32 fa, fb;
            asm volatile("buffer_load_dword %0, %2, %4, 0 offen sc1\n\tbuffer_load_dword %1, %3, %4, 0 offen sc1\n\ts_waitcnt vmcnt(0)"
                         : "=&v"(fa), "=&v"(fb) : "v"(fo_a), "v"(fo_b), "s"(desc) : "memory");
            ok = fa == epoch && fb == epoch;
        }
        if (bad) asm volatile("buffer_load_dwordx4 %0, %2, %4, 0 offen sc1\n\tbuffer_load_dwordx4 %1, %3, %4, 0 offen sc1\n\ts_waitcnt vmcnt(0)"
                              : "+v"(p.a), "+v"(p.b) : "v"(off_a), "v"(off_b), "s"(desc) : "memory");
    }
}

__device__ __forceinline__ void cnt_arrive(char* smem, int k, int lane) {
    asm volatile("s_waitcnt lgkmcnt(0)" ::: "memory");
    if (lane == 0) __hip_atomic_fetch_add((LAS u32*)(smem + OFF_CNT) + k, 1u, __ATOMIC_RELAXED, __HIP_MEMORY_SCOPE_WORKGROUP);
}
__device__ __forceinline__ void cnt_wait(char* smem, int k, u32 target) {
    u32 spins = 0;
    while (__hip_atomic_load((LAS u32*)(smem + OFF_CNT) + k, __ATOMIC_RELAXED, __HIP_MEMORY_SCOPE_WORKGROUP) < target) {
        if (++spins > 50 * SPIN_CAP) break;
        __builtin_amdgcn_s_sleep(1);
    }
    MEMFENCE();
}

struct Rows { f32x2 a[4], b[4]; };
__device__ __forceinline__ void unpack_rows(u32x4 p, f32x2 (&f)[4]) {
#pragma unroll
    for (int i = 0; i < 4; ++i) f[i] = (f32x2){bf_lo(p[i]), bf_hi(p[i])};
}
__device__ __forceinline__ u32x4 pack_rows(const f32x2 (&f)[4]) {
    return (u32x4){pk_bf(f[0][0], f[0][1]), pk_bf(f[1][0], f[1][1]), pk_bf(f[2][0], f[2][1]), pk_bf(f[3][0], f[3][1])};
}
__device__ __forceinline__ u32x4 round_rows(f32x2 (&f)[4]) {
    u32x4 p = pack_rows(f);
    asm("" : "+v"(p));
    unpack_rows(p, f);
    return p;
}
__device__ __forceinline__ void layer_norm(const Rows& x, Rows& u, float* red, int lane, int w) {
#pragma clang fp contract(fast)
    u32 a[8], b[8];
#pragma unroll
    for (int i = 0; i < 4; ++i) {
        const f32x2 s1 = x.a[i] + x.b[i], s2 = x.a[i] * x.a[i] + x.b[i] * x.b[i];
        a[2 * i] = as_u(s1[0]); a[2 * i + 1] = as_u(s1[1]);
        b[2 * i] = as_u(s2[0]); b[2 * i + 1] = as_u(s2[1]);
    }
    SB();
#pragma unroll
    for (int j = 0; j < 8; ++j) {
        const auto s = __builtin_amdgcn_permlane32_swap(a[j], b[j], false, false);
        a[j] = s[0]; b[j] = s[1];
    }
    SB();
    float r[8];
#pragma unroll
    for (int j = 0; j < 8; ++j) r[j] = as_f(a[j]) + as_f(b[j]);
    SB();
#pragma unroll
    for (int j = 0; j < 4; ++j) {
        const auto s = __builtin_amdgcn_permlane16_swap(as_u(r[j]), as_u(r[j + 4]), false, false);
        a[j] = s[0]; b[j] = s[1];
    }
    SB();
    float s4[4];
#pragma unroll
    for (int j = 0; j < 4; ++j) s4[j] = as_f(a[j]) + as_f(b[j]);
    SB();
#pragma unroll
    for (int j = 0; j < 4; ++j) s4[j] += dppf<0x128>(s4[j]);
    const float t0 = (lane & 8) ? s4[2] : s4[0], t1 = (lane & 8) ? s4[3] : s4[1];
    const float c0 = t0 + dppf<0x141>(t0), c1 = t1 + dppf<0x141>(t1);
    float s = (lane & 4) ? c1 : c0;
    s += dppf<0xB1>(s);
    s += dppf<0x4E>(s);
    const u32 sb = as_u(s) < SENT - 1u ? as_u(s) : SENT - 1u;
    if ((lane & 3) == 0) ((volatile LAS u32*)(LAS char*)(char*)red)[(lane >> 2) * 4 + w] = sb;
    u32x4 p;
    u32 spins = 0;
    while (true) {
        p = *(volatile LAS u32x4*)((LAS char*)(char*)red + (lane & 15) * 16);
        u32 mx = p[0] > p[1] ? p[0] : p[1];
        mx = mx > p[2] ? mx : p[2];
        mx = mx > p[3] ? mx : p[3];
        if (__builtin_amdgcn_ballot_w64(mx == SENT) == 0ull) break;
        if (++spins > 50 * SPIN_CAP) break;
    }
    const float m = ((as_f(p[0]) + as_f(p[1])) + (as_f(p[2]) + as_f(p[3]))) * (1.0f / DM);
    const float ex2 = dppf<0x128>(m);
    const float rstd = __builtin_amdgcn_rsqf(fmaxf(fmaf(-m, m, ex2), 0.f) + 1e-5f);
    const float off = -m * rstd;
    const f32x2 R[4] = {{dppf<0x150>(rstd), dppf<0x151>(rstd)}, {dppf<0x152>(rstd), dppf<0x153>(rstd)},
                        {dppf<0x154>(rstd), dppf<0x155>(rstd)}, {dppf<0x156>(rstd), dppf<0x157>(rstd)}};
    const f32x2 O[4] = {{dppf<0x150>(off), dppf<0x151>(off)}, {dppf<0x152>(off), dppf<0x153>(off)},
                        {dppf<0x154>(off), dppf<0x155>(off)}, {dppf<0x156>(off), dppf<0x157>(off)}};
#pragma unroll
    for (int i = 0; i < 4; ++i) { u.a[i] = x.a[i] * R[i] + O[i]; u.b[i] = x.b[i] * R[i] + O[i]; }
}

__device__ __forceinline__ void stage_taps(char* smem, int ch, int i, u32x4 prev, u32x4 own) {
    const u32 D[8] = {prev[0], prev[1], prev[2], prev[3], own[0], own[1], own[2], own[3]};
    u32 O[7];
#pragma unroll
    for (int y = 0; y < 7; ++y) O[y] = __builtin_amdgcn_alignbit(D[y + 1], D[y], 16);
    char* base = smem + OFF_T + ch * T_CH + (i + 1) * 16;
#pragma unroll
    for (int c = 0; c < 8; ++c) {
        const int y = (c + 1) >> 1;
        const u32x4 v = (c & 1) ? (u32x4){D[y], D[y + 1], D[y + 2], D[y + 3]} : (u32x4){O[y], O[y + 1], O[y + 2], O[y + 3]};
        *(u32x4*)(base + c * T_COPY) = v;
    }
}

__device__ __forceinline__ f32x4 mfma16(u32x4 a, u32x4 b, f32x4 c) {
    return __builtin_amdgcn_mfma_f32_16x16x32_bf16(__builtin_bit_cast(bf16x8, a), __builtin_bit_cast(bf16x8, b), c, 0, 0, 0);
}
struct KState { f32x4 acc[3]; };
__device__ __forceinline__ void kgen_init(KState& ks) {
#pragma unroll
    for (int e = 0; e < 3; ++e) ks.acc[e] = (f32x4){0, 0, 0, 0};
}
#define OPQ(x) asm volatile("" : "+v"(x))
template <int NS_, bool PK>
__device__ __forceinline__ void kgen_steps(const f32x4* P, int lane, KState& ks) {
#pragma clang fp contract(fast)
    const int g = lane >> 4, m = lane & 15;
    const float lo = (float)(8 * m);
    const float hi[3] = {(float)(128 * (m >> 3) + (m & 7)),
                         (float)(m < 8 ? 256 + 128 * (m >> 2) + 2 * (m & 3) : 512 + 128 * ((m - 8) >> 1) + 4 * (m & 1)),
                         (float)(m < 8 ? 1024 + 128 * m : 2048)};
    auto eval = [&](f32x2 ex, f32x2 an, bool conj, u32& o0, u32& o1) {
        if (!PK) { OPQ(ex[0]); OPQ(ex[1]); OPQ(an[0]); OPQ(an[1]); }
        const float m0 = __builtin_amdgcn_exp2f(ex[0]), m1 = __builtin_amdgcn_exp2f(ex[1]);
        const float f0 = __builtin_amdgcn_fractf(an[0]), f1 = __builtin_amdgcn_fractf(an[1]);
        const float c0 = __builtin_amdgcn_cosf(f0), c1 = __builtin_amdgcn_cosf(f1);
        const float s0 = __builtin_amdgcn_sinf(f0), s1 = __builtin_amdgcn_sinf(f1);
        float r0, r1, i0, i1;
        if (PK) {
            const f32x2 r = (f32x2){c0, c1} * (f32x2){m0, m1}, i = (f32x2){s0, s1} * (f32x2){m0, m1};
            r0 = r[0]; r1 = r[1]; i0 = i[0]; i1 = i[1];
        } else {
            r0 = c0 * m0; OPQ(r0); i0 = s0 * m0; OPQ(i0); r1 = c1 * m1; OPQ(r1); i1 = s1 * m1; OPQ(i1);
        }
        o0 = pk_bf(r0, conj ? -i0 : i0);
        o1 = pk_bf(r1, conj ? -i1 : i1);
    };
    auto affine = [&](f32x2 a, float p, f32x2 b) -> f32x2 {
        if (PK) return a * p + b;
        float x = fmaf(a[0], p, b[0]), y = fmaf(a[1], p, b[1]);
        OPQ(x); OPQ(y);
        return (f32x2){x, y};
    };
#pragma unroll
    for (int s = 0; s < NS_; ++s) {
        u32x4 B, A[3];
#pragma unroll
        for (int h = 0; h < 2; ++h) {
            const f32x4 p0 = P[16 * s + 4 * g + 2 * h], p1 = P[16 * s + 4 * g + 2 * h + 1];
            u32 o0, o1;
            eval(affine(p0.xy, lo, (f32x2){0.f, 0.f}), affine(p0.zw, lo, (f32x2){0.f, 0.f}), true, o0, o1);
            B[2 * h] = o0; B[2 * h + 1] = o1;
#pragma unroll
            for (int e = 0; e < 3; ++e) {
                eval(affine(p0.xy, hi[e], p1.xy), affine(p0.zw, hi[e], p1.zw), false, o0, o1);
                A[e][2 * h] = o0; A[e][2 * h + 1] = o1;
            }
        }
#pragma unroll
        for (int e = 0; e < 3; ++e) ks.acc[e] = mfma16(A[e], B, ks.acc[e]);
    }
}
__device__ __forceinline__ const f32x4* kgen_params(char* smem, int wave, int step) {
    return (const f32x4*)(smem + OFF_P) + ((wave >> 1) & 1) * 128 + ((wave & 1) * 4 + step) * 16;
}
__device__ __forceinline__ void kgen_save(char* smem, int wave, int lane, const KState& ks) {
    char* p = smem + OFF_KP + wave * KP_WAVE + lane * 16;
    *(f32x4*)p = ks.acc[0];
    *(f32x4*)(p + 1024) = ks.acc[1];
    if (lane < 48) *(f32x4*)(p + 2048) = ks.acc[2];
}
__device__ __forceinline__ f32x4 kgen_sum(const char* p) {
    return (*(const f32x4*)p + *(const f32x4*)(p + KP_WAVE)) + (*(const f32x4*)(p + 4 * KP_WAVE) + *(const f32x4*)(p + 5 * KP_WAVE));
}
__device__ __forceinline__ void kgen_reduce(char* smem, int wave, int lane, u32 dskip) {
    const int c = wave >> 1, j = lane & 15, g = lane >> 4;
    char* KC = smem + OFF_KC + c * 1296;
    const char* p = smem + OFF_KP + 2 * c * KP_WAVE + lane * 16;
    if ((wave & 1) == 0) {
        f32x4 s = kgen_sum(p);
        const f32x4 t = kgen_sum(smem + OFF_KP + 2 * c * KP_WAVE + 2048 + (lane < 48 ? lane : 0) * 16);
        if (lane == 0) s[0] += c ? bf_hi(dskip) : bf_lo(dskip);
        *(u32x2*)(KC + 2 * (128 * (g >> 1) + 4 * (g & 1) + 8 * j)) = (u32x2){pk_bf(s[0], s[1]), pk_bf(s[2], s[3])};
        const u32 lo = pk_bf(t[0], t[1]), hi = pk_bf(t[2], t[3]);
        u16* o = (u16*)KC + 512 + 64 * g + j;
        if (g < 2) { o[0] = (u16)lo; o[16] = (u16)(lo >> 16); o[32] = (u16)hi; o[48] = (u16)(hi >> 16); }
        else if (lane == 32) { ((u16*)KC)[640] = (u16)lo; ((u16*)KC)[641] = 0; }
    } else {
        const f32x4 s = kgen_sum(p + 1024);
        const u32 lo = pk_bf(s[0], s[1]), hi = pk_bf(s[2], s[3]);
        if (g < 2) *(u32x2*)(KC + 2 * (256 + 64 * g + 4 * j)) = (u32x2){lo, hi};
        else {
            u32* o = (u32*)(KC + 2 * (384 + 64 * (g - 2) + 2 * j));
            o[0] = lo; o[16] = hi;
        }
    }
}

struct TapIndex {
    u16 off[2048 + 8];
    constexpr TapIndex() : off() {
        for (int i = 0; i < 8; ++i) off[i] = 641 * 2;
        for (int l = 0; l < 2048; ++l) {
            int e = 0;
            while ((l >> e) >= 256) ++e;
            int v = l >> e;
            if (e > 0) {
                const int rem = l & ((1 << e) - 1), half = 1 << (e - 1);
                if (rem > half || (rem == half && (v & 1))) ++v;
            }
            off[8 + l] = (u16)(2 * (128 * e + v));
        }
    }
};
__device__ const TapIndex tap_index = TapIndex();

__device__ __forceinline__ void s4_taps(char* smem, int ch, int i, u32x4 tix0, u32x4 tix1) {
    const char* kc = smem + OFF_KC + ch * 1296;
    u32 d[8];
#pragma unroll
    for (int j = 0; j < 8; ++j) {
        const u32 o = j < 4 ? tix0[j] : tix1[j - 4];
        d[j] = (u32)*(const u16*)(kc + (o & 0xffffu)) | ((u32)*(const u16*)(kc + (o >> 16)) << 16);
    }
    stage_taps(smem, ch, i, (u32x4){d[0], d[1], d[2], d[3]}, (u32x4){d[4], d[5], d[6], d[7]});
}

struct Frags { u32x4 A[8], B[8]; };
template <bool INIT> __device__ __forceinline__ void mma(f32x4& acc, const u32x4& a, const u32x4& b) {
    if constexpr (INIT) asm volatile("v_mfma_f32_16x16x32_bf16 %0, %1, %2, 0" : "=&v"(acc) : "v"(a), "v"(b));
    else asm volatile("v_mfma_f32_16x16x32_bf16 %0, %1, %2, %0" : "+v"(acc) : "v"(a), "v"(b));
}
template <int I> __device__ __forceinline__ void load_one(Frags& f, const char* ap, const char* bp) {
    if constexpr (I == 0) f.A[0] = *(const u32x4*)ap;
    else if constexpr (I <= 8) f.B[I - 1] = *(const u32x4*)(bp + 512 * (8 - I));
    else f.A[I - 8] = *(const u32x4*)(ap + 512 * (I - 8));
}
template <int I> __device__ __forceinline__ void load_all(Frags& f, const char* ap, const char* bp) {
    if constexpr (I < 16) { load_one<I>(f, ap, bp); SB(); load_all<I + 1>(f, ap, bp); }
}
template <int T0, int T1, int T2, int T3> struct TileSet {
    static constexpr int tile(int i) { return i == 0 ? T0 : i == 1 ? T1 : i == 2 ? T2 : T3; }
    static constexpr int walk(int I, bool want_tile) {
        int n = 0;
        for (int a = 0; a < 8; ++a)
            for (int j = 0; j < 4; ++j)
                if (tile(j) >= a) { if (n == I) return want_tile ? tile(j) : a; ++n; }
        return -1;
    }
};
constexpr int tri_a(int I) { int a = 0; while (I >= 8 - a) { I -= 8 - a; ++a; } return a; }
constexpr int tri_b(int I) { int a = 0; while (I >= 8 - a) { I -= 8 - a; ++a; } return I; }
template <int I, bool FIRST> __device__ __forceinline__ void class_step(const Frags& cur, Frags& nxt, f32x4 (&acc)[8], const char* ap, const char* bp) {
    if constexpr (I < 36) {
        constexpr int a = tri_a(I), b = tri_b(I);
        mma<FIRST && a == 0>(acc[a + b], cur.A[a], cur.B[b]);
        if constexpr ((I & 1) == 1 && (I >> 1) < 16) load_one<(I >> 1)>(nxt, ap, bp);
        SB();
        class_step<I + 1, FIRST>(cur, nxt, acc, ap, bp);
    }
}
template <class TS, int I, int TO, int PRE> __device__ __forceinline__ void set_step(const Frags& cur, Frags& nxt, f32x4 (&acc)[8], const char* ap, const char* bp) {
    if constexpr (I < TO) {
        constexpr int t = TS::walk(I, true), a = TS::walk(I, false);
        mma<false>(acc[t], cur.A[a], cur.B[t - a]);
        if constexpr (PRE == 1 && (I * 11) / 18 != ((I + 1) * 11) / 18) load_one<(I * 11) / 18>(nxt, ap, bp);
        if constexpr (PRE == 2 && I < 5) load_one<11 + I>(nxt, ap, bp);
        SB();
        set_step<TS, I + 1, TO, PRE>(cur, nxt, acc, ap, bp);
    }
}
template <class TS, int J> __device__ __forceinline__ void tile_step(const Frags& cur, f32x4 (&acc)[8]) {
    constexpr int t = TS::tile(J);
#pragma unroll
    for (int a = 0; a <= t; ++a) { mma<false>(acc[t], cur.A[a], cur.B[t - a]); SB(); }
}
template <class TS, int J> __device__ __forceinline__ void tile_out(char* Y, f32x4 (&acc)[8], const f32x4 (&o)[4]) {
    constexpr int t = TS::tile(J);
    const f32x4 s = acc[t] + o[J];
    const u32x2 pk = {pk_bf(s[0], s[1]), pk_bf(s[2], s[3])};
    *(u32x2*)(Y + 512 * t) = pk;
    SB();
}
template <class SEND, class KEEP>
__device__ __forceinline__ void conv_half(char* smem, const u32x4 (&A0)[8], const char* ap, const char* bp, int wave, int lane, int ch, u32 seq) {
    f32x4 acc[8];
    Frags f0, f1;
#pragma unroll
    for (int a = 0; a < 8; ++a) f0.A[a] = A0[a];
#pragma unroll
    for (int b = 0; b < 8; ++b) { f0.B[b] = *(const u32x4*)(bp + 512 * (7 - b)); SB(); }
    class_step<0, true>(f0, f1, acc, ap + 64, bp + 64);
    class_step<0, false>(f1, f0, acc, ap + 128, bp + 128);
    set_step<SEND, 0, 18, 1>(f0, f1, acc, ap + 192, bp + 192);
    set_step<SEND, 0, 18, 2>(f1, f1, acc, ap + 192, bp + 192);
    set_step<KEEP, 0, 4, 0>(f0, f1, acc, ap, bp);
    asm volatile("s_nop 3" : "+v"(acc[SEND::tile(0)]), "+v"(acc[SEND::tile(1)]), "+v"(acc[SEND::tile(2)]), "+v"(acc[SEND::tile(3)]));
    f32x4* X = (f32x4*)(smem + OFF_X);
#pragma unroll
    for (int i = 0; i < 4; ++i) X[(wave * 4 + i) * 64 + lane] = acc[SEND::tile(i)];
    volatile LAS u32* xf = (volatile LAS u32*)(LAS char*)(smem + OFF_CNT + 32);
    xf[wave] = seq;
    SB();
    set_step<KEEP, 4, 14, 0>(f0, f1, acc, ap, bp);
    u32 seen = xf[wave ^ 1];
    SB();
    set_step<KEEP, 14, 18, 0>(f0, f1, acc, ap, bp);
    for (u32 spins = 0; seen < seq && spins < 50 * SPIN_CAP; ++spins) seen = xf[wave ^ 1];
    SB();
    f32x4 o[4];
#pragma unroll
    for (int i = 0; i < 4; ++i) o[i] = X[((wave ^ 1) * 4 + i) * 64 + lane];
    SB();
    char* Y = smem + OFF_Y + ch * 4096 + (16 * (lane & 15) + 4 * (lane >> 4)) * 2;
    tile_step<KEEP, 0>(f1, acc);
    tile_step<KEEP, 1>(f1, acc);
    tile_step<KEEP, 2>(f1, acc);
    asm volatile("s_nop 3" : "+v"(acc[KEEP::tile(0)]), "+v"(acc[KEEP::tile(1)]));
    tile_out<KEEP, 0>(Y, acc, o);
    tile_out<KEEP, 1>(Y, acc, o);
    tile_step<KEEP, 3>(f1, acc);
    asm volatile("" : "+v"(acc[KEEP::tile(2)]));
    tile_out<KEEP, 2>(Y, acc, o);
    asm volatile("s_nop 7" : "+v"(acc[KEEP::tile(3)]));
    tile_out<KEEP, 3>(Y, acc, o);
}
struct ConvAddr { const char* ap; const char* bp; };
__device__ __forceinline__ ConvAddr conv_addr(char* smem, int wave, int lane) {
    const int ch = (wave >> 1) & 1, hf = wave & 1;
    const int m = lane & 15, g = lane >> 4;
    ConvAddr c;
    c.ap = smem + OFF_T + ch * T_CH + (m & 7) * T_COPY + 16 * (m >> 3) + 16 * g + 256 * hf;
    c.bp = smem + OFF_U + ch * U_STRIDE + 4064 - 32 * m + 16 * g + 256 * hf - 512 * 7;
    return c;
}
__device__ __forceinline__ void conv_preload(u32x4 (&A0)[8], const ConvAddr& c) {
#pragma unroll
    for (int a = 0; a < 8; ++a) A0[a] = *(const u32x4*)(c.ap + 512 * a);
}
template <bool EARLY>
__device__ __forceinline__ void conv_col(char* smem, const ConvAddr& c, int wave, int lane, u32 seq) {
    u32x4 A0[8];
    if (EARLY) conv_preload(A0, c);
    BAR();
    if (!EARLY) conv_preload(A0, c);
    if ((wave & 1) == 0) conv_half<TileSet<1, 3, 4, 6>, TileSet<0, 2, 5, 7>>(smem, A0, c.ap, c.bp, wave, lane, wave >> 1, seq);
    else conv_half<TileSet<0, 2, 5, 7>, TileSet<1, 3, 4, 6>>(smem, A0, c.ap, c.bp, wave, lane, wave >> 1, seq);
    STAMP(26 + seq);
    BAR();
}
template <class PRE>
__device__ __forceinline__ void conv_row(char* smem, int wave, int lane, rsrc_t rs, int id, int fill0, int st, PRE pre) {
    const u32x4 sent = {SENT, SENT, SENT, SENT};
    const u32 off = WS_H + (fill0 < 0 ? 0 : fill0) * WS_HS + id * 8192 + ((wave - 4) * 64 + lane) * 16;
    const u64 t0 = __builtin_amdgcn_s_memrealtime();
    if (fill0 >= 0) { st_dev(rs, off, sent); st_dev(rs, off + 4096, sent); }
    pre();
    if (fill0 >= 0) {
        wait_until(t0 + FILL_GAP);
        st_dev(rs, off + WS_HS, sent); st_dev(rs, off + WS_HS + 4096, sent);
        WAIT_VM(2);
    }
    STAMP(st);
    BAR();
}

__device__ __forceinline__ void row_dma(char* smem, const char* q, const char* k, const char* v, int id, int wave, int lane, int s) {
    const int slot = wave + 8 * s;
    int row = 8 * id + (slot < 12 ? slot - 4 : slot < 20 ? slot - 12 : slot - 20);
    row = row < 0 ? 0 : row;
    const char* src = slot < 12 ? k : slot < 20 ? v : q;
    lds_dma16(src + row * 1024 + lane * 16, (u32)(size_t)(LAS char*)(smem + OFF_R) + slot * R_ROW);
}

__global__ void __launch_bounds__(512, 2)
h3s4_kernel(const char* __restrict__ q, const char* __restrict__ k, const char* __restrict__ v,
            const u32x2* __restrict__ shiftk, const u32x4* __restrict__ ssmk, const u32* __restrict__ ssmD,
            const u16* __restrict__ Are, const u16* __restrict__ Aim, const u16* __restrict__ Bre, const u16* __restrict__ Bim,
            const u16* __restrict__ Cre, const u16* __restrict__ Cim, const u16* __restrict__ logdt, const u32* __restrict__ Dskip,
            u16* __restrict__ out, char* ws
            ) {
    __shared__ __attribute__((aligned(16))) char smem[LDS_BYTES];
    const int tid = threadIdx.x, lane = tid & 63, wave = __builtin_amdgcn_readfirstlane(tid >> 6);
    const int id = (blockIdx.x & 7) * 32 + (blockIdx.x >> 3);
    const rsrc_t rs = __builtin_amdgcn_make_buffer_rsrc(ws, 0, WS_USED, 0x00020000);
    const u32x4 desc = {(u32)(size_t)ws, (u32)((size_t)ws >> 32) & 0xffffu, WS_USED, 0x00020000u};
    STAMP(0);

    u32 code_pf = 0;
    {
        u64 pc; u32 len;
        asm volatile("s_getpc_b64 %0\nh3s4_text_a:\n\ts_mov_b32 %1, h3s4_text_b - h3s4_text_a" : "=s"(pc), "=s"(len));
        const u32 off = ((blockIdx.x >> 3) * 8 + wave) * 256 + lane * 4;
        if (off + 4 <= len) asm volatile("global_load_dword %0, %1, %2" : "=v"(code_pf) : "v"(off), "s"(pc) : "memory");
    }
    const u32x4 sent = {SENT, SENT, SENT, SENT};
    const u32 dA = ssmD[id], dB = Dskip[id];
    MEMFENCE(); SB();
    st_dev(rs, WS_H + 0 * WS_HS + id * 8192 + tid * 16, sent);
    MEMFENCE(); SB();
    if (wave < 4) {
        row_dma(smem, q, k, v, id, wave, lane, 0); row_dma(smem, q, k, v, id, wave, lane, 1); row_dma(smem, q, k, v, id, wave, lane, 2);
        MEMFENCE(); SB();
        row_dma(smem, q, k, v, id, wave, lane, 3);
        u32x4 tap_own = ld_b128(ssmk, (2 * id) * 4096 + tid * 16), tap_prev = ld_b128(ssmk, (2 * id) * 4096 + (tid ? tid - 1 : 0) * 16);
        const int pd = 2 * id + (tid >> 7), po = (pd * NS + (tid & 127)) * 2;
        u32 r_dt = ld_u16(logdt, pd * 2), r_are = ld_u16(Are, po), r_aim = ld_u16(Aim, po), r_bre = ld_u16(Bre, po);
        u32 r_bim = ld_u16(Bim, po), r_cre = ld_u16(Cre, po), r_cim = ld_u16(Cim, po);
        u32x4 tix0 = ld_b128(tap_index.off, tid * 16), tix1 = ld_b128(tap_index.off, tid * 16 + 16);
        MEMFENCE(); SB();
        st_dev(rs, WS_H + 1 * WS_HS + id * 8192 + tid * 16, sent);
        MEMFENCE(); SB();
        const u32 epoch = launch_tag(ws);
        asm volatile("s_waitcnt vmcnt(15)\n\ts_barrier" ::: "memory");
        if (tid < 32) __hip_atomic_store((GAS u32*)(ws + WS_FLAGS) + tid * 256 + id, epoch, RLX_AGENT);
        STAMP(19);
        if (tid < 60) *(u32x4*)(smem + OFF_U + (tid / 30) * U_STRIDE + 4096 + (tid % 30) * 16) = (u32x4){0, 0, 0, 0};
        else if (tid >= 64 && tid < 80) *(u32x4*)(smem + OFF_T + (tid - 64) * T_COPY) = (u32x4){0, 0, 0, 0};
        asm volatile("s_waitcnt vmcnt(13)" ::: "memory");
        BAR();
        STAMP(1);
        asm volatile("s_cmp_eq_u32 %11, 0\n\ts_cbranch_scc1 1f\n\ts_waitcnt vmcnt(1)\n\ts_branch 2f\n1:\n\ts_waitcnt vmcnt(0)\n2:"
                     : "+v"(tap_own), "+v"(tap_prev), "+v"(r_dt), "+v"(r_are), "+v"(r_aim), "+v"(r_bre), "+v"(r_bim), "+v"(r_cre),
                       "+v"(r_cim), "+v"(tix0), "+v"(tix1)
                     : "s"((0x1u >> wave) & 1u) : "memory", "scc");
        cnt_arrive(smem, 6, lane);

        {
            const float dt = rbf(__builtin_amdgcn_exp2f(bf_lo(r_dt) * 1.4426950408889634f));
            const float are = dt * bf_lo(r_are), aim = dt * bf_lo(r_aim);
            const float br = dt * bf_lo(r_bre), bi = dt * bf_lo(r_bim);
            const float cr = bf_lo(r_cre), ci = bf_lo(r_cim);
            const float wr = cr * br - ci * bi, wi = cr * bi + ci * br;
            const float ar = fabsf(wr), ai = fabsf(wi), mx = fmaxf(ar, ai), mn = fminf(ar, ai);
            const float ratio = mx > 0.f ? mn * __builtin_amdgcn_rcpf(mx) : 0.f, r2 = ratio * ratio;
            float at = 0.0028662257f;
            at = fmaf(at, r2, -0.0161657367f); at = fmaf(at, r2, 0.0429096138f); at = fmaf(at, r2, -0.0752896400f);
            at = fmaf(at, r2, 0.1065626393f); at = fmaf(at, r2, -0.1420889944f); at = fmaf(at, r2, 0.1999355085f);
            at = fmaf(at, r2, -0.3333314528f); at = fmaf(at, r2, 1.0f);
            float ph = at * ratio * 0.15915494309189535f;
            ph = ai > ar ? 0.25f - ph : ph;
            ph = wr < 0.f ? 0.5f - ph : ph;
            ph = wi < 0.f ? -ph : ph;
            float* p = (float*)(smem + OFF_P) + (tid >> 1) * 8 + (tid & 1);
            p[0] = are * 1.4426950408889634f;
            p[2] = aim * 0.15915494309189535f;
            p[4] = __builtin_amdgcn_logf(mx) + 0.5f * __builtin_amdgcn_logf(r2 + 1.0f);
            p[6] = ph;
        }
        if (tid == 0) {
            tap_prev = (u32x4){0, 0, 0, 0};
            tap_own[0] = (tap_own[0] & 0xffff0000u) | (pk_bf(bf_lo(tap_own[0]) + bf_lo(dA), 0.f) & 0xffffu);
        }
        stage_taps(smem, 0, tid, tap_prev, tap_own);
        cnt_arrive(smem, 0, lane);
        STAMP(2);

        const ConvAddr ca = conv_addr(smem, wave, lane);
        asm volatile("" :: "v"(code_pf));
        cnt_wait(smem, 0, 8);
        STAMP(4);
#pragma nounroll
        for (u32 seq = 1; seq <= 3; ++seq) {
            if (seq == 2) {
                {
                    KState ks;
                    kgen_init(ks);
                    kgen_steps<4 - R_STEPS, true>(kgen_params(smem, wave, 0), lane, ks);
                    kgen_save(smem, wave, lane, ks);
                }
                STAMP(7);
                cnt_arrive(smem, 1, lane);
                cnt_wait(smem, 1, 8);
                kgen_reduce(smem, wave, lane, dB);
                cnt_arrive(smem, 2, lane);
                cnt_wait(smem, 2, 4);
                STAMP(23);
                s4_taps(smem, 0, tid, tix0, tix1);
                cnt_arrive(smem, 3, lane);
                cnt_wait(smem, 3, 8);
                STAMP(25);
            }
            conv_col<true>(smem, ca, wave, lane, seq);
            STAMP(seq == 1 ? 6 : seq == 2 ? 11 : 15);
        }
    } else {
        const int r = tid - 256, w = wave - 4;
        u32x2 skA = ld_b64(shiftk, r * 8), skB = ld_b64(shiftk, (r + 256) * 8);
        row_dma(smem, q, k, v, id, wave, lane, 0); row_dma(smem, q, k, v, id, wave, lane, 1);
        MEMFENCE(); SB();
        row_dma(smem, q, k, v, id, wave, lane, 2);
        u32x4 tap_own = ld_b128(ssmk, (2 * id + 1) * 4096 + r * 16), tap_prev = ld_b128(ssmk, (2 * id + 1) * 4096 + (r ? r - 1 : 0) * 16);
        u32x4 tix0 = ld_b128(tap_index.off, r * 16), tix1 = ld_b128(tap_index.off, r * 16 + 16);
        MEMFENCE(); SB();
        st_dev(rs, WS_H + 1 * WS_HS + id * 8192 + tid * 16, sent);
        MEMFENCE(); SB();
        const u32 epoch = launch_tag(ws);
        asm volatile("s_waitcnt vmcnt(9)\n\ts_barrier" ::: "memory");
        __builtin_amdgcn_s_setprio(2);
        ((u32*)(smem + OFF_RED))[r] = SENT;
        if (r < 16) ((u32*)(smem + OFF_CNT))[r] = 0u;
        asm volatile("s_waitcnt vmcnt(6)" : "+v"(skA), "+v"(skB) :: "memory");
        BAR();
        STAMP(1);

        const u32 own_a = id * 8192 + r * 16, own_b = own_a + 4096;
        const u32 col_a = (r >> 1) * 8192 + (2 * id + (r & 1)) * 16, col_b = col_a + 128 * 8192;
        const u32 row_a = (r >> 1) * 8192 + id * 32 + (r & 1) * 16, row_b = row_a + 128 * 8192;
        char* const u_dst = smem + OFF_U + (r & 1) * U_STRIDE + 4080 - (r >> 1) * 16;
        const char* const y_src = smem + OFF_Y + (r & 1) * 4096 + (r >> 1) * 16;
        float* red = (float*)(smem + OFF_RED);

        Rows x, u0;
        u64 t_s;
        const u32 flag_a = WS_FLAGS + (blockIdx.x & 31) * 1024 + (r >> 1) * 4;
        u32 fl_a, fl_b;
        {
            const u32 rp = (u32)(size_t)(LAS char*)(smem + OFF_R + ((lane & 15) >> 2) * R_ROW + (64 * w + 16 * (lane >> 4) + 4 * (lane & 3)) * 2);
            u32x2 kk[2][3], vv[2][2], qq[2][2];
            static_assert(4 * R_ROW == 4352, "offsets below");
            asm volatile("ds_read_b64_tr_b16 %0, %10\n\tds_read_b64_tr_b16 %1, %10 offset:4352\n\tds_read_b64_tr_b16 %2, %10 offset:8704\n\t"
                         "ds_read_b64_tr_b16 %3, %10 offset:13056\n\tds_read_b64_tr_b16 %4, %10 offset:17408\n\t"
                         "ds_read_b64_tr_b16 %5, %10 offset:512\n\tds_read_b64_tr_b16 %6, %10 offset:4864\n\tds_read_b64_tr_b16 %7, %10 offset:9216\n\t"
                         "ds_read_b64_tr_b16 %8, %10 offset:13568\n\tds_read_b64_tr_b16 %9, %10 offset:17920\n\ts_waitcnt lgkmcnt(0)"
                         : "=&v"(kk[0][0]), "=&v"(kk[0][1]), "=&v"(kk[0][2]), "=&v"(vv[0][0]), "=&v"(vv[0][1]),
                           "=&v"(kk[1][0]), "=&v"(kk[1][1]), "=&v"(kk[1][2]), "=&v"(vv[1][0]), "=&v"(vv[1][1]) : "v"(rp) : "memory");
            const u32 first = id ? 0xffffffffu : 0u;
            u32x4 o[2];
#pragma unroll
            for (int c = 0; c < 2; ++c) {
                const u32x2 sk = c ? skB : skA;
                const u32 kp = kk[c][0][0] & first, kq = kk[c][0][1] & first;
                const float kf[12] = {0.f, bf_hi(kp), bf_lo(kq), bf_hi(kq), bf_lo(kk[c][1][0]), bf_hi(kk[c][1][0]),
                                      bf_lo(kk[c][1][1]), bf_hi(kk[c][1][1]), bf_lo(kk[c][2][0]), bf_hi(kk[c][2][0]),
                                      bf_lo(kk[c][2][1]), bf_hi(kk[c][2][1])};
                float vf[8];
                unpack8((u32x4){vv[c][0][0], vv[c][0][1], vv[c][1][0], vv[c][1][1]}, vf);
                const float w0 = bf_lo(sk[0]), w1 = bf_hi(sk[0]), w2 = bf_lo(sk[1]), w3 = bf_hi(sk[1]);
                float kv[8];
#pragma unroll
                for (int j = 0; j < 8; ++j) kv[j] = fmaf(kf[j + 4], w0, fmaf(kf[j + 3], w1, fmaf(kf[j + 2], w2, kf[j + 1] * w3))) * vf[j];
                o[c] = pack8(kv);
            }
            publish(rs, WS_H + 0 * WS_HS + own_a, o[0]);
            publish(rs, WS_H + 0 * WS_HS + own_b, o[1]);
            t_s = __builtin_amdgcn_s_memrealtime();
            asm volatile("s_waitcnt vmcnt(2)" : "+v"(tap_own), "+v"(tap_prev), "+v"(tix0), "+v"(tix1) :: "memory");
            fl_a = ld_flag(ws, flag_a); fl_b = ld_flag(ws, flag_a + 512);
            cnt_arrive(smem, 6, lane);
            cnt_wait(smem, 6, 8);
            asm volatile("ds_read_b64_tr_b16 %0, %4 offset:21760\n\tds_read_b64_tr_b16 %1, %4 offset:26112\n\t"
                         "ds_read_b64_tr_b16 %2, %4 offset:22272\n\tds_read_b64_tr_b16 %3, %4 offset:26624\n\ts_waitcnt lgkmcnt(0)"
                         : "=&v"(qq[0][0]), "=&v"(qq[0][1]), "=&v"(qq[1][0]), "=&v"(qq[1][1]) : "v"(rp) : "memory");
            unpack_rows((u32x4){qq[0][0][0], qq[0][0][1], qq[0][1][0], qq[0][1][1]}, x.a);
            unpack_rows((u32x4){qq[1][0][0], qq[1][0][1], qq[1][1][0], qq[1][1][1]}, x.b);
        }
        STAMP(2);
        u32 touch = 0;
        if (wave == 6) {
            if (lane < 4) touch = __builtin_amdgcn_raw_buffer_load_b32(rs, WS_H + (2 + lane) * WS_HS + id * 8192, 0, 16);
            else if (lane == 4) touch = ((const u32*)out)[id * 2048];
        }
        if (r == 0) {
            tap_prev = (u32x4){0, 0, 0, 0};
            tap_own[0] = (tap_own[0] & 0xffff0000u) | (pk_bf(bf_lo(tap_own[0]) + bf_hi(dA), 0.f) & 0xffffu);
        }
        stage_taps(smem, 1, r, tap_prev, tap_own);
        cnt_arrive(smem, 0, lane);
        if (wave == 7 && lane < 32) __hip_atomic_store((GAS u32*)(ws + WS_FLAGS + 32768) + lane * 256 + id, epoch, RLX_AGENT);
        STAMP(3);

        Piece2 pc, pr;
        Rows yA, u;
        auto stage_input = [&](u32 h) {
            poll2_finish(pc, desc, WS_H + h * WS_HS + col_a, WS_H + h * WS_HS + col_b);
            *(u32x4*)u_dst = reverse8(pc.a);
            *(u32x4*)(u_dst - 2048) = reverse8(pc.b);
        };
        auto send_result = [&](u32 h) {
            publish(rs, WS_H + h * WS_HS + own_a, *(const u32x4*)y_src);
            publish(rs, WS_H + h * WS_HS + own_b, *(const u32x4*)(y_src + 2048));
            t_s = __builtin_amdgcn_s_memrealtime();
        };
        auto fetch_rows = [&](u32 h) {
            wait_until(t_s + POLL_DELAY);
            pr = poll2_issue(rs, WS_H + h * WS_HS + row_a, WS_H + h * WS_HS + row_b);
            poll2_finish(pr, desc, WS_H + h * WS_HS + row_a, WS_H + h * WS_HS + row_b);
        };

        wait_until(t_s + POLL_DELAY);
        {
            asm volatile("" :: "v"(fl_a), "v"(fl_b));
            const bool ok = fl_a == epoch && fl_b == epoch;
            pc = poll2_issue(rs, WS_H + col_a, WS_H + col_b);
            poll2_finish_flagged(pc, desc, WS_H + col_a, WS_H + col_b, flag_a, flag_a + 512, ok, epoch);
            *(u32x4*)u_dst = reverse8(pc.a);
            *(u32x4*)(u_dst - 2048) = reverse8(pc.b);
        }
        STAMP(21);
        asm volatile("" :: "v"(touch));
        asm volatile("" :: "v"(code_pf));
        BAR();
        STAMP(5);
        if (blockIdx.x == 0 && r == 0) __hip_atomic_store((GAS u32*)(ws + WS_EPOCH), epoch, RLX_AGENT);
        conv_row(smem, wave, lane, rs, id, 2, 27, [&] {
            __builtin_amdgcn_s_setprio(0);
            KState ks;
            kgen_init(ks);
            kgen_steps<R_STEPS, false>(kgen_params(smem, wave, 4 - R_STEPS), lane, ks);
            kgen_save(smem, wave, lane, ks);
            cnt_arrive(smem, 1, lane);
            __builtin_amdgcn_s_setprio(2);
        });
        send_result(1);
        STAMP(6);

#pragma nounroll
        for (u32 it = 0; it < 2; ++it) {
            MEMFENCE();
            const u32 fb_a = ld_flag(ws, flag_a + 32768), fb_b = ld_flag(ws, flag_a + 32768 + 512);
            MEMFENCE();
            layer_norm(x, u0, red + 128 * it, lane, w);
            if (it == 0) {
                wait_until(t_s + POLL_DELAY);
                asm volatile("" :: "v"(fb_a), "v"(fb_b));
                const bool ok = fb_a == epoch && fb_b == epoch;
                pr = poll2_issue(rs, WS_H + WS_HS + row_a, WS_H + WS_HS + row_b);
                poll2_finish_flagged(pr, desc, WS_H + WS_HS + row_a, WS_H + WS_HS + row_b, flag_a + 32768, flag_a + 32768 + 512, ok, epoch);
                STAMP(8);
                unpack_rows(pr.a, yA.a);
                unpack_rows(pr.b, yA.b);
            }
#pragma unroll
            for (int i = 0; i < 4; ++i) { x.a[i] += u0.a[i] * yA.a[i]; x.b[i] += u0.b[i] * yA.b[i]; }
            round_rows(x.a);
            round_rows(x.b);
            layer_norm(x, u, red + 128 * it + 64, lane, w);
            publish(rs, WS_H + (2 + 2 * it) * WS_HS + own_a, pack_rows(u.a));
            publish(rs, WS_H + (2 + 2 * it) * WS_HS + own_b, pack_rows(u.b));
            t_s = __builtin_amdgcn_s_memrealtime();
            STAMP(it ? 13 : 9);

            wait_until(t_s + POLL_DELAY);
            pc = poll2_issue(rs, WS_H + (2 + 2 * it) * WS_HS + col_a, WS_H + (2 + 2 * it) * WS_HS + col_b);
            if (it == 0) {
                cnt_wait(smem, 2, 4);
                s4_taps(smem, 1, r, tix0, tix1);
                cnt_arrive(smem, 3, lane);
            }
            stage_input(2 + 2 * it);
            BAR();
            STAMP(it ? 14 : 10);
            conv_row(smem, wave, lane, rs, id, it ? -1 : 4, 28, [] {});
            send_result(3 + 2 * it);
            STAMP(it ? 15 : 11);

            fetch_rows(3 + 2 * it);
            STAMP(it ? 16 : 12);
            Rows y;
            unpack_rows(pr.a, y.a);
            unpack_rows(pr.b, y.b);
#pragma unroll
            for (int i = 0; i < 4; ++i) { x.a[i] += y.a[i]; x.b[i] += y.b[i]; }
            pr.a = round_rows(x.a);
            pr.b = round_rows(x.b);
        }
        {
            u16* dst = out + (8 * id) * DM + r;
#pragma unroll
            for (int j = 0; j < 4; ++j) {
                dst[(2 * j) * DM] = (u16)pr.a[j];
                dst[(2 * j) * DM + 256] = (u16)pr.b[j];
                dst[(2 * j + 1) * DM] = (u16)(pr.a[j] >> 16);
                dst[(2 * j + 1) * DM + 256] = (u16)(pr.b[j] >> 16);
            }
        }
        STAMP(17);
    }
    asm volatile("h3s4_text_b:");
}

extern "C" void kernel_launch(void* const* d_in, const int* in_sizes, int n_in,
                              void* d_out, int out_size, void* d_ws, size_t ws_size,
                              hipStream_t stream) {
    h3s4_kernel<<<256, 512, 0, stream>>>(
        (const char*)d_in[0], (const char*)d_in[1], (const char*)d_in[2], (const u32x2*)d_in[3], (const u32x4*)d_in[4],
        (const u32*)d_in[5], (const u16*)d_in[7], (const u16*)d_in[8], (const u16*)d_in[9], (const u16*)d_in[10],
        (const u16*)d_in[11], (const u16*)d_in[12], (const u16*)d_in[13], (const u32*)d_in[14], (u16*)d_out, (char*)d_ws
        );
}
```
